```python
import torch

def _softmax(x, axis=-1):
    return torch.softmax(x, dim=axis)
def _gelu(x):
    return torch.nn.functional.gelu(x, approximate="tanh")
def _silu(x):
    return torch.nn.functional.silu(x)
def _sigmoid(x):
    return torch.sigmoid(x)
def _layernorm(x, axis=-1, eps=1e-5):
    m = x.mean(dim=axis, keepdim=True)
    v = x.var(dim=axis, keepdim=True, unbiased=False)
    return (x - m) / torch.sqrt(v + eps)
def _scatter_add(tgt, idx, src, axis=0):
    return tgt.clone().index_add_(axis, idx, src)
def _segment_sum(src, seg, n):
    out = torch.zeros((n,) + src.shape[1:], dtype=src.dtype)
    return out.index_add_(0, seg, src)
def _pad(x, pw):
    flat = tuple(v for pair in reversed(pw) for v in pair)
    return torch.nn.functional.pad(x, flat)

kernel_name = 'concat_heads__mha_block_cached__gqa_block_cached__near_ridge'

def setup_inputs():

    _w = (lambda a: (lambda t: t.to(torch.bfloat16) if t.is_floating_point() else t)(torch.as_tensor(a)))
    def _A_setup():
        B, Sq, D = 16, 96, 448
        H, Sk, Dh = 16, 96, 256
        return {
            "x": _w((lambda *s: torch.randn(*s, dtype=torch.bfloat16) * 0.8)(B, Sq, D)),
            "k_cache": _w((lambda *s: torch.randn(*s, dtype=torch.bfloat16) * 0.8)(B, H, Sk, Dh)),
            "v_cache": _w((lambda *s: torch.randn(*s, dtype=torch.bfloat16) * 0.05)(B, H, Sk, Dh)),
            "wq": _w((lambda *s: torch.randn(*s, dtype=torch.bfloat16) * 0.8)(D, H * Dh) * (D ** -0.5)),
            "wo": _w((lambda *s: torch.randn(*s, dtype=torch.bfloat16) * 0.05)(H * Dh, D) * ((H * Dh) ** -0.5)),
        }
    _A_in = _A_setup()

    def _B_setup():
        B, Sq, D = 16, 96, 448
        Hq, Hkv, Sk, Dh = 16, 16, 96, 256
        assert Hq % Hkv == 0, (Hq, Hkv)
        return {
            "x": _w((lambda *s: torch.randn(*s, dtype=torch.bfloat16) * 0.8)(B, Sq, D)),
            "k_cache": _w((lambda *s: torch.randn(*s, dtype=torch.bfloat16) * 0.8)(B, Hkv, Sk, Dh)),
            "v_cache": _w((lambda *s: torch.randn(*s, dtype=torch.bfloat16) * 0.05)(B, Hkv, Sk, Dh)),
            "wq": _w((lambda *s: torch.randn(*s, dtype=torch.bfloat16) * 0.8)(D, Hq * Dh) * (D ** -0.5)),
            "wo": _w((lambda *s: torch.randn(*s, dtype=torch.bfloat16) * 0.05)(Hq * Dh, D) * ((Hq * Dh) ** -0.5)),
        }
    _B_in = _B_setup()

    return {
        "A_x": _A_in["x"],
        "A_k_cache": _A_in["k_cache"],
        "A_v_cache": _A_in["v_cache"],
        "A_wq": _A_in["wq"],
        "A_wo": _A_in["wo"],
        "B_x": _B_in["x"],
        "B_k_cache": _B_in["k_cache"],
        "B_v_cache": _B_in["v_cache"],
        "B_wq": _B_in["wq"],
        "B_wo": _B_in["wo"],
    }

def reference(A_x, A_k_cache, A_v_cache, A_wq, A_wo, B_x, B_k_cache, B_v_cache, B_wq, B_wo):
    def _A_ref(x, k_cache, v_cache, wq, wo):
        B, Sq, D = x.shape
        H, Dh = 16, 256
        q = (lambda x, *d: x.permute(*d))((x @ wq).reshape(B, Sq, H, Dh), 0, 2, 1, 3)
        scores = torch.einsum("bhqd,bhkd->bhqk", q, k_cache) * (Dh ** -0.5)
        p = _softmax(scores, axis=-1)
        o = torch.einsum("bhqk,bhkd->bhqd", p, v_cache)
        return (lambda x, *d: x.permute(*d))(o, 0, 2, 1, 3).reshape(B, Sq, H * Dh) @ wo

    def _B_ref(x, k_cache, v_cache, wq, wo):
        B, Sq, D = x.shape
        Hq, Dh = 16, 256
        Hkv = k_cache.shape[1]
        rep = Hq // Hkv
        q = (lambda x, *d: x.permute(*d))((x @ wq).reshape(B, Sq, Hq, Dh), 0, 2, 1, 3)
        k = (lambda x, n, axis: torch.repeat_interleave(x, n, dim=axis))(k_cache, rep, axis=1)
        v = (lambda x, n, axis: torch.repeat_interleave(x, n, dim=axis))(v_cache, rep, axis=1)
        scores = torch.einsum("bhqd,bhkd->bhqk", q, k) * (Dh ** -0.5)
        p = _softmax(scores, axis=-1)
        o = torch.einsum("bhqk,bhkd->bhqd", p, v)
        return (lambda x, *d: x.permute(*d))(o, 0, 2, 1, 3).reshape(B, Sq, Hq * Dh) @ wo

    a = _A_ref(A_x, A_k_cache, A_v_cache, A_wq, A_wo)
    b = _B_ref(B_x, B_k_cache, B_v_cache, B_wq, B_wo)
    return torch.concatenate([a, b], axis=-1)
```

```cpp
#include <hip/hip_runtime.h>
#include <hip/hip_bf16.h>
typedef unsigned short u16;
__device__ __forceinline__ float b2f(u16 v){ return __uint_as_float(((unsigned)v)<<16); }
__device__ __forceinline__ u16 f2b(float f){ __hip_bfloat16 h = __float2bfloat16(f); return *reinterpret_cast<u16*>(&h); }
constexpr int B=16,S=96,D=448,H=16,DH=256,HD=H*DH;
__global__ void k_q(const u16* x, const u16* wq, u16* q){
  int n = blockIdx.x*256+threadIdx.x; int row = blockIdx.y;
  float acc=0.f;
  for(int k=0;k<D;++k) acc += b2f(x[row*D+k])*b2f(wq[k*HD+n]);
  q[row*HD+n]=f2b(acc);
}
__global__ void k_attn(const u16* q, const u16* kc, const u16* vc, u16* o){
  int row = blockIdx.x, h = blockIdx.y, b = row/S, t = threadIdx.x;
  __shared__ float p[S]; __shared__ float qs[DH];
  qs[t]=b2f(q[row*HD+h*DH+t]);
  __syncthreads();
  const u16* kb = kc + (size_t)(b*H+h)*S*DH; const u16* vb = vc + (size_t)(b*H+h)*S*DH;
  if(t<S){ float a=0.f; for(int d=0;d<DH;++d) a+=qs[d]*b2f(kb[t*DH+d]); p[t]=a*0.0625f; }
  __syncthreads();
  float m=-1e30f; for(int j=0;j<S;++j) m=fmaxf(m,p[j]);
  float s=0.f; for(int j=0;j<S;++j) s+=__expf(p[j]-m);
  float a=0.f; for(int j=0;j<S;++j) a+=b2f(f2b(__expf(p[j]-m)/s))*b2f(vb[j*DH+t]);
  o[row*HD+h*DH+t]=f2b(a);
}
__global__ void k_o(const u16* o, const u16* wo, u16* out, int coff){
  int n = threadIdx.x; int row = blockIdx.x;
  float acc=0.f;
  for(int k=0;k<HD;++k) acc += b2f(o[row*HD+k])*b2f(wo[k*D+n]);
  out[row*(2*D)+coff+n]=f2b(acc);
}
extern "C" void kernel_launch(void* const* d_in, const int* in_sizes, int n_in, void* d_out, int out_size, void* d_ws, size_t ws_size, hipStream_t stream){
  u16* ws=(u16*)d_ws;
  for(int br=0;br<2;++br){
    const u16* x=(const u16*)d_in[br*5+0]; const u16* kc=(const u16*)d_in[br*5+1]; const u16* vc=(const u16*)d_in[br*5+2];
    const u16* wq=(const u16*)d_in[br*5+3]; const u16* wo=(const u16*)d_in[br*5+4];
    u16* q=ws+(size_t)br*2*B*S*HD; u16* o=q+(size_t)B*S*HD;
    k_q<<<dim3(HD/256,B*S),256,0,stream>>>(x,wq,q);
    k_attn<<<dim3(B*S,H),256,0,stream>>>(q,kc,vc,o);
    k_o<<<B*S,D,0,stream>>>(o,wo,(u16*)d_out,br*D);
  }
}
```

```cpp
#include <hip/hip_runtime.h>

__global__ __launch_bounds__(256, 1) __attribute__((aligned(4096))) void fused_kernel(const void* xa, const void* ka, const void* va, const void* wqa, const void* woa,
                                                      const void* xb, const void* kb, const void* vb, const void* wqb, const void* wob,
                                                      void* out, void* ws) {
  __shared__ __attribute__((aligned(16))) char smem[163840];
  asm volatile(
      "s_and_b32 s81, %1, 1\n"
      "s_mul_i32 s88, s81, 40\n"
      "s_load_dwordx8 s[40:47], %0, s88\n"
      "s_add_u32 s88, s88, 32\n"
      "s_load_dwordx2 s[48:49], %0, s88\n"
      "s_load_dwordx4 s[52:55], %0, 0x50\n"
      "v_lshrrev_b32 v2, 6, %2\n"
      "v_and_b32 v1, 63, %2\n"
      "v_readfirstlane_b32 s78, v2\n"
      "s_lshr_b32 s88, %1, 3\n"
      "s_and_b32 s79, s88, 15\n"
      "s_lshr_b32 s88, s88, 4\n"
      "s_and_b32 s89, %1, 6\n"
      "s_add_u32 s88, s88, s89\n"
      "s_lshl_b32 s80, s88, 1\n"
      "v_lshrrev_b32 v2, 3, v1\n"
      "v_and_b32 v3, 7, v1\n"
      "v_lshrrev_b32 v4, 1, v2\n"
      "s_mul_i32 s90, s78, 0x5400\n"
      "v_mul_u32_u24 v5, 0x380, v2\n"
      "v_add_u32 v5, s90, v5\n"
      "s_and_b32 s87, s78, 1\n"
      "s_lshl_b32 s92, s87, 2\n"
      "v_or_b32 v6, s92, v4\n"
      "v_xor_b32 v6, v3, v6\n"
      "v_lshl_add_u32 v196, v6, 4, v5\n"
      "v_add_u32 v198, 0x3000, v196\n"
      "v_xor_b32 v6, 64, v196\n"
      "v_add_u32 v197, 0x1800, v6\n"
      "s_lshr_b32 s95, s78, 1\n"
      "s_lshl_b32 s90, s87, 18\n"
      "s_lshl_b32 s92, s95, 9\n"
      "s_add_u32 s90, s90, s92\n"
      "v_lshlrev_b32 v5, 13, v2\n"
      "v_add_u32 v5, s90, v5\n"
      "v_and_b32 v6, 1, v4\n"
      "v_lshlrev_b32 v6, 1, v6\n"
      "v_xor_b32 v6, v3, v6\n"
      "v_lshl_add_u32 v192, v6, 4, v5\n"
      "v_add_u32 v194, 0x1f800, v192\n"
      "v_xor_b32 v6, 64, v192\n"
      "v_add_u32 v193, 0xfc00, v6\n"
      "v_add_u32 v195, 0x2f400, v6\n"
      "s_lshr_b32 s90, s79, 2\n"
      "s_mul_i32 s90, s90, 0x100\n"
      "s_lshl_b32 s56, s80, 6\n"
      "s_add_u32 s56, s56, s90\n"
      "s_cmp_ge_u32 s56, 0x380\n"
      "s_cselect_b32 s92, 0x380, 0\n"
      "s_sub_u32 s56, s56, s92\n"
      "s_lshl_b32 s63, s56, 12\n"
      "s_and_b32 s90, s79, 3\n"
      "s_lshl_b32 s70, s90, 7\n"
      "s_mul_i32 s84, s78, 0xc00\n"
      "s_lshl_b32 s85, s78, 12\n"
      "s_mul_i32 s90, s79, 0x15000\n"
      "s_waitcnt lgkmcnt(0)\n"
      "s_add_u32 s8, s40, s90\n"
      "s_addc_u32 s9, s41, 0\n"
      "s_and_b32 s9, s9, 0xffff\n"
      "s_mov_b32 s10, 0x15000\n"
      "s_mov_b32 s11, 0x20000\n"
      "s_lshl_b32 s90, s80, 9\n"
      "s_sub_u32 s92, 0x380000, s90\n"
      "s_add_u32 s20, s46, s90\n"
      "s_addc_u32 s21, s47, 0\n"
      "s_and_b32 s21, s21, 0xffff\n"
      "s_mov_b32 s22, s92\n"
      "s_mov_b32 s23, 0x20000\n"
      "s_add_u32 m0, s84, 0x24000\n"
      "s_nop 0\n"
      "buffer_load_dwordx4 v196, s[8:11], s56 offen lds\n"
      "buffer_load_dwordx4 v197, s[8:11], s56 offen offset:1024 lds\n"
      "buffer_load_dwordx4 v198, s[8:11], s56 offen offset:2048 lds\n"
      "s_add_u32 s88, s63, s70\n"
      "s_add_u32 m0, s85, 0x18000\n"
      "s_nop 0\n"
      "buffer_load_dwordx4 v192, s[20:23], s88 offen lds\n"
      "buffer_load_dwordx4 v193, s[20:23], s88 offen offset:1024 lds\n"
      "buffer_load_dwordx4 v194, s[20:23], s88 offen offset:2048 lds\n"
      "buffer_load_dwordx4 v195, s[20:23], s88 offen offset:3072 lds\n"
      "s_add_u32 s57, s56, 0x80\n"
      "s_cmp_eq_u32 s57, 0x380\n"
      "s_cselect_b32 s57, 0, s57\n"
      "s_lshl_b32 s64, s57, 12\n"
      "s_and_b32 s90, s79, 7\n"
      "s_lshl_b32 s90, s90, 2\n"
      "s_add_u32 s90, s90, s78\n"
      "s_lshl_b32 s90, s90, 14\n"
      "s_lshr_b32 s92, s79, 3\n"
      "s_lshl_b32 s92, s92, 9\n"
      "s_add_u32 s90, s90, s92\n"
      "v_lshrrev_b32 v5, 1, v1\n"
      "v_lshlrev_b32 v5, 19, v5\n"
      "v_and_b32 v6, 1, v1\n"
      "v_lshl_add_u32 v5, v6, 13, v5\n"
      "v_add_u32 v199, s90, v5\n"
      "s_getpc_b64 s[96:97]\n"
      "1:\n"
      "s_and_b32 s97, s97, 0xffff\n"
      "s_mov_b32 s98, 9f-1b\n"
      "s_mov_b32 s99, 0x20000\n"
      "s_lshl_b32 s90, s79, 2\n"
      "s_add_u32 s90, s90, s78\n"
      "s_lshl_b32 s90, s90, 7\n"
      "v_lshlrev_b32 v208, 13, v1\n"
      "buffer_load_dword v208, v208, s[96:99], s90 offen\n"
      "s_add_u32 s58, s57, 0x80\n"
      "s_cmp_eq_u32 s58, 0x380\n"
      "s_cselect_b32 s58, 0, s58\n"
      "s_lshl_b32 s65, s58, 12\n"
      "s_add_u32 s59, s58, 0x80\n"
      "s_cmp_eq_u32 s59, 0x380\n"
      "s_cselect_b32 s59, 0, s59\n"
      "s_lshl_b32 s66, s59, 12\n"
      "s_add_u32 s60, s59, 0x80\n"
      "s_cmp_eq_u32 s60, 0x380\n"
      "s_cselect_b32 s60, 0, s60\n"
      "s_lshl_b32 s67, s60, 12\n"
      "s_add_u32 s61, s60, 0x80\n"
      "s_cmp_eq_u32 s61, 0x380\n"
      "s_cselect_b32 s61, 0, s61\n"
      "s_lshl_b32 s68, s61, 12\n"
      "s_add_u32 s62, s61, 0x80\n"
      "s_cmp_eq_u32 s62, 0x380\n"
      "s_cselect_b32 s62, 0, s62\n"
      "s_lshl_b32 s69, s62, 12\n"
      "s_lshl_b32 s86, s78, 11\n"
      "s_lshl_b32 s90, s79, 4\n"
      "s_add_u32 s90, s90, s80\n"
      "s_mul_i32 s90, s90, 0xc000\n"
      "s_add_u32 s12, s42, s90\n"
      "s_addc_u32 s13, s43, 0\n"
      "s_and_b32 s13, s13, 0xffff\n"
      "s_mov_b32 s14, 0x18000\n"
      "s_mov_b32 s15, 0x20000\n"
      "s_add_u32 s16, s44, s90\n"
      "s_addc_u32 s17, s45, 0\n"
      "s_and_b32 s17, s17, 0xffff\n"
      "s_mov_b32 s18, 0x18000\n"
      "s_mov_b32 s19, 0x20000\n"
      "s_mul_i32 s90, s95, 0xc000\n"
      "s_lshl_b32 s92, s87, 13\n"
      "s_add_u32 s90, s90, s92\n"
      "v_lshlrev_b32 v5, 9, v2\n"
      "v_add_u32 v5, s90, v5\n"
      "v_xor_b32 v6, v3, v4\n"
      "v_lshl_add_u32 v204, v6, 4, v5\n"
      "v_xor_b32 v6, 64, v204\n"
      "v_add_u32 v205, 0xc00, v6\n"
      "v_lshlrev_b32 v7, 1, v4\n"
      "v_xor_b32 v7, v3, v7\n"
      "v_lshl_add_u32 v216, v7, 4, v5\n"
      "v_add_u32 v217, 0xc00, v216\n"
      "s_lshl_b32 s90, s95, 8\n"
      "s_lshl_b32 s92, s87, 5\n"
      "s_add_u32 s90, s90, s92\n"
      "s_mul_i32 s90, s90, 0x380\n"
      "v_mul_u32_u24 v5, 0x380, v2\n"
      "v_add_u32 v5, s90, v5\n"
      "v_lshl_add_u32 v10, v7, 4, v5\n"
      "v_add_u32 v11, 0x1800, v10\n"
      "v_add_u32 v12, 0x3000, v10\n"
      "v_add_u32 v13, 0x4800, v10\n"
      "s_mul_i32 s90, s80, 0x38000\n"
      "s_add_u32 s24, s48, s90\n"
      "s_addc_u32 s25, s49, 0\n"
      "s_and_b32 s25, s25, 0xffff\n"
      "s_mov_b32 s26, 0x70000\n"
      "s_mov_b32 s27, 0x20000\n"
      "s_add_u32 s71, s70, 0x80\n"
      "s_and_b32 s71, s71, 0x1ff\n"
      "s_add_u32 s72, s71, 0x80\n"
      "s_and_b32 s72, s72, 0x1ff\n"
      "s_add_u32 s73, s72, 0x80\n"
      "s_and_b32 s73, s73, 0x1ff\n"
      "s_mul_i32 s74, s70, 0x1c0\n"
      "s_mul_i32 s75, s71, 0x1c0\n"
      "s_mul_i32 s76, s72, 0x1c0\n"
      "s_mul_i32 s77, s73, 0x1c0\n"
      "s_lshr_b32 s90, s80, 1\n"
      "s_sub_u32 s90, s90, s79\n"
      "s_and_b32 s50, s90, 7\n"
      "s_add_u32 s90, s50, 1\n"
      "s_cmp_ge_u32 s90, 7\n"
      "s_cselect_b32 s92, 7, 0\n"
      "s_sub_u32 s90, s90, s92\n"
      "s_lshl_b32 s82, s90, 7\n"
      "s_mov_b32 s83, s82\n"
      "s_mov_b32 s48, s82\n"
      "s_lshl_b32 s90, s81, 4\n"
      "s_add_u32 s90, s90, s79\n"
      "s_mul_i32 s90, s90, 0xa9000\n"
      "s_add_u32 s36, s54, s90\n"
      "s_addc_u32 s37, s55, 0\n"
      "s_and_b32 s37, s37, 0xffff\n"
      "s_mov_b32 s38, 0xa9000\n"
      "s_mov_b32 s39, 0x20000\n"
      "s_mul_i32 s90, s79, 0x2a000\n"
      "s_mul_i32 s92, s81, 0x380\n"
      "s_add_u32 s90, s90, s92\n"
      "s_sub_u32 s92, 0x2a000, s92\n"
      "s_add_u32 s28, s52, s90\n"
      "s_addc_u32 s29, s53, 0\n"
      "s_and_b32 s29, s29, 0xffff\n"
      "s_mov_b32 s30, s92\n"
      "s_mov_b32 s31, 0x20000\n"
      "v_lshrrev_b32 v2, 4, v1\n"
      "v_and_b32 v3, 15, v1\n"
      "v_lshrrev_b32 v4, 2, v3\n"
      "v_and_b32 v5, 3, v1\n"
      "v_lshrrev_b32 v6, 1, v4\n"
      "v_and_b32 v7, 1, v2\n"
      "v_lshl_or_b32 v6, v7, 1, v6\n"
      "v_lshlrev_b32 v7, 3, v5\n"
      "v_lshl_add_u32 v7, v6, 5, v7\n"
      "v_lshl_add_u32 v7, v4, 7, v7\n"
      "s_lshl_b32 s90, s95, 13\n"
      "s_add_u32 s90, s90, 0xc000\n"
      "v_lshl_add_u32 v8, v2, 10, v7\n"
      "v_add_u32 v14, s90, v8\n"
      "v_xor_b32 v15, 32, v14\n"
      "v_xor_b32 v16, 64, v14\n"
      "v_xor_b32 v17, 0x60, v14\n"
      "v_add_u32 v200, 0x10000, v14\n"
      "v_add_u32 v201, 0x10000, v15\n"
      "v_add_u32 v202, 0x10000, v16\n"
      "v_add_u32 v203, 0x10000, v17\n"
      "v_lshl_add_u32 v8, v2, 9, v7\n"
      "s_lshl_b32 s90, s95, 12\n"
      "v_add_u32 v20, s90, v8\n"
      "v_xor_b32 v21, 32, v20\n"
      "v_xor_b32 v22, 64, v20\n"
      "v_xor_b32 v23, 0x60, v20\n"
      "v_add_u32 v40, 0x18000, v20\n"
      "v_add_u32 v41, 0x18000, v21\n"
      "v_add_u32 v42, 0x18000, v22\n"
      "v_add_u32 v43, 0x18000, v23\n"
      "s_mul_i32 s92, s95, 0x2040\n"
      "v_xor_b32 v18, s92, v8\n"
      "v_xor_b32 v19, 32, v18\n"
      "v_lshrrev_b32 v7, 1, v3\n"
      "v_lshrrev_b32 v8, 1, v2\n"
      "v_xor_b32 v8, v8, v7\n"
      "v_lshlrev_b32 v8, 4, v8\n"
      "v_and_b32 v9, 1, v2\n"
      "v_lshl_add_u32 v8, v9, 3, v8\n"
      "v_lshl_add_u32 v8, v3, 7, v8\n"
      "v_add_u32 v24, s90, v8\n"
      "v_xor_b32 v25, 32, v24\n"
      "v_xor_b32 v26, 64, v24\n"
      "v_xor_b32 v27, 0x60, v24\n"
      "v_xor_b32 v8, v2, v7\n"
      "v_lshlrev_b32 v8, 4, v8\n"
      "v_lshl_add_u32 v8, v3, 7, v8\n"
      "s_mul_i32 s90, s87, 0x1800\n"
      "v_add_u32 v28, s90, v8\n"
      "v_xor_b32 v29, 64, v28\n"
      "v_lshlrev_b32 v8, 4, v1\n"
      "v_add_u32 v30, s86, v8\n"
      "v_xor_b32 v31, 0x1000, v30\n"
      "v_add_u32 v44, 0x18000, v30\n"
      "v_add_u32 v45, 0x18000, v31\n"
      "v_add_u32 v33, s84, v8\n"
      "s_mul_i32 s90, s50, 0x3000\n"
      "v_add_u32 v32, s90, v33\n"
      "s_lshl_b32 s90, s78, 10\n"
      "s_add_u32 s90, s90, 0xa8000\n"
      "s_lshl_b32 s92, s50, 3\n"
      "s_add_u32 s92, s92, s90\n"
      "v_mov_b32 v4, 0x40000000\n"
      "v_mov_b32 v5, s92\n"
      "v_cmp_eq_u32 vcc, 0, v1\n"
      "s_nop 1\n"
      "v_cndmask_b32 v36, v4, v5, vcc\n"
      "s_lshl_b32 s92, s50, 7\n"
      "s_add_u32 s92, s92, s90\n"
      "v_lshl_add_u32 v5, v1, 3, s92\n"
      "v_cmp_gt_u32 vcc, 8, v1\n"
      "s_nop 1\n"
      "v_cndmask_b32 v37, v4, v5, vcc\n"
      "s_add_u32 s90, s90, 0x380\n"
      "v_mov_b32 v34, s90\n"
      "s_mul_i32 s51, s50, 0x18000\n"
      "s_cmp_eq_u32 s50, 7\n"
      "s_cselect_b32 s51, 0x40000000, s51\n"
      "s_sub_u32 s90, s50, 1\n"
      "s_cmp_lt_u32 s90, 4\n"
      "s_cselect_b32 s40, 0x80, 0\n"
      "s_add_u32 s41, s51, 0x15000\n"
      "s_cmp_eq_u32 s40, 0\n"
      "s_cselect_b32 s41, 0x40000000, s41\n"
      "v_and_b32 v4, 1, v2\n"
      "v_lshlrev_b32 v4, 1, v4\n"
      "v_lshrrev_b32 v5, 1, v2\n"
      "v_or_b32 v4, v4, v5\n"
      "s_lshl_b32 s92, s95, 2\n"
      "v_or_b32 v4, s92, v4\n"
      "v_and_b32 v5, 7, v1\n"
      "v_xor_b32 v4, v4, v5\n"
      "v_lshlrev_b32 v4, 4, v4\n"
      "v_lshl_add_u32 v4, v3, 7, v4\n"
      "s_mul_i32 s90, s87, 0x1800\n"
      "s_add_u32 s90, s90, 0x20000\n"
      "v_add_u32 v46, s90, v4\n"
      "v_lshrrev_b32 v4, 3, v1\n"
      "v_xor_b32 v6, v4, v5\n"
      "v_lshlrev_b32 v6, 4, v6\n"
      "v_lshl_add_u32 v6, v4, 7, v6\n"
      "s_add_u32 s90, s84, 0x20000\n"
      "v_add_u32 v47, s90, v6\n"
      "s_mul_i32 s90, s78, 0xa800\n"
      "s_lshl_b32 s92, s50, 7\n"
      "s_add_u32 s90, s90, s92\n"
      "v_mul_u32_u24 v4, 0x700, v4\n"
      "v_lshl_add_u32 v4, v5, 4, v4\n"
      "v_add_u32 v38, s90, v4\n"
      "s_mov_b32 s96, 0x3db8aa3b\n"
      "s_mov_b32 s97, 0x3db8aa3b\n"
      "s_mov_b32 s99, s50\n"
      "s_add_u32 m0, s84, 0x6000\n"
      "s_nop 0\n"
      "buffer_load_dwordx4 v196, s[8:11], s57 offen lds\n"
      "buffer_load_dwordx4 v197, s[8:11], s57 offen offset:1024 lds\n"
      "buffer_load_dwordx4 v198, s[8:11], s57 offen offset:2048 lds\n"
      "s_add_u32 s88, s64, s70\n"
      "s_add_u32 m0, s85, 0x1c000\n"
      "s_nop 0\n"
      "buffer_load_dwordx4 v192, s[20:23], s88 offen lds\n"
      "buffer_load_dwordx4 v193, s[20:23], s88 offen offset:1024 lds\n"
      "buffer_load_dwordx4 v194, s[20:23], s88 offen offset:2048 lds\n"
      "buffer_load_dwordx4 v195, s[20:23], s88 offen offset:3072 lds\n"
      "s_and_b32 s90, s79, 2\n"
      "s_cmp_eq_u32 s90, 0\n"
      "s_cbranch_scc0 3f\n"
      "19:\n"
      "s_waitcnt vmcnt(8)\n"
      "s_barrier\n"
      "ds_read_b64_tr_b16 a[168:169], v14 offset:49152\n"
      "ds_read_b64_tr_b16 a[170:171], v14 offset:49664\n"
      "ds_read_b64_tr_b16 a[172:173], v14 offset:53248\n"
      "ds_read_b64_tr_b16 a[174:175], v14 offset:53760\n"
      "v_add_u32 v28, 0x24000, v28\n"
      "ds_read_b128 a[0:3], v28\n"
      "v_add_u32 v29, 0x24000, v29\n"
      "ds_read_b128 a[12:15], v29\n"
      "ds_read_b128 a[4:7], v28 offset:2048\n"
      "ds_read_b128 a[16:19], v29 offset:2048\n"
      "ds_read_b128 a[8:11], v28 offset:4096\n"
      "ds_read_b128 a[20:23], v29 offset:4096\n"
      "ds_read_b64_tr_b16 a[176:177], v15 offset:49152\n"
      "ds_read_b64_tr_b16 a[178:179], v15 offset:49664\n"
      "ds_read_b64_tr_b16 a[180:181], v15 offset:53248\n"
      "ds_read_b64_tr_b16 a[182:183], v15 offset:53760\n"
      "ds_read_b64_tr_b16 a[184:185], v16 offset:49152\n"
      "ds_read_b64_tr_b16 a[186:187], v16 offset:49664\n"
      "ds_read_b64_tr_b16 a[188:189], v16 offset:53248\n"
      "ds_read_b64_tr_b16 a[190:191], v16 offset:53760\n"
      "ds_read_b64_tr_b16 a[192:193], v17 offset:49152\n"
      "ds_read_b64_tr_b16 a[194:195], v17 offset:49664\n"
      "ds_read_b64_tr_b16 a[196:197], v17 offset:53248\n"
      "ds_read_b64_tr_b16 a[198:199], v17 offset:53760\n"
      "s_waitcnt lgkmcnt(15)\n"
      "v_mfma_f32_16x16x32_bf16 v[120:123], a[168:171], a[0:3], 0\n"
      "s_add_u32 m0, s84, 0x9000\n"
      "v_mfma_f32_16x16x32_bf16 v[120:123], a[172:175], a[12:15], v[120:123]\n"
      "buffer_load_dwordx4 v196, s[8:11], s58 offen lds\n"
      "v_mfma_f32_16x16x32_bf16 v[124:127], a[168:171], a[4:7], 0\n"
      "buffer_load_dwordx4 v197, s[8:11], s58 offen offset:1024 lds\n"
      "s_waitcnt lgkmcnt(14)\n"
      "v_mfma_f32_16x16x32_bf16 v[124:127], a[172:175], a[16:19], v[124:127]\n"
      "buffer_load_dwordx4 v198, s[8:11], s58 offen offset:2048 lds\n"
      "s_add_u32 s88, s65, s70\n"
      "s_add_u32 m0, s85, 0x20000\n"
      "s_waitcnt lgkmcnt(13)\n"
      "v_mfma_f32_16x16x32_bf16 v[128:131], a[168:171], a[8:11], 0\n"
      "buffer_load_dwordx4 v192, s[20:23], s88 offen lds\n"
      "s_waitcnt lgkmcnt(12)\n"
      "v_mfma_f32_16x16x32_bf16 v[128:131], a[172:175], a[20:23], v[128:131]\n"
      "buffer_load_dwordx4 v193, s[20:23], s88 offen offset:1024 lds\n"
      "s_waitcnt lgkmcnt(10)\n"
      "v_mfma_f32_16x16x32_bf16 v[132:135], a[176:179], a[0:3], 0\n"
      "buffer_load_dwordx4 v194, s[20:23], s88 offen offset:2048 lds\n"
      "s_waitcnt lgkmcnt(8)\n"
      "v_mfma_f32_16x16x32_bf16 v[132:135], a[180:183], a[12:15], v[132:135]\n"
      "buffer_load_dwordx4 v195, s[20:23], s88 offen offset:3072 lds\n"
      "s_add_u32 m0, s84, 0xc000\n"
      "v_mfma_f32_16x16x32_bf16 v[136:139], a[176:179], a[4:7], 0\n"
      "buffer_load_dwordx4 v196, s[8:11], s59 offen lds\n"
      "v_mfma_f32_16x16x32_bf16 v[136:139], a[180:183], a[16:19], v[136:139]\n"
      "buffer_load_dwordx4 v197, s[8:11], s59 offen offset:1024 lds\n"
      "v_mfma_f32_16x16x32_bf16 v[140:143], a[176:179], a[8:11], 0\n"
      "buffer_load_dwordx4 v198, s[8:11], s59 offen offset:2048 lds\n"
      "v_mfma_f32_16x16x32_bf16 v[140:143], a[180:183], a[20:23], v[140:143]\n"
      "s_waitcnt vmcnt(10) lgkmcnt(0)\n"
      "s_barrier\n"
      "v_mfma_f32_16x16x32_bf16 v[144:147], a[184:187], a[0:3], 0\n"
      "ds_read_b64_tr_b16 a[168:169], v200\n"
      "v_add_u32 v28, 0xfffe2000, v28\n"
      "ds_read_b128 a[24:27], v28\n"
      "s_add_u32 s88, s66, s70\n"
      "s_add_u32 m0, s85, 0x24000\n"
      "v_mfma_f32_16x16x32_bf16 v[144:147], a[188:191], a[12:15], v[144:147]\n"
      "ds_read_b64_tr_b16 a[170:171], v200 offset:512\n"
      "v_add_u32 v29, 0xfffe2000, v29\n"
      "ds_read_b128 a[36:39], v29\n"
      "buffer_load_dwordx4 v192, s[20:23], s88 offen lds\n"
      "v_mfma_f32_16x16x32_bf16 v[148:151], a[184:187], a[4:7], 0\n"
      "ds_read_b64_tr_b16 a[172:173], v200 offset:4096\n"
      "ds_read_b128 a[28:31], v28 offset:2048\n"
      "v_mfma_f32_16x16x32_bf16 v[148:151], a[188:191], a[16:19], v[148:151]\n"
      "ds_read_b64_tr_b16 a[174:175], v200 offset:4608\n"
      "ds_read_b128 a[40:43], v29 offset:2048\n"
      "buffer_load_dwordx4 v193, s[20:23], s88 offen offset:1024 lds\n"
      "v_mfma_f32_16x16x32_bf16 v[152:155], a[184:187], a[8:11], 0\n"
      "ds_read_b64_tr_b16 a[176:177], v201\n"
      "ds_read_b128 a[32:35], v28 offset:4096\n"
      "v_mfma_f32_16x16x32_bf16 v[152:155], a[188:191], a[20:23], v[152:155]\n"
      "ds_read_b64_tr_b16 a[178:179], v201 offset:512\n"
      "ds_read_b128 a[44:47], v29 offset:4096\n"
      "buffer_load_dwordx4 v194, s[20:23], s88 offen offset:2048 lds\n"
      "v_mfma_f32_16x16x32_bf16 v[156:159], a[192:195], a[0:3], 0\n"
      "ds_read_b64_tr_b16 a[180:181], v201 offset:4096\n"
      "v_mfma_f32_16x16x32_bf16 v[156:159], a[196:199], a[12:15], v[156:159]\n"
      "ds_read_b64_tr_b16 a[182:183], v201 offset:4608\n"
      "buffer_load_dwordx4 v195, s[20:23], s88 offen offset:3072 lds\n"
      "v_mfma_f32_16x16x32_bf16 v[160:163], a[192:195], a[4:7], 0\n"
      "ds_read_b64_tr_b16 a[184:185], v202\n"
      "s_add_u32 s88, s67, s70\n"
      "s_add_u32 m0, s85, 0x18000\n"
      "v_mfma_f32_16x16x32_bf16 v[160:163], a[196:199], a[16:19], v[160:163]\n"
      "ds_read_b64_tr_b16 a[186:187], v202 offset:512\n"
      "buffer_load_dwordx4 v192, s[20:23], s88 offen lds\n"
      "v_mfma_f32_16x16x32_bf16 v[164:167], a[192:195], a[8:11], 0\n"
      "ds_read_b64_tr_b16 a[188:189], v202 offset:4096\n"
      "v_mfma_f32_16x16x32_bf16 v[164:167], a[196:199], a[20:23], v[164:167]\n"
      "ds_read_b64_tr_b16 a[190:191], v202 offset:4608\n"
      "buffer_load_dwordx4 v193, s[20:23], s88 offen offset:1024 lds\n"
      "s_waitcnt lgkmcnt(15)\n"
      "v_mfma_f32_16x16x32_bf16 v[120:123], a[168:171], a[24:27], v[120:123]\n"
      "ds_read_b64_tr_b16 a[192:193], v203\n"
      "s_waitcnt lgkmcnt(12)\n"
      "v_mfma_f32_16x16x32_bf16 v[120:123], a[172:175], a[36:39], v[120:123]\n"
      "ds_read_b64_tr_b16 a[194:195], v203 offset:512\n"
      "buffer_load_dwordx4 v194, s[20:23], s88 offen offset:2048 lds\n"
      "v_mfma_f32_16x16x32_bf16 v[124:127], a[168:171], a[28:31], v[124:127]\n"
      "ds_read_b64_tr_b16 a[196:197], v203 offset:4096\n"
      "s_waitcnt lgkmcnt(13)\n"
      "v_mfma_f32_16x16x32_bf16 v[124:127], a[172:175], a[40:43], v[124:127]\n"
      "ds_read_b64_tr_b16 a[198:199], v203 offset:4608\n"
      "buffer_load_dwordx4 v195, s[20:23], s88 offen offset:3072 lds\n"
      "s_waitcnt lgkmcnt(12)\n"
      "v_mfma_f32_16x16x32_bf16 v[128:131], a[168:171], a[32:35], v[128:131]\n"
      "s_add_u32 m0, s84, 0xf000\n"
      "s_waitcnt lgkmcnt(10)\n"
      "v_mfma_f32_16x16x32_bf16 v[128:131], a[172:175], a[44:47], v[128:131]\n"
      "buffer_load_dwordx4 v196, s[8:11], s60 offen lds\n"
      "v_mfma_f32_16x16x32_bf16 v[132:135], a[176:179], a[24:27], v[132:135]\n"
      "buffer_load_dwordx4 v197, s[8:11], s60 offen offset:1024 lds\n"
      "s_waitcnt lgkmcnt(8)\n"
      "v_mfma_f32_16x16x32_bf16 v[132:135], a[180:183], a[36:39], v[132:135]\n"
      "buffer_load_dwordx4 v198, s[8:11], s60 offen offset:2048 lds\n"
      "s_add_u32 m0, s84, 0x12000\n"
      "v_mfma_f32_16x16x32_bf16 v[136:139], a[176:179], a[28:31], v[136:139]\n"
      "buffer_load_dwordx4 v196, s[8:11], s61 offen lds\n"
      "v_mfma_f32_16x16x32_bf16 v[136:139], a[180:183], a[40:43], v[136:139]\n"
      "buffer_load_dwordx4 v197, s[8:11], s61 offen offset:1024 lds\n"
      "v_mfma_f32_16x16x32_bf16 v[140:143], a[176:179], a[32:35], v[140:143]\n"
      "buffer_load_dwordx4 v198, s[8:11], s61 offen offset:2048 lds\n"
      "v_mfma_f32_16x16x32_bf16 v[140:143], a[180:183], a[44:47], v[140:143]\n"
      "s_waitcnt vmcnt(17) lgkmcnt(0)\n"
      "s_barrier\n"
      "v_mfma_f32_16x16x32_bf16 v[144:147], a[184:187], a[24:27], v[144:147]\n"
      "ds_read_b64_tr_b16 a[168:169], v200 offset:16384\n"
      "ds_read_b128 a[48:51], v28 offset:12288\n"
      "s_add_u32 s88, s68, s70\n"
      "s_add_u32 m0, s85, 0x1c000\n"
      "v_mfma_f32_16x16x32_bf16 v[144:147], a[188:191], a[36:39], v[144:147]\n"
      "ds_read_b64_tr_b16 a[170:171], v200 offset:16896\n"
      "ds_read_b128 a[60:63], v29 offset:12288\n"
      "buffer_load_dwordx4 v192, s[20:23], s88 offen lds\n"
      "v_mfma_f32_16x16x32_bf16 v[148:151], a[184:187], a[28:31], v[148:151]\n"
      "ds_read_b64_tr_b16 a[172:173], v200 offset:20480\n"
      "ds_read_b128 a[52:55], v28 offset:14336\n"
      "v_mfma_f32_16x16x32_bf16 v[148:151], a[188:191], a[40:43], v[148:151]\n"
      "ds_read_b64_tr_b16 a[174:175], v200 offset:20992\n"
      "ds_read_b128 a[64:67], v29 offset:14336\n"
      "buffer_load_dwordx4 v193, s[20:23], s88 offen offset:1024 lds\n"
      "v_mfma_f32_16x16x32_bf16 v[152:155], a[184:187], a[32:35], v[152:155]\n"
      "ds_read_b64_tr_b16 a[176:177], v201 offset:16384\n"
      "ds_read_b128 a[56:59], v28 offset:16384\n"
      "v_mfma_f32_16x16x32_bf16 v[152:155], a[188:191], a[44:47], v[152:155]\n"
      "ds_read_b64_tr_b16 a[178:179], v201 offset:16896\n"
      "ds_read_b128 a[68:71], v29 offset:16384\n"
      "buffer_load_dwordx4 v194, s[20:23], s88 offen offset:2048 lds\n"
      "v_mfma_f32_16x16x32_bf16 v[156:159], a[192:195], a[24:27], v[156:159]\n"
      "ds_read_b64_tr_b16 a[180:181], v201 offset:20480\n"
      "v_mfma_f32_16x16x32_bf16 v[156:159], a[196:199], a[36:39], v[156:159]\n"
      "ds_read_b64_tr_b16 a[182:183], v201 offset:20992\n"
      "buffer_load_dwordx4 v195, s[20:23], s88 offen offset:3072 lds\n"
      "v_mfma_f32_16x16x32_bf16 v[160:163], a[192:195], a[28:31], v[160:163]\n"
      "ds_read_b64_tr_b16 a[184:185], v202 offset:16384\n"
      "s_add_u32 m0, s84, 0x15000\n"
      "v_mfma_f32_16x16x32_bf16 v[160:163], a[196:199], a[40:43], v[160:163]\n"
      "ds_read_b64_tr_b16 a[186:187], v202 offset:16896\n"
      "buffer_load_dwordx4 v196, s[8:11], s62 offen lds\n"
      "v_mfma_f32_16x16x32_bf16 v[164:167], a[192:195], a[32:35], v[164:167]\n"
      "ds_read_b64_tr_b16 a[188:189], v202 offset:20480\n"
      "v_mfma_f32_16x16x32_bf16 v[164:167], a[196:199], a[44:47], v[164:167]\n"
      "ds_read_b64_tr_b16 a[190:191], v202 offset:20992\n"
      "buffer_load_dwordx4 v197, s[8:11], s62 offen offset:1024 lds\n"
      "s_waitcnt lgkmcnt(15)\n"
      "v_mfma_f32_16x16x32_bf16 v[120:123], a[168:171], a[48:51], v[120:123]\n"
      "ds_read_b64_tr_b16 a[192:193], v203 offset:16384\n"
      "s_waitcnt lgkmcnt(12)\n"
      "v_mfma_f32_16x16x32_bf16 v[120:123], a[172:175], a[60:63], v[120:123]\n"
      "ds_read_b64_tr_b16 a[194:195], v203 offset:16896\n"
      "buffer_load_dwordx4 v198, s[8:11], s62 offen offset:2048 lds\n"
      "v_mfma_f32_16x16x32_bf16 v[124:127], a[168:171], a[52:55], v[124:127]\n"
      "ds_read_b64_tr_b16 a[196:197], v203 offset:20480\n"
      "s_add_u32 s89, s70, 0\n"
      "s_add_u32 m0, s86, 0\n"
      "s_waitcnt lgkmcnt(13)\n"
      "v_mfma_f32_16x16x32_bf16 v[124:127], a[172:175], a[64:67], v[124:127]\n"
      "ds_read_b64_tr_b16 a[198:199], v203 offset:20992\n"
      "buffer_load_dwordx4 v204, s[12:15], s89 offen nt lds\n"
      "s_waitcnt lgkmcnt(12)\n"
      "v_mfma_f32_16x16x32_bf16 v[128:131], a[168:171], a[56:59], v[128:131]\n"
      "s_waitcnt lgkmcnt(10)\n"
      "v_mfma_f32_16x16x32_bf16 v[128:131], a[172:175], a[68:71], v[128:131]\n"
      "buffer_load_dwordx4 v205, s[12:15], s89 offen offset:1024 nt lds\n"
      "v_mfma_f32_16x16x32_bf16 v[132:135], a[176:179], a[48:51], v[132:135]\n"
      "s_add_u32 s89, s70, 0x4000\n"
      "s_add_u32 m0, s86, 0x2000\n"
      "s_waitcnt lgkmcnt(8)\n"
      "v_mfma_f32_16x16x32_bf16 v[132:135], a[180:183], a[60:63], v[132:135]\n"
      "buffer_load_dwordx4 v204, s[12:15], s89 offen nt lds\n"
      "v_mfma_f32_16x16x32_bf16 v[136:139], a[176:179], a[52:55], v[136:139]\n"
      "buffer_load_dwordx4 v205, s[12:15], s89 offen offset:1024 nt lds\n"
      "s_add_u32 s89, s70, 0x8000\n"
      "s_add_u32 m0, s86, 0x4000\n"
      "v_mfma_f32_16x16x32_bf16 v[136:139], a[180:183], a[64:67], v[136:139]\n"
      "buffer_load_dwordx4 v204, s[12:15], s89 offen nt lds\n"
      "v_mfma_f32_16x16x32_bf16 v[140:143], a[176:179], a[56:59], v[140:143]\n"
      "buffer_load_dwordx4 v205, s[12:15], s89 offen offset:1024 nt lds\n"
      "v_mfma_f32_16x16x32_bf16 v[140:143], a[180:183], a[68:71], v[140:143]\n"
      "s_waitcnt vmcnt(23) lgkmcnt(0)\n"
      "s_barrier\n"
      "v_mfma_f32_16x16x32_bf16 v[144:147], a[184:187], a[48:51], v[144:147]\n"
      "ds_read_b64_tr_b16 a[168:169], v200 offset:32768\n"
      "ds_read_b128 a[72:75], v28 offset:24576\n"
      "v_mfma_f32_16x16x32_bf16 v[144:147], a[188:191], a[60:63], v[144:147]\n"
      "ds_read_b64_tr_b16 a[170:171], v200 offset:33280\n"
      "ds_read_b128 a[84:87], v29 offset:24576\n"
      "v_mfma_f32_16x16x32_bf16 v[148:151], a[184:187], a[52:55], v[148:151]\n"
      "ds_read_b64_tr_b16 a[172:173], v200 offset:36864\n"
      "ds_read_b128 a[76:79], v28 offset:26624\n"
      "v_mfma_f32_16x16x32_bf16 v[148:151], a[188:191], a[64:67], v[148:151]\n"
      "ds_read_b64_tr_b16 a[174:175], v200 offset:37376\n"
      "ds_read_b128 a[88:91], v29 offset:26624\n"
      "v_mfma_f32_16x16x32_bf16 v[152:155], a[184:187], a[56:59], v[152:155]\n"
      "ds_read_b64_tr_b16 a[176:177], v201 offset:32768\n"
      "ds_read_b128 a[80:83], v28 offset:28672\n"
      "v_mfma_f32_16x16x32_bf16 v[152:155], a[188:191], a[68:71], v[152:155]\n"
      "ds_read_b64_tr_b16 a[178:179], v201 offset:33280\n"
      "ds_read_b128 a[92:95], v29 offset:28672\n"
      "v_mfma_f32_16x16x32_bf16 v[156:159], a[192:195], a[48:51], v[156:159]\n"
      "ds_read_b64_tr_b16 a[180:181], v201 offset:36864\n"
      "v_mfma_f32_16x16x32_bf16 v[156:159], a[196:199], a[60:63], v[156:159]\n"
      "ds_read_b64_tr_b16 a[182:183], v201 offset:37376\n"
      "v_mfma_f32_16x16x32_bf16 v[160:163], a[192:195], a[52:55], v[160:163]\n"
      "ds_read_b64_tr_b16 a[184:185], v202 offset:32768\n"
      "v_mfma_f32_16x16x32_bf16 v[160:163], a[196:199], a[64:67], v[160:163]\n"
      "ds_read_b64_tr_b16 a[186:187], v202 offset:33280\n"
      "v_mfma_f32_16x16x32_bf16 v[164:167], a[192:195], a[56:59], v[164:167]\n"
      "ds_read_b64_tr_b16 a[188:189], v202 offset:36864\n"
      "v_mfma_f32_16x16x32_bf16 v[164:167], a[196:199], a[68:71], v[164:167]\n"
      "ds_read_b64_tr_b16 a[190:191], v202 offset:37376\n"
      "s_waitcnt lgkmcnt(15)\n"
      "v_mfma_f32_16x16x32_bf16 v[120:123], a[168:171], a[72:75], v[120:123]\n"
      "ds_read_b64_tr_b16 a[192:193], v203 offset:32768\n"
      "s_waitcnt lgkmcnt(12)\n"
      "v_mfma_f32_16x16x32_bf16 v[120:123], a[172:175], a[84:87], v[120:123]\n"
      "ds_read_b64_tr_b16 a[194:195], v203 offset:33280\n"
      "v_mfma_f32_16x16x32_bf16 v[124:127], a[168:171], a[76:79], v[124:127]\n"
      "ds_read_b64_tr_b16 a[196:197], v203 offset:36864\n"
      "s_waitcnt lgkmcnt(13)\n"
      "v_mfma_f32_16x16x32_bf16 v[124:127], a[172:175], a[88:91], v[124:127]\n"
      "ds_read_b64_tr_b16 a[198:199], v203 offset:37376\n"
      "s_add_u32 s88, s69, s70\n"
      "s_add_u32 m0, s85, 0x20000\n"
      "s_waitcnt lgkmcnt(12)\n"
      "v_mfma_f32_16x16x32_bf16 v[128:131], a[168:171], a[80:83], v[128:131]\n"
      "buffer_load_dwordx4 v192, s[20:23], s88 offen lds\n"
      "s_waitcnt lgkmcnt(10)\n"
      "v_mfma_f32_16x16x32_bf16 v[128:131], a[172:175], a[92:95], v[128:131]\n"
      "v_mfma_f32_16x16x32_bf16 v[132:135], a[176:179], a[72:75], v[132:135]\n"
      "buffer_load_dwordx4 v193, s[20:23], s88 offen offset:1024 lds\n"
      "s_waitcnt lgkmcnt(8)\n"
      "v_mfma_f32_16x16x32_bf16 v[132:135], a[180:183], a[84:87], v[132:135]\n"
      "v_mfma_f32_16x16x32_bf16 v[136:139], a[176:179], a[76:79], v[136:139]\n"
      "buffer_load_dwordx4 v194, s[20:23], s88 offen offset:2048 lds\n"
      "v_mfma_f32_16x16x32_bf16 v[136:139], a[180:183], a[88:91], v[136:139]\n"
      "v_mfma_f32_16x16x32_bf16 v[140:143], a[176:179], a[80:83], v[140:143]\n"
      "buffer_load_dwordx4 v195, s[20:23], s88 offen offset:3072 lds\n"
      "v_mfma_f32_16x16x32_bf16 v[140:143], a[180:183], a[92:95], v[140:143]\n"
      "s_waitcnt vmcnt(20) lgkmcnt(0)\n"
      "s_barrier\n"
      "v_mfma_f32_16x16x32_bf16 v[144:147], a[184:187], a[72:75], v[144:147]\n"
      "ds_read_b64_tr_b16 a[168:169], v14 offset:49152\n"
      "ds_read_b128 a[96:99], v28 offset:36864\n"
      "v_mfma_f32_16x16x32_bf16 v[144:147], a[188:191], a[84:87], v[144:147]\n"
      "ds_read_b64_tr_b16 a[170:171], v14 offset:49664\n"
      "ds_read_b128 a[108:111], v29 offset:36864\n"
      "v_mfma_f32_16x16x32_bf16 v[148:151], a[184:187], a[76:79], v[148:151]\n"
      "ds_read_b64_tr_b16 a[172:173], v14 offset:53248\n"
      "ds_read_b128 a[100:103], v28 offset:38912\n"
      "v_mfma_f32_16x16x32_bf16 v[148:151], a[188:191], a[88:91], v[148:151]\n"
      "ds_read_b64_tr_b16 a[174:175], v14 offset:53760\n"
      "ds_read_b128 a[112:115], v29 offset:38912\n"
      "v_mfma_f32_16x16x32_bf16 v[152:155], a[184:187], a[80:83], v[152:155]\n"
      "ds_read_b64_tr_b16 a[176:177], v15 offset:49152\n"
      "ds_read_b128 a[104:107], v28 offset:40960\n"
      "v_mfma_f32_16x16x32_bf16 v[152:155], a[188:191], a[92:95], v[152:155]\n"
      "ds_read_b64_tr_b16 a[178:179], v15 offset:49664\n"
      "ds_read_b128 a[116:119], v29 offset:40960\n"
      "v_mfma_f32_16x16x32_bf16 v[156:159], a[192:195], a[72:75], v[156:159]\n"
      "ds_read_b64_tr_b16 a[180:181], v15 offset:53248\n"
      "v_mfma_f32_16x16x32_bf16 v[156:159], a[196:199], a[84:87], v[156:159]\n"
      "ds_read_b64_tr_b16 a[182:183], v15 offset:53760\n"
      "v_mfma_f32_16x16x32_bf16 v[160:163], a[192:195], a[76:79], v[160:163]\n"
      "ds_read_b64_tr_b16 a[184:185], v16 offset:49152\n"
      "v_mfma_f32_16x16x32_bf16 v[160:163], a[196:199], a[88:91], v[160:163]\n"
      "ds_read_b64_tr_b16 a[186:187], v16 offset:49664\n"
      "v_mfma_f32_16x16x32_bf16 v[164:167], a[192:195], a[80:83], v[164:167]\n"
      "ds_read_b64_tr_b16 a[188:189], v16 offset:53248\n"
      "v_mfma_f32_16x16x32_bf16 v[164:167], a[196:199], a[92:95], v[164:167]\n"
      "ds_read_b64_tr_b16 a[190:191], v16 offset:53760\n"
      "s_waitcnt lgkmcnt(15)\n"
      "v_mfma_f32_16x16x32_bf16 v[120:123], a[168:171], a[96:99], v[120:123]\n"
      "ds_read_b64_tr_b16 a[192:193], v17 offset:49152\n"
      "s_waitcnt lgkmcnt(12)\n"
      "v_mfma_f32_16x16x32_bf16 v[120:123], a[172:175], a[108:111], v[120:123]\n"
      "ds_read_b64_tr_b16 a[194:195], v17 offset:49664\n"
      "v_mfma_f32_16x16x32_bf16 v[124:127], a[168:171], a[100:103], v[124:127]\n"
      "ds_read_b64_tr_b16 a[196:197], v17 offset:53248\n"
      "s_waitcnt lgkmcnt(13)\n"
      "v_mfma_f32_16x16x32_bf16 v[124:127], a[172:175], a[112:115], v[124:127]\n"
      "ds_read_b64_tr_b16 a[198:199], v17 offset:53760\n"
      "s_add_u32 s88, s63, s71\n"
      "s_add_u32 m0, s85, 0x24000\n"
      "s_waitcnt lgkmcnt(12)\n"
      "v_mfma_f32_16x16x32_bf16 v[128:131], a[168:171], a[104:107], v[128:131]\n"
      "buffer_load_dwordx4 v192, s[20:23], s88 offen lds\n"
      "s_waitcnt lgkmcnt(10)\n"
      "v_mfma_f32_16x16x32_bf16 v[128:131], a[172:175], a[116:119], v[128:131]\n"
      "v_mfma_f32_16x16x32_bf16 v[132:135], a[176:179], a[96:99], v[132:135]\n"
      "buffer_load_dwordx4 v193, s[20:23], s88 offen offset:1024 lds\n"
      "s_waitcnt lgkmcnt(8)\n"
      "v_mfma_f32_16x16x32_bf16 v[132:135], a[180:183], a[108:111], v[132:135]\n"
      "v_mfma_f32_16x16x32_bf16 v[136:139], a[176:179], a[100:103], v[136:139]\n"
      "buffer_load_dwordx4 v194, s[20:23], s88 offen offset:2048 lds\n"
      "v_mfma_f32_16x16x32_bf16 v[136:139], a[180:183], a[112:115], v[136:139]\n"
      "v_mfma_f32_16x16x32_bf16 v[140:143], a[176:179], a[104:107], v[140:143]\n"
      "buffer_load_dwordx4 v195, s[20:23], s88 offen offset:3072 lds\n"
      "v_mfma_f32_16x16x32_bf16 v[140:143], a[180:183], a[116:119], v[140:143]\n"
      "s_waitcnt vmcnt(17) lgkmcnt(0)\n"
      "s_barrier\n"
      "v_mfma_f32_16x16x32_bf16 v[144:147], a[184:187], a[96:99], v[144:147]\n"
      "ds_read_b64_tr_b16 a[168:169], v200\n"
      "ds_read_b128 a[120:123], v28 offset:49152\n"
      "v_mfma_f32_16x16x32_bf16 v[144:147], a[188:191], a[108:111], v[144:147]\n"
      "ds_read_b64_tr_b16 a[170:171], v200 offset:512\n"
      "ds_read_b128 a[132:135], v29 offset:49152\n"
      "v_mfma_f32_16x16x32_bf16 v[148:151], a[184:187], a[100:103], v[148:151]\n"
      "ds_read_b64_tr_b16 a[172:173], v200 offset:4096\n"
      "ds_read_b128 a[124:127], v28 offset:51200\n"
      "v_mfma_f32_16x16x32_bf16 v[148:151], a[188:191], a[112:115], v[148:151]\n"
      "ds_read_b64_tr_b16 a[174:175], v200 offset:4608\n"
      "ds_read_b128 a[136:139], v29 offset:51200\n"
      "v_mfma_f32_16x16x32_bf16 v[152:155], a[184:187], a[104:107], v[152:155]\n"
      "ds_read_b64_tr_b16 a[176:177], v201\n"
      "ds_read_b128 a[128:131], v28 offset:53248\n"
      "v_mfma_f32_16x16x32_bf16 v[152:155], a[188:191], a[116:119], v[152:155]\n"
      "ds_read_b64_tr_b16 a[178:179], v201 offset:512\n"
      "ds_read_b128 a[140:143], v29 offset:53248\n"
      "v_mfma_f32_16x16x32_bf16 v[156:159], a[192:195], a[96:99], v[156:159]\n"
      "ds_read_b64_tr_b16 a[180:181], v201 offset:4096\n"
      "v_mfma_f32_16x16x32_bf16 v[156:159], a[196:199], a[108:111], v[156:159]\n"
      "ds_read_b64_tr_b16 a[182:183], v201 offset:4608\n"
      "s_add_u32 s88, s64, s71\n"
      "s_add_u32 m0, s85, 0x18000\n"
      "v_mfma_f32_16x16x32_bf16 v[160:163], a[192:195], a[100:103], v[160:163]\n"
      "ds_read_b64_tr_b16 a[184:185], v202\n"
      "buffer_load_dwordx4 v192, s[20:23], s88 offen lds\n"
      "v_mfma_f32_16x16x32_bf16 v[160:163], a[196:199], a[112:115], v[160:163]\n"
      "ds_read_b64_tr_b16 a[186:187], v202 offset:512\n"
      "v_mfma_f32_16x16x32_bf16 v[164:167], a[192:195], a[104:107], v[164:167]\n"
      "ds_read_b64_tr_b16 a[188:189], v202 offset:4096\n"
      "buffer_load_dwordx4 v193, s[20:23], s88 offen offset:1024 lds\n"
      "v_mfma_f32_16x16x32_bf16 v[164:167], a[196:199], a[116:119], v[164:167]\n"
      "ds_read_b64_tr_b16 a[190:191], v202 offset:4608\n"
      "s_waitcnt lgkmcnt(15)\n"
      "v_mfma_f32_16x16x32_bf16 v[120:123], a[168:171], a[120:123], v[120:123]\n"
      "ds_read_b64_tr_b16 a[192:193], v203\n"
      "buffer_load_dwordx4 v194, s[20:23], s88 offen offset:2048 lds\n"
      "s_waitcnt lgkmcnt(12)\n"
      "v_mfma_f32_16x16x32_bf16 v[120:123], a[172:175], a[132:135], v[120:123]\n"
      "ds_read_b64_tr_b16 a[194:195], v203 offset:512\n"
      "v_mfma_f32_16x16x32_bf16 v[124:127], a[168:171], a[124:127], v[124:127]\n"
      "ds_read_b64_tr_b16 a[196:197], v203 offset:4096\n"
      "buffer_load_dwordx4 v195, s[20:23], s88 offen offset:3072 lds\n"
      "s_waitcnt lgkmcnt(13)\n"
      "v_mfma_f32_16x16x32_bf16 v[124:127], a[172:175], a[136:139], v[124:127]\n"
      "ds_read_b64_tr_b16 a[198:199], v203 offset:4608\n"
      "s_add_u32 s88, s65, s71\n"
      "s_add_u32 m0, s85, 0xc000\n"
      "s_waitcnt lgkmcnt(12)\n"
      "v_mfma_f32_16x16x32_bf16 v[128:131], a[168:171], a[128:131], v[128:131]\n"
      "buffer_load_dwordx4 v192, s[20:23], s88 offen lds\n"
      "s_waitcnt lgkmcnt(10)\n"
      "v_mfma_f32_16x16x32_bf16 v[128:131], a[172:175], a[140:143], v[128:131]\n"
      "v_mfma_f32_16x16x32_bf16 v[132:135], a[176:179], a[120:123], v[132:135]\n"
      "buffer_load_dwordx4 v193, s[20:23], s88 offen offset:1024 lds\n"
      "s_waitcnt lgkmcnt(8)\n"
      "v_mfma_f32_16x16x32_bf16 v[132:135], a[180:183], a[132:135], v[132:135]\n"
      "v_mfma_f32_16x16x32_bf16 v[136:139], a[176:179], a[124:127], v[136:139]\n"
      "buffer_load_dwordx4 v194, s[20:23], s88 offen offset:2048 lds\n"
      "v_mfma_f32_16x16x32_bf16 v[136:139], a[180:183], a[136:139], v[136:139]\n"
      "v_mfma_f32_16x16x32_bf16 v[140:143], a[176:179], a[128:131], v[140:143]\n"
      "buffer_load_dwordx4 v195, s[20:23], s88 offen offset:3072 lds\n"
      "v_mfma_f32_16x16x32_bf16 v[140:143], a[180:183], a[140:143], v[140:143]\n"
      "s_waitcnt vmcnt(12) lgkmcnt(0)\n"
      "s_barrier\n"
      "v_mfma_f32_16x16x32_bf16 v[144:147], a[184:187], a[120:123], v[144:147]\n"
      "ds_read_b64_tr_b16 a[168:169], v200 offset:16384\n"
      "ds_read_b128 a[144:147], v28 offset:61440\n"
      "v_mfma_f32_16x16x32_bf16 v[144:147], a[188:191], a[132:135], v[144:147]\n"
      "ds_read_b64_tr_b16 a[170:171], v200 offset:16896\n"
      "ds_read_b128 a[156:159], v29 offset:61440\n"
      "v_mfma_f32_16x16x32_bf16 v[148:151], a[184:187], a[124:127], v[148:151]\n"
      "ds_read_b64_tr_b16 a[172:173], v200 offset:20480\n"
      "ds_read_b128 a[148:151], v28 offset:63488\n"
      "v_mfma_f32_16x16x32_bf16 v[148:151], a[188:191], a[136:139], v[148:151]\n"
      "ds_read_b64_tr_b16 a[174:175], v200 offset:20992\n"
      "ds_read_b128 a[160:163], v29 offset:63488\n"
      "v_mfma_f32_16x16x32_bf16 v[152:155], a[184:187], a[128:131], v[152:155]\n"
      "ds_read_b64_tr_b16 a[176:177], v201 offset:16384\n"
      "v_add_u32 v28, 0x10000, v28\n"
      "ds_read_b128 a[152:155], v28\n"
      "v_mfma_f32_16x16x32_bf16 v[152:155], a[188:191], a[140:143], v[152:155]\n"
      "ds_read_b64_tr_b16 a[178:179], v201 offset:16896\n"
      "v_add_u32 v29, 0x10000, v29\n"
      "ds_read_b128 a[164:167], v29\n"
      "v_mfma_f32_16x16x32_bf16 v[156:159], a[192:195], a[120:123], v[156:159]\n"
      "ds_read_b64_tr_b16 a[180:181], v201 offset:20480\n"
      "s_add_u32 s88, s66, s71\n"
      "s_add_u32 m0, s85, 0x1c000\n"
      "v_mfma_f32_16x16x32_bf16 v[156:159], a[196:199], a[132:135], v[156:159]\n"
      "ds_read_b64_tr_b16 a[182:183], v201 offset:20992\n"
      "buffer_load_dwordx4 v192, s[20:23], s88 offen lds\n"
      "v_mfma_f32_16x16x32_bf16 v[160:163], a[192:195], a[124:127], v[160:163]\n"
      "ds_read_b64_tr_b16 a[184:185], v202 offset:16384\n"
      "v_mfma_f32_16x16x32_bf16 v[160:163], a[196:199], a[136:139], v[160:163]\n"
      "ds_read_b64_tr_b16 a[186:187], v202 offset:16896\n"
      "buffer_load_dwordx4 v193, s[20:23], s88 offen offset:1024 lds\n"
      "v_mfma_f32_16x16x32_bf16 v[164:167], a[192:195], a[128:131], v[164:167]\n"
      "ds_read_b64_tr_b16 a[188:189], v202 offset:20480\n"
      "v_mfma_f32_16x16x32_bf16 v[164:167], a[196:199], a[140:143], v[164:167]\n"
      "ds_read_b64_tr_b16 a[190:191], v202 offset:20992\n"
      "buffer_load_dwordx4 v194, s[20:23], s88 offen offset:2048 lds\n"
      "s_waitcnt lgkmcnt(15)\n"
      "v_mfma_f32_16x16x32_bf16 v[120:123], a[168:171], a[144:147], v[120:123]\n"
      "ds_read_b64_tr_b16 a[192:193], v203 offset:16384\n"
      "ds_read_b64 a[200:201], v24\n"
      "s_waitcnt lgkmcnt(13)\n"
      "v_mfma_f32_16x16x32_bf16 v[120:123], a[172:175], a[156:159], v[120:123]\n"
      "ds_read_b64_tr_b16 a[194:195], v203 offset:16896\n"
      "ds_read_b64 a[202:203], v25\n"
      "v_mfma_f32_16x16x32_bf16 v[124:127], a[168:171], a[148:151], v[124:127]\n"
      "ds_read_b64_tr_b16 a[196:197], v203 offset:20480\n"
      "ds_read_b64 a[204:205], v24 offset:2048\n"
      "buffer_load_dwordx4 v195, s[20:23], s88 offen offset:3072 lds\n"
      "s_waitcnt lgkmcnt(15)\n"
      "v_mfma_f32_16x16x32_bf16 v[124:127], a[172:175], a[160:163], v[124:127]\n"
      "ds_read_b64_tr_b16 a[198:199], v203 offset:20992\n"
      "ds_read_b64 a[206:207], v25 offset:2048\n"
      "s_add_u32 s88, s67, s71\n"
      "s_add_u32 m0, s85, 0x10000\n"
      "s_waitcnt lgkmcnt(15)\n"
      "v_mfma_f32_16x16x32_bf16 v[128:131], a[168:171], a[152:155], v[128:131]\n"
      "ds_read_b64 a[208:209], v24 offset:8192\n"
      "buffer_load_dwordx4 v192, s[20:23], s88 offen lds\n"
      "s_waitcnt lgkmcnt(15)\n"
      "v_mfma_f32_16x16x32_bf16 v[128:131], a[172:175], a[164:167], v[128:131]\n"
      "ds_read_b64 a[210:211], v25 offset:8192\n"
      "v_mfma_f32_16x16x32_bf16 v[132:135], a[176:179], a[144:147], v[132:135]\n"
      "ds_read_b64 a[212:213], v24 offset:10240\n"
      "buffer_load_dwordx4 v193, s[20:23], s88 offen offset:1024 lds\n"
      "s_waitcnt lgkmcnt(15)\n"
      "v_mfma_f32_16x16x32_bf16 v[132:135], a[180:183], a[156:159], v[132:135]\n"
      "ds_read_b64 a[214:215], v25 offset:10240\n"
      "v_mfma_f32_16x16x32_bf16 v[136:139], a[176:179], a[148:151], v[136:139]\n"
      "ds_read_b64 a[216:217], v24 offset:16384\n"
      "buffer_load_dwordx4 v194, s[20:23], s88 offen offset:2048 lds\n"
      "v_mfma_f32_16x16x32_bf16 v[136:139], a[180:183], a[160:163], v[136:139]\n"
      "ds_read_b64 a[218:219], v25 offset:16384\n"
      "v_mfma_f32_16x16x32_bf16 v[140:143], a[176:179], a[152:155], v[140:143]\n"
      "ds_read_b64 a[220:221], v24 offset:18432\n"
      "buffer_load_dwordx4 v195, s[20:23], s88 offen offset:3072 lds\n"
      "v_mfma_f32_16x16x32_bf16 v[140:143], a[180:183], a[164:167], v[140:143]\n"
      "ds_read_b64 a[222:223], v25 offset:18432\n"
      "s_waitcnt vmcnt(16) lgkmcnt(9)\n"
      "s_barrier\n"
      "v_mfma_f32_16x16x32_bf16 v[144:147], a[184:187], a[144:147], v[144:147]\n"
      "ds_read_b64_tr_b16 a[168:169], v200 offset:32768\n"
      "v_cvt_pk_bf16_f32 v220, v120, v121\n"
      "v_mfma_f32_16x16x32_bf16 v[144:147], a[188:191], a[156:159], v[144:147]\n"
      "ds_read_b64_tr_b16 a[170:171], v200 offset:33280\n"
      "v_cvt_pk_bf16_f32 v221, v122, v123\n"
      "v_mfma_f32_16x16x32_bf16 v[148:151], a[184:187], a[148:151], v[148:151]\n"
      "ds_read_b64_tr_b16 a[172:173], v200 offset:36864\n"
      "v_cvt_pk_bf16_f32 v224, v124, v125\n"
      "v_mfma_f32_16x16x32_bf16 v[148:151], a[188:191], a[160:163], v[148:151]\n"
      "ds_read_b64_tr_b16 a[174:175], v200 offset:37376\n"
      "v_cvt_pk_bf16_f32 v225, v126, v127\n"
      "v_mfma_f32_16x16x32_bf16 v[152:155], a[184:187], a[152:155], v[152:155]\n"
      "ds_read_b64_tr_b16 a[176:177], v201 offset:32768\n"
      "v_cvt_pk_bf16_f32 v228, v128, v129\n"
      "s_add_u32 s88, s68, s71\n"
      "s_add_u32 m0, s85, 0x20000\n"
      "v_mfma_f32_16x16x32_bf16 v[152:155], a[188:191], a[164:167], v[152:155]\n"
      "ds_read_b64_tr_b16 a[178:179], v201 offset:33280\n"
      "v_cvt_pk_bf16_f32 v229, v130, v131\n"
      "buffer_load_dwordx4 v192, s[20:23], s88 offen lds\n"
      "v_mfma_f32_16x16x32_bf16 v[156:159], a[192:195], a[144:147], v[156:159]\n"
      "ds_read_b64_tr_b16 a[180:181], v201 offset:36864\n"
      "v_cvt_pk_bf16_f32 v222, v132, v133\n"
      "v_mfma_f32_16x16x32_bf16 v[156:159], a[196:199], a[156:159], v[156:159]\n"
      "ds_read_b64_tr_b16 a[182:183], v201 offset:37376\n"
      "v_cvt_pk_bf16_f32 v223, v134, v135\n"
      "buffer_load_dwordx4 v193, s[20:23], s88 offen offset:1024 lds\n"
      "v_mfma_f32_16x16x32_bf16 v[160:163], a[192:195], a[148:151], v[160:163]\n"
      "ds_read_b64_tr_b16 a[184:185], v202 offset:32768\n"
      "v_cvt_pk_bf16_f32 v226, v136, v137\n"
      "v_mfma_f32_16x16x32_bf16 v[160:163], a[196:199], a[160:163], v[160:163]\n"
      "ds_read_b64_tr_b16 a[186:187], v202 offset:33280\n"
      "v_cvt_pk_bf16_f32 v227, v138, v139\n"
      "buffer_load_dwordx4 v194, s[20:23], s88 offen offset:2048 lds\n"
      "v_mfma_f32_16x16x32_bf16 v[164:167], a[192:195], a[152:155], v[164:167]\n"
      "ds_read_b64_tr_b16 a[188:189], v202 offset:36864\n"
      "v_cvt_pk_bf16_f32 v230, v140, v141\n"
      "v_mfma_f32_16x16x32_bf16 v[164:167], a[196:199], a[164:167], v[164:167]\n"
      "ds_read_b64_tr_b16 a[190:191], v202 offset:37376\n"
      "v_cvt_pk_bf16_f32 v231, v142, v143\n"
      "buffer_load_dwordx4 v195, s[20:23], s88 offen offset:3072 lds\n"
      "s_waitcnt lgkmcnt(10)\n"
      "v_mfma_f32_16x16x32_bf16 v[120:123], a[168:171], a[0:3], 0\n"
      "ds_read_b64_tr_b16 a[192:193], v203 offset:32768\n"
      "v_cvt_pk_bf16_f32 v232, v144, v145\n"
      "ds_read_b64 a[224:225], v26\n"
      "s_waitcnt lgkmcnt(10)\n"
      "v_mfma_f32_16x16x32_bf16 v[120:123], a[172:175], a[12:15], v[120:123]\n"
      "ds_read_b64_tr_b16 a[194:195], v203 offset:33280\n"
      "v_cvt_pk_bf16_f32 v233, v146, v147\n"
      "ds_read_b64 a[226:227], v27\n"
      "v_mfma_f32_16x16x32_bf16 v[124:127], a[168:171], a[4:7], 0\n"
      "ds_read_b64_tr_b16 a[196:197], v203 offset:36864\n"
      "v_cvt_pk_bf16_f32 v236, v148, v149\n"
      "ds_read_b64 a[228:229], v26 offset:2048\n"
      "v_mfma_f32_16x16x32_bf16 v[124:127], a[172:175], a[16:19], v[124:127]\n"
      "ds_read_b64_tr_b16 a[198:199], v203 offset:37376\n"
      "v_cvt_pk_bf16_f32 v237, v150, v151\n"
      "ds_read_b64 a[230:231], v27 offset:2048\n"
      "s_add_u32 s88, s69, s71\n"
      "s_add_u32 m0, s85, 0x14000\n"
      "v_mfma_f32_16x16x32_bf16 v[128:131], a[168:171], a[8:11], 0\n"
      "v_cvt_pk_bf16_f32 v240, v152, v153\n"
      "ds_read_b64 a[232:233], v26 offset:8192\n"
      "buffer_load_dwordx4 v192, s[20:23], s88 offen lds\n"
      "v_mfma_f32_16x16x32_bf16 v[128:131], a[172:175], a[20:23], v[128:131]\n"
      "v_cvt_pk_bf16_f32 v241, v154, v155\n"
      "ds_read_b64 a[234:235], v27 offset:8192\n"
      "s_waitcnt lgkmcnt(15)\n"
      "v_mfma_f32_16x16x32_bf16 v[132:135], a[176:179], a[0:3], 0\n"
      "v_cvt_pk_bf16_f32 v234, v156, v157\n"
      "ds_read_b64 a[236:237], v26 offset:10240\n"
      "buffer_load_dwordx4 v193, s[20:23], s88 offen offset:1024 lds\n"
      "s_waitcnt lgkmcnt(15)\n"
      "v_mfma_f32_16x16x32_bf16 v[132:135], a[180:183], a[12:15], v[132:135]\n"
      "v_cvt_pk_bf16_f32 v235, v158, v159\n"
      "ds_read_b64 a[238:239], v27 offset:10240\n"
      "v_mfma_f32_16x16x32_bf16 v[136:139], a[176:179], a[4:7], 0\n"
      "v_cvt_pk_bf16_f32 v238, v160, v161\n"
      "ds_read_b64 a[240:241], v26 offset:16384\n"
      "buffer_load_dwordx4 v194, s[20:23], s88 offen offset:2048 lds\n"
      "v_mfma_f32_16x16x32_bf16 v[136:139], a[180:183], a[16:19], v[136:139]\n"
      "v_cvt_pk_bf16_f32 v239, v162, v163\n"
      "ds_read_b64 a[242:243], v27 offset:16384\n"
      "v_mfma_f32_16x16x32_bf16 v[140:143], a[176:179], a[8:11], 0\n"
      "v_cvt_pk_bf16_f32 v242, v164, v165\n"
      "ds_read_b64 a[244:245], v26 offset:18432\n"
      "buffer_load_dwordx4 v195, s[20:23], s88 offen offset:3072 lds\n"
      "v_mfma_f32_16x16x32_bf16 v[140:143], a[180:183], a[20:23], v[140:143]\n"
      "v_cvt_pk_bf16_f32 v243, v166, v167\n"
      "ds_read_b64 a[246:247], v27 offset:18432\n"
      "s_waitcnt vmcnt(20) lgkmcnt(9)\n"
      "s_barrier\n"
      "v_mfma_f32_16x16x32_bf16 v[144:147], a[184:187], a[0:3], 0\n"
      "ds_read_b64_tr_b16 a[168:169], v14 offset:49152\n"
      "v_mfma_f32_16x16x32_bf16 v[144:147], a[188:191], a[12:15], v[144:147]\n"
      "ds_read_b64_tr_b16 a[170:171], v14 offset:49664\n"
      "v_mfma_f32_16x16x32_bf16 v[148:151], a[184:187], a[4:7], 0\n"
      "ds_read_b64_tr_b16 a[172:173], v14 offset:53248\n"
      "v_mfma_f32_16x16x32_bf16 v[148:151], a[188:191], a[16:19], v[148:151]\n"
      "ds_read_b64_tr_b16 a[174:175], v14 offset:53760\n"
      "v_mfma_f32_16x16x32_bf16 v[152:155], a[184:187], a[8:11], 0\n"
      "ds_read_b64_tr_b16 a[176:177], v15 offset:49152\n"
      "v_mfma_f32_16x16x32_bf16 v[152:155], a[188:191], a[20:23], v[152:155]\n"
      "ds_read_b64_tr_b16 a[178:179], v15 offset:49664\n"
      "v_mfma_f32_16x16x32_bf16 v[156:159], a[192:195], a[0:3], 0\n"
      "ds_read_b64_tr_b16 a[180:181], v15 offset:53248\n"
      "v_mfma_f32_16x16x32_bf16 v[156:159], a[196:199], a[12:15], v[156:159]\n"
      "ds_read_b64_tr_b16 a[182:183], v15 offset:53760\n"
      "v_mfma_f32_16x16x32_bf16 v[160:163], a[192:195], a[4:7], 0\n"
      "ds_read_b64_tr_b16 a[184:185], v16 offset:49152\n"
      "v_mfma_f32_16x16x32_bf16 v[160:163], a[196:199], a[16:19], v[160:163]\n"
      "ds_read_b64_tr_b16 a[186:187], v16 offset:49664\n"
      "v_mfma_f32_16x16x32_bf16 v[164:167], a[192:195], a[8:11], 0\n"
      "ds_read_b64_tr_b16 a[188:189], v16 offset:53248\n"
      "v_mfma_f32_16x16x32_bf16 v[164:167], a[196:199], a[20:23], v[164:167]\n"
      "ds_read_b64_tr_b16 a[190:191], v16 offset:53760\n"
      "v_mfma_f32_16x16x32_bf16 v[48:51], a[200:203], v[220:223], 0\n"
      "ds_read_b64_tr_b16 a[192:193], v17 offset:49152\n"
      "v_mfma_f32_16x16x32_bf16 v[48:51], a[224:227], v[232:235], v[48:51]\n"
      "ds_read_b64_tr_b16 a[194:195], v17 offset:49664\n"
      "v_mfma_f32_16x16x32_bf16 v[52:55], a[200:203], v[224:227], 0\n"
      "ds_read_b64_tr_b16 a[196:197], v17 offset:53248\n"
      "v_mfma_f32_16x16x32_bf16 v[52:55], a[224:227], v[236:239], v[52:55]\n"
      "ds_read_b64_tr_b16 a[198:199], v17 offset:53760\n"
      "v_mfma_f32_16x16x32_bf16 v[56:59], a[200:203], v[228:231], 0\n"
      "v_mfma_f32_16x16x32_bf16 v[56:59], a[224:227], v[240:243], v[56:59]\n"
      "v_mfma_f32_16x16x32_bf16 v[60:63], a[204:207], v[220:223], 0\n"
      "s_waitcnt lgkmcnt(15)\n"
      "v_mfma_f32_16x16x32_bf16 v[60:63], a[228:231], v[232:235], v[60:63]\n"
      "v_mfma_f32_16x16x32_bf16 v[64:67], a[204:207], v[224:227], 0\n"
      "v_mfma_f32_16x16x32_bf16 v[64:67], a[228:231], v[236:239], v[64:67]\n"
      "v_mfma_f32_16x16x32_bf16 v[68:71], a[204:207], v[228:231], 0\n"
      "v_mfma_f32_16x16x32_bf16 v[68:71], a[228:231], v[240:243], v[68:71]\n"
      "v_mfma_f32_16x16x32_bf16 v[72:75], a[208:211], v[220:223], 0\n"
      "v_mfma_f32_16x16x32_bf16 v[72:75], a[232:235], v[232:235], v[72:75]\n"
      "v_mfma_f32_16x16x32_bf16 v[76:79], a[208:211], v[224:227], 0\n"
      "v_mfma_f32_16x16x32_bf16 v[76:79], a[232:235], v[236:239], v[76:79]\n"
      "v_mfma_f32_16x16x32_bf16 v[80:83], a[208:211], v[228:231], 0\n"
      "v_mfma_f32_16x16x32_bf16 v[80:83], a[232:235], v[240:243], v[80:83]\n"
      "v_mfma_f32_16x16x32_bf16 v[84:87], a[212:215], v[220:223], 0\n"
      "v_mfma_f32_16x16x32_bf16 v[84:87], a[236:239], v[232:235], v[84:87]\n"
      "v_mfma_f32_16x16x32_bf16 v[88:91], a[212:215], v[224:227], 0\n"
      "v_mfma_f32_16x16x32_bf16 v[88:91], a[236:239], v[236:239], v[88:91]\n"
      "v_mfma_f32_16x16x32_bf16 v[92:95], a[212:215], v[228:231], 0\n"
      "v_mfma_f32_16x16x32_bf16 v[92:95], a[236:239], v[240:243], v[92:95]\n"
      "v_mfma_f32_16x16x32_bf16 v[96:99], a[216:219], v[220:223], 0\n"
      "v_mfma_f32_16x16x32_bf16 v[96:99], a[240:243], v[232:235], v[96:99]\n"
      "v_mfma_f32_16x16x32_bf16 v[100:103], a[216:219], v[224:227], 0\n"
      "v_mfma_f32_16x16x32_bf16 v[100:103], a[240:243], v[236:239], v[100:103]\n"
      "s_add_u32 s88, s63, s72\n"
      "s_add_u32 m0, s85, 0x24000\n"
      "v_mfma_f32_16x16x32_bf16 v[104:107], a[216:219], v[228:231], 0\n"
      "buffer_load_dwordx4 v192, s[20:23], s88 offen lds\n"
      "v_mfma_f32_16x16x32_bf16 v[104:107], a[240:243], v[240:243], v[104:107]\n"
      "v_mfma_f32_16x16x32_bf16 v[108:111], a[220:223], v[220:223], 0\n"
      "buffer_load_dwordx4 v193, s[20:23], s88 offen offset:1024 lds\n"
      "v_mfma_f32_16x16x32_bf16 v[108:111], a[244:247], v[232:235], v[108:111]\n"
      "v_mfma_f32_16x16x32_bf16 v[112:115], a[220:223], v[224:227], 0\n"
      "buffer_load_dwordx4 v194, s[20:23], s88 offen offset:2048 lds\n"
      "v_mfma_f32_16x16x32_bf16 v[112:115], a[244:247], v[236:239], v[112:115]\n"
      "v_mfma_f32_16x16x32_bf16 v[116:119], a[220:223], v[228:231], 0\n"
      "buffer_load_dwordx4 v195, s[20:23], s88 offen offset:3072 lds\n"
      "v_mfma_f32_16x16x32_bf16 v[116:119], a[244:247], v[240:243], v[116:119]\n"
      "s_add_u32 s89, s71, 0\n"
      "s_add_u32 m0, s86, 0x6000\n"
      "s_waitcnt lgkmcnt(14)\n"
      "v_mfma_f32_16x16x32_bf16 v[120:123], a[168:171], a[24:27], v[120:123]\n"
      "buffer_load_dwordx4 v204, s[12:15], s89 offen nt lds\n"
      "s_waitcnt lgkmcnt(12)\n"
      "v_mfma_f32_16x16x32_bf16 v[120:123], a[172:175], a[36:39], v[120:123]\n"
      "v_mfma_f32_16x16x32_bf16 v[124:127], a[168:171], a[28:31], v[124:127]\n"
      "buffer_load_dwordx4 v205, s[12:15], s89 offen offset:1024 nt lds\n"
      "v_mfma_f32_16x16x32_bf16 v[124:127], a[172:175], a[40:43], v[124:127]\n"
      "s_add_u32 s89, s71, 0x4000\n"
      "s_add_u32 m0, s86, 0x8000\n"
      "v_mfma_f32_16x16x32_bf16 v[128:131], a[168:171], a[32:35], v[128:131]\n"
      "buffer_load_dwordx4 v204, s[12:15], s89 offen nt lds\n"
      "v_mfma_f32_16x16x32_bf16 v[128:131], a[172:175], a[44:47], v[128:131]\n"
      "s_waitcnt lgkmcnt(10)\n"
      "v_mfma_f32_16x16x32_bf16 v[132:135], a[176:179], a[24:27], v[132:135]\n"
      "buffer_load_dwordx4 v205, s[12:15], s89 offen offset:1024 nt lds\n"
      "s_waitcnt lgkmcnt(8)\n"
      "v_mfma_f32_16x16x32_bf16 v[132:135], a[180:183], a[36:39], v[132:135]\n"
      "s_add_u32 s89, s71, 0x8000\n"
      "s_add_u32 m0, s86, 0xa000\n"
      "v_mfma_f32_16x16x32_bf16 v[136:139], a[176:179], a[28:31], v[136:139]\n"
      "buffer_load_dwordx4 v204, s[12:15], s89 offen nt lds\n"
      "v_mfma_f32_16x16x32_bf16 v[136:139], a[180:183], a[40:43], v[136:139]\n"
      "v_mfma_f32_16x16x32_bf16 v[140:143], a[176:179], a[32:35], v[140:143]\n"
      "buffer_load_dwordx4 v205, s[12:15], s89 offen offset:1024 nt lds\n"
      "v_mfma_f32_16x16x32_bf16 v[140:143], a[180:183], a[44:47], v[140:143]\n"
      "s_waitcnt vmcnt(26)\n"
      "s_barrier\n"
      "s_waitcnt lgkmcnt(6)\n"
      "v_mfma_f32_16x16x32_bf16 v[144:147], a[184:187], a[24:27], v[144:147]\n"
      "ds_read_b64_tr_b16 a[168:169], v14\n"
      "s_waitcnt lgkmcnt(5)\n"
      "v_mfma_f32_16x16x32_bf16 v[144:147], a[188:191], a[36:39], v[144:147]\n"
      "ds_read_b64_tr_b16 a[170:171], v14 offset:512\n"
      "v_mfma_f32_16x16x32_bf16 v[148:151], a[184:187], a[28:31], v[148:151]\n"
      "ds_read_b64_tr_b16 a[172:173], v14 offset:4096\n"
      "v_mfma_f32_16x16x32_bf16 v[148:151], a[188:191], a[40:43], v[148:151]\n"
      "ds_read_b64_tr_b16 a[174:175], v14 offset:4608\n"
      "v_mfma_f32_16x16x32_bf16 v[152:155], a[184:187], a[32:35], v[152:155]\n"
      "ds_read_b64_tr_b16 a[176:177], v15\n"
      "v_mfma_f32_16x16x32_bf16 v[152:155], a[188:191], a[44:47], v[152:155]\n"
      "ds_read_b64_tr_b16 a[178:179], v15 offset:512\n"
      "s_waitcnt lgkmcnt(8)\n"
      "v_mfma_f32_16x16x32_bf16 v[156:159], a[192:195], a[24:27], v[156:159]\n"
      "ds_read_b64_tr_b16 a[180:181], v15 offset:4096\n"
      "s_waitcnt lgkmcnt(7)\n"
      "v_mfma_f32_16x16x32_bf16 v[156:159], a[196:199], a[36:39], v[156:159]\n"
      "ds_read_b64_tr_b16 a[182:183], v15 offset:4608\n"
      "v_mfma_f32_16x16x32_bf16 v[160:163], a[192:195], a[28:31], v[160:163]\n"
      "ds_read_b64_tr_b16 a[184:185], v16\n"
      "v_mfma_f32_16x16x32_bf16 v[160:163], a[196:199], a[40:43], v[160:163]\n"
      "ds_read_b64_tr_b16 a[186:187], v16 offset:512\n"
      "v_mfma_f32_16x16x32_bf16 v[164:167], a[192:195], a[32:35], v[164:167]\n"
      "ds_read_b64_tr_b16 a[188:189], v16 offset:4096\n"
      "v_mfma_f32_16x16x32_bf16 v[164:167], a[196:199], a[44:47], v[164:167]\n"
      "ds_read_b64_tr_b16 a[190:191], v16 offset:4608\n"
      "s_add_u32 s89, s72, 0\n"
      "s_add_u32 m0, s86, 0\n"
      "s_waitcnt lgkmcnt(10)\n"
      "v_mfma_f32_16x16x32_bf16 v[120:123], a[168:171], a[48:51], v[120:123]\n"
      "ds_read_b64_tr_b16 a[192:193], v17\n"
      "buffer_load_dwordx4 v204, s[12:15], s89 offen nt lds\n"
      "s_waitcnt lgkmcnt(9)\n"
      "v_mfma_f32_16x16x32_bf16 v[120:123], a[172:175], a[60:63], v[120:123]\n"
      "ds_read_b64_tr_b16 a[194:195], v17 offset:512\n"
      "v_mfma_f32_16x16x32_bf16 v[124:127], a[168:171], a[52:55], v[124:127]\n"
      "ds_read_b64_tr_b16 a[196:197], v17 offset:4096\n"
      "buffer_load_dwordx4 v205, s[12:15], s89 offen offset:1024 nt lds\n"
      "v_mfma_f32_16x16x32_bf16 v[124:127], a[172:175], a[64:67], v[124:127]\n"
      "ds_read_b64_tr_b16 a[198:199], v17 offset:4608\n"
      "s_add_u32 s89, s72, 0x4000\n"
      "s_add_u32 m0, s86, 0x2000\n"
      "v_mfma_f32_16x16x32_bf16 v[128:131], a[168:171], a[56:59], v[128:131]\n"
      "buffer_load_dwordx4 v204, s[12:15], s89 offen nt lds\n"
      "v_mfma_f32_16x16x32_bf16 v[128:131], a[172:175], a[68:71], v[128:131]\n"
      "s_waitcnt lgkmcnt(10)\n"
      "v_mfma_f32_16x16x32_bf16 v[132:135], a[176:179], a[48:51], v[132:135]\n"
      "buffer_load_dwordx4 v205, s[12:15], s89 offen offset:1024 nt lds\n"
      "s_waitcnt lgkmcnt(8)\n"
      "v_mfma_f32_16x16x32_bf16 v[132:135], a[180:183], a[60:63], v[132:135]\n"
      "s_add_u32 s89, s72, 0x8000\n"
      "s_add_u32 m0, s86, 0x4000\n"
      "v_mfma_f32_16x16x32_bf16 v[136:139], a[176:179], a[52:55], v[136:139]\n"
      "buffer_load_dwordx4 v204, s[12:15], s89 offen nt lds\n"
      "v_mfma_f32_16x16x32_bf16 v[136:139], a[180:183], a[64:67], v[136:139]\n"
      "v_mfma_f32_16x16x32_bf16 v[140:143], a[176:179], a[56:59], v[140:143]\n"
      "buffer_load_dwordx4 v205, s[12:15], s89 offen offset:1024 nt lds\n"
      "v_mfma_f32_16x16x32_bf16 v[140:143], a[180:183], a[68:71], v[140:143]\n"
      "s_waitcnt vmcnt(28)\n"
      "s_barrier\n"
      "s_waitcnt lgkmcnt(6)\n"
      "v_mfma_f32_16x16x32_bf16 v[144:147], a[184:187], a[48:51], v[144:147]\n"
      "ds_read_b64_tr_b16 a[168:169], v200\n"
      "s_waitcnt lgkmcnt(5)\n"
      "v_mfma_f32_16x16x32_bf16 v[144:147], a[188:191], a[60:63], v[144:147]\n"
      "ds_read_b64_tr_b16 a[170:171], v200 offset:512\n"
      "v_mfma_f32_16x16x32_bf16 v[148:151], a[184:187], a[52:55], v[148:151]\n"
      "ds_read_b64_tr_b16 a[172:173], v200 offset:4096\n"
      "v_mfma_f32_16x16x32_bf16 v[148:151], a[188:191], a[64:67], v[148:151]\n"
      "ds_read_b64_tr_b16 a[174:175], v200 offset:4608\n"
      "v_mfma_f32_16x16x32_bf16 v[152:155], a[184:187], a[56:59], v[152:155]\n"
      "ds_read_b64_tr_b16 a[176:177], v201\n"
      "v_mfma_f32_16x16x32_bf16 v[152:155], a[188:191], a[68:71], v[152:155]\n"
      "ds_read_b64_tr_b16 a[178:179], v201 offset:512\n"
      "s_waitcnt lgkmcnt(8)\n"
      "v_mfma_f32_16x16x32_bf16 v[156:159], a[192:195], a[48:51], v[156:159]\n"
      "ds_read_b64_tr_b16 a[180:181], v201 offset:4096\n"
      "s_waitcnt lgkmcnt(7)\n"
      "v_mfma_f32_16x16x32_bf16 v[156:159], a[196:199], a[60:63], v[156:159]\n"
      "ds_read_b64_tr_b16 a[182:183], v201 offset:4608\n"
      "v_mfma_f32_16x16x32_bf16 v[160:163], a[192:195], a[52:55], v[160:163]\n"
      "ds_read_b64_tr_b16 a[184:185], v202\n"
      "v_mfma_f32_16x16x32_bf16 v[160:163], a[196:199], a[64:67], v[160:163]\n"
      "ds_read_b64_tr_b16 a[186:187], v202 offset:512\n"
      "v_mfma_f32_16x16x32_bf16 v[164:167], a[192:195], a[56:59], v[164:167]\n"
      "ds_read_b64_tr_b16 a[188:189], v202 offset:4096\n"
      "v_mfma_f32_16x16x32_bf16 v[164:167], a[196:199], a[68:71], v[164:167]\n"
      "ds_read_b64_tr_b16 a[190:191], v202 offset:4608\n"
      "s_waitcnt lgkmcnt(10)\n"
      "v_mfma_f32_16x16x32_bf16 v[120:123], a[168:171], a[72:75], v[120:123]\n"
      "ds_read_b64_tr_b16 a[192:193], v203\n"
      "s_waitcnt lgkmcnt(9)\n"
      "v_mfma_f32_16x16x32_bf16 v[120:123], a[172:175], a[84:87], v[120:123]\n"
      "ds_read_b64_tr_b16 a[194:195], v203 offset:512\n"
      "v_mfma_f32_16x16x32_bf16 v[124:127], a[168:171], a[76:79], v[124:127]\n"
      "ds_read_b64_tr_b16 a[196:197], v203 offset:4096\n"
      "v_mfma_f32_16x16x32_bf16 v[124:127], a[172:175], a[88:91], v[124:127]\n"
      "ds_read_b64_tr_b16 a[198:199], v203 offset:4608\n"
      "v_mfma_f32_16x16x32_bf16 v[128:131], a[168:171], a[80:83], v[128:131]\n"
      "v_mfma_f32_16x16x32_bf16 v[128:131], a[172:175], a[92:95], v[128:131]\n"
      "s_waitcnt lgkmcnt(10)\n"
      "v_mfma_f32_16x16x32_bf16 v[132:135], a[176:179], a[72:75], v[132:135]\n"
      "s_waitcnt lgkmcnt(8)\n"
      "v_mfma_f32_16x16x32_bf16 v[132:135], a[180:183], a[84:87], v[132:135]\n"
      "v_mfma_f32_16x16x32_bf16 v[136:139], a[176:179], a[76:79], v[136:139]\n"
      "v_mfma_f32_16x16x32_bf16 v[136:139], a[180:183], a[88:91], v[136:139]\n"
      "v_mfma_f32_16x16x32_bf16 v[140:143], a[176:179], a[80:83], v[140:143]\n"
      "v_mfma_f32_16x16x32_bf16 v[140:143], a[180:183], a[92:95], v[140:143]\n"
      "s_waitcnt vmcnt(24)\n"
      "s_barrier\n"
      "s_waitcnt lgkmcnt(6)\n"
      "v_mfma_f32_16x16x32_bf16 v[144:147], a[184:187], a[72:75], v[144:147]\n"
      "ds_read_b64_tr_b16 a[168:169], v14 offset:16384\n"
      "s_waitcnt lgkmcnt(5)\n"
      "v_mfma_f32_16x16x32_bf16 v[144:147], a[188:191], a[84:87], v[144:147]\n"
      "ds_read_b64_tr_b16 a[170:171], v14 offset:16896\n"
      "v_mfma_f32_16x16x32_bf16 v[148:151], a[184:187], a[76:79], v[148:151]\n"
      "ds_read_b64_tr_b16 a[172:173], v14 offset:20480\n"
      "v_mfma_f32_16x16x32_bf16 v[148:151], a[188:191], a[88:91], v[148:151]\n"
      "ds_read_b64_tr_b16 a[174:175], v14 offset:20992\n"
      "v_mfma_f32_16x16x32_bf16 v[152:155], a[184:187], a[80:83], v[152:155]\n"
      "ds_read_b64_tr_b16 a[176:177], v15 offset:16384\n"
      "v_mfma_f32_16x16x32_bf16 v[152:155], a[188:191], a[92:95], v[152:155]\n"
      "ds_read_b64_tr_b16 a[178:179], v15 offset:16896\n"
      "s_waitcnt lgkmcnt(8)\n"
      "v_mfma_f32_16x16x32_bf16 v[156:159], a[192:195], a[72:75], v[156:159]\n"
      "ds_read_b64_tr_b16 a[180:181], v15 offset:20480\n"
      "s_waitcnt lgkmcnt(7)\n"
      "v_mfma_f32_16x16x32_bf16 v[156:159], a[196:199], a[84:87], v[156:159]\n"
      "ds_read_b64_tr_b16 a[182:183], v15 offset:20992\n"
      "v_mfma_f32_16x16x32_bf16 v[160:163], a[192:195], a[76:79], v[160:163]\n"
      "ds_read_b64_tr_b16 a[184:185], v16 offset:16384\n"
      "v_mfma_f32_16x16x32_bf16 v[160:163], a[196:199], a[88:91], v[160:163]\n"
      "ds_read_b64_tr_b16 a[186:187], v16 offset:16896\n"
      "v_mfma_f32_16x16x32_bf16 v[164:167], a[192:195], a[80:83], v[164:167]\n"
      "ds_read_b64_tr_b16 a[188:189], v16 offset:20480\n"
      "v_mfma_f32_16x16x32_bf16 v[164:167], a[196:199], a[92:95], v[164:167]\n"
      "ds_read_b64_tr_b16 a[190:191], v16 offset:20992\n"
      "s_waitcnt lgkmcnt(10)\n"
      "v_mfma_f32_16x16x32_bf16 v[120:123], a[168:171], a[96:99], v[120:123]\n"
      "ds_read_b64_tr_b16 a[192:193], v17 offset:16384\n"
      "s_waitcnt lgkmcnt(9)\n"
      "v_mfma_f32_16x16x32_bf16 v[120:123], a[172:175], a[108:111], v[120:123]\n"
      "ds_read_b64_tr_b16 a[194:195], v17 offset:16896\n"
      "v_mfma_f32_16x16x32_bf16 v[124:127], a[168:171], a[100:103], v[124:127]\n"
      "ds_read_b64_tr_b16 a[196:197], v17 offset:20480\n"
      "v_mfma_f32_16x16x32_bf16 v[124:127], a[172:175], a[112:115], v[124:127]\n"
      "ds_read_b64_tr_b16 a[198:199], v17 offset:20992\n"
      "v_mfma_f32_16x16x32_bf16 v[128:131], a[168:171], a[104:107], v[128:131]\n"
      "v_mfma_f32_16x16x32_bf16 v[128:131], a[172:175], a[116:119], v[128:131]\n"
      "s_waitcnt lgkmcnt(10)\n"
      "v_mfma_f32_16x16x32_bf16 v[132:135], a[176:179], a[96:99], v[132:135]\n"
      "s_waitcnt lgkmcnt(8)\n"
      "v_mfma_f32_16x16x32_bf16 v[132:135], a[180:183], a[108:111], v[132:135]\n"
      "v_mfma_f32_16x16x32_bf16 v[136:139], a[176:179], a[100:103], v[136:139]\n"
      "v_mfma_f32_16x16x32_bf16 v[136:139], a[180:183], a[112:115], v[136:139]\n"
      "v_mfma_f32_16x16x32_bf16 v[140:143], a[176:179], a[104:107], v[140:143]\n"
      "v_mfma_f32_16x16x32_bf16 v[140:143], a[180:183], a[116:119], v[140:143]\n"
      "s_waitcnt vmcnt(20)\n"
      "s_barrier\n"
      "s_waitcnt lgkmcnt(6)\n"
      "v_mfma_f32_16x16x32_bf16 v[144:147], a[184:187], a[96:99], v[144:147]\n"
      "ds_read_b64_tr_b16 a[168:169], v200 offset:16384\n"
      "s_waitcnt lgkmcnt(5)\n"
      "v_mfma_f32_16x16x32_bf16 v[144:147], a[188:191], a[108:111], v[144:147]\n"
      "ds_read_b64_tr_b16 a[170:171], v200 offset:16896\n"
      "v_mfma_f32_16x16x32_bf16 v[148:151], a[184:187], a[100:103], v[148:151]\n"
      "ds_read_b64_tr_b16 a[172:173], v200 offset:20480\n"
      "v_mfma_f32_16x16x32_bf16 v[148:151], a[188:191], a[112:115], v[148:151]\n"
      "ds_read_b64_tr_b16 a[174:175], v200 offset:20992\n"
      "v_mfma_f32_16x16x32_bf16 v[152:155], a[184:187], a[104:107], v[152:155]\n"
      "ds_read_b64_tr_b16 a[176:177], v201 offset:16384\n"
      "v_mfma_f32_16x16x32_bf16 v[152:155], a[188:191], a[116:119], v[152:155]\n"
      "ds_read_b64_tr_b16 a[178:179], v201 offset:16896\n"
      "s_waitcnt lgkmcnt(8)\n"
      "v_mfma_f32_16x16x32_bf16 v[156:159], a[192:195], a[96:99], v[156:159]\n"
      "ds_read_b64_tr_b16 a[180:181], v201 offset:20480\n"
      "s_waitcnt lgkmcnt(7)\n"
      "v_mfma_f32_16x16x32_bf16 v[156:159], a[196:199], a[108:111], v[156:159]\n"
      "ds_read_b64_tr_b16 a[182:183], v201 offset:20992\n"
      "v_mfma_f32_16x16x32_bf16 v[160:163], a[192:195], a[100:103], v[160:163]\n"
      "ds_read_b64_tr_b16 a[184:185], v202 offset:16384\n"
      "v_mfma_f32_16x16x32_bf16 v[160:163], a[196:199], a[112:115], v[160:163]\n"
      "ds_read_b64_tr_b16 a[186:187], v202 offset:16896\n"
      "v_mfma_f32_16x16x32_bf16 v[164:167], a[192:195], a[104:107], v[164:167]\n"
      "ds_read_b64_tr_b16 a[188:189], v202 offset:20480\n"
      "v_mfma_f32_16x16x32_bf16 v[164:167], a[196:199], a[116:119], v[164:167]\n"
      "ds_read_b64_tr_b16 a[190:191], v202 offset:20992\n"
      "s_waitcnt lgkmcnt(10)\n"
      "v_mfma_f32_16x16x32_bf16 v[120:123], a[168:171], a[120:123], v[120:123]\n"
      "ds_read_b64_tr_b16 a[192:193], v203 offset:16384\n"
      "s_waitcnt lgkmcnt(9)\n"
      "v_mfma_f32_16x16x32_bf16 v[120:123], a[172:175], a[132:135], v[120:123]\n"
      "ds_read_b64_tr_b16 a[194:195], v203 offset:16896\n"
      "v_mfma_f32_16x16x32_bf16 v[124:127], a[168:171], a[124:127], v[124:127]\n"
      "ds_read_b64_tr_b16 a[196:197], v203 offset:20480\n"
      "v_mfma_f32_16x16x32_bf16 v[124:127], a[172:175], a[136:139], v[124:127]\n"
      "ds_read_b64_tr_b16 a[198:199], v203 offset:20992\n"
      "s_add_u32 s88, s64, s72\n"
      "s_add_u32 m0, s85, 0x18000\n"
      "v_mfma_f32_16x16x32_bf16 v[128:131], a[168:171], a[128:131], v[128:131]\n"
      "buffer_load_dwordx4 v192, s[20:23], s88 offen lds\n"
      "v_mfma_f32_16x16x32_bf16 v[128:131], a[172:175], a[140:143], v[128:131]\n"
      "s_waitcnt lgkmcnt(10)\n"
      "v_mfma_f32_16x16x32_bf16 v[132:135], a[176:179], a[120:123], v[132:135]\n"
      "buffer_load_dwordx4 v193, s[20:23], s88 offen offset:1024 lds\n"
      "s_waitcnt lgkmcnt(8)\n"
      "v_mfma_f32_16x16x32_bf16 v[132:135], a[180:183], a[132:135], v[132:135]\n"
      "v_mfma_f32_16x16x32_bf16 v[136:139], a[176:179], a[124:127], v[136:139]\n"
      "buffer_load_dwordx4 v194, s[20:23], s88 offen offset:2048 lds\n"
      "v_mfma_f32_16x16x32_bf16 v[136:139], a[180:183], a[136:139], v[136:139]\n"
      "v_mfma_f32_16x16x32_bf16 v[140:143], a[176:179], a[128:131], v[140:143]\n"
      "buffer_load_dwordx4 v195, s[20:23], s88 offen offset:3072 lds\n"
      "v_mfma_f32_16x16x32_bf16 v[140:143], a[180:183], a[140:143], v[140:143]\n"
      "s_waitcnt vmcnt(10)\n"
      "s_barrier\n"
      "s_waitcnt lgkmcnt(6)\n"
      "v_mfma_f32_16x16x32_bf16 v[144:147], a[184:187], a[120:123], v[144:147]\n"
      "ds_read_b64_tr_b16 a[168:169], v14 offset:32768\n"
      "s_waitcnt lgkmcnt(5)\n"
      "v_mfma_f32_16x16x32_bf16 v[144:147], a[188:191], a[132:135], v[144:147]\n"
      "ds_read_b64_tr_b16 a[170:171], v14 offset:33280\n"
      "v_mfma_f32_16x16x32_bf16 v[148:151], a[184:187], a[124:127], v[148:151]\n"
      "ds_read_b64_tr_b16 a[172:173], v14 offset:36864\n"
      "v_mfma_f32_16x16x32_bf16 v[148:151], a[188:191], a[136:139], v[148:151]\n"
      "ds_read_b64_tr_b16 a[174:175], v14 offset:37376\n"
      "v_mfma_f32_16x16x32_bf16 v[152:155], a[184:187], a[128:131], v[152:155]\n"
      "ds_read_b64_tr_b16 a[176:177], v15 offset:32768\n"
      "s_add_u32 s88, s65, s72\n"
      "s_add_u32 m0, s85, 0xc000\n"
      "v_mfma_f32_16x16x32_bf16 v[152:155], a[188:191], a[140:143], v[152:155]\n"
      "ds_read_b64_tr_b16 a[178:179], v15 offset:33280\n"
      "buffer_load_dwordx4 v192, s[20:23], s88 offen lds\n"
      "s_waitcnt lgkmcnt(8)\n"
      "v_mfma_f32_16x16x32_bf16 v[156:159], a[192:195], a[120:123], v[156:159]\n"
      "ds_read_b64_tr_b16 a[180:181], v15 offset:36864\n"
      "s_waitcnt lgkmcnt(7)\n"
      "v_mfma_f32_16x16x32_bf16 v[156:159], a[196:199], a[132:135], v[156:159]\n"
      "ds_read_b64_tr_b16 a[182:183], v15 offset:37376\n"
      "buffer_load_dwordx4 v193, s[20:23], s88 offen offset:1024 lds\n"
      "v_mfma_f32_16x16x32_bf16 v[160:163], a[192:195], a[124:127], v[160:163]\n"
      "ds_read_b64_tr_b16 a[184:185], v16 offset:32768\n"
      "v_mfma_f32_16x16x32_bf16 v[160:163], a[196:199], a[136:139], v[160:163]\n"
      "ds_read_b64_tr_b16 a[186:187], v16 offset:33280\n"
      "buffer_load_dwordx4 v194, s[20:23], s88 offen offset:2048 lds\n"
      "v_mfma_f32_16x16x32_bf16 v[164:167], a[192:195], a[128:131], v[164:167]\n"
      "ds_read_b64_tr_b16 a[188:189], v16 offset:36864\n"
      "v_mfma_f32_16x16x32_bf16 v[164:167], a[196:199], a[140:143], v[164:167]\n"
      "ds_read_b64_tr_b16 a[190:191], v16 offset:37376\n"
      "buffer_load_dwordx4 v195, s[20:23], s88 offen offset:3072 lds\n"
      "s_waitcnt lgkmcnt(10)\n"
      "v_mfma_f32_16x16x32_bf16 v[120:123], a[168:171], a[144:147], v[120:123]\n"
      "ds_read_b64_tr_b16 a[192:193], v17 offset:32768\n"
      "ds_read_b64 a[200:201], v24 offset:24576\n"
      "s_waitcnt lgkmcnt(10)\n"
      "v_mfma_f32_16x16x32_bf16 v[120:123], a[172:175], a[156:159], v[120:123]\n"
      "ds_read_b64_tr_b16 a[194:195], v17 offset:33280\n"
      "ds_read_b64 a[202:203], v25 offset:24576\n"
      "v_mfma_f32_16x16x32_bf16 v[124:127], a[168:171], a[148:151], v[124:127]\n"
      "ds_read_b64_tr_b16 a[196:197], v17 offset:36864\n"
      "ds_read_b64 a[204:205], v24 offset:26624\n"
      "v_mfma_f32_16x16x32_bf16 v[124:127], a[172:175], a[160:163], v[124:127]\n"
      "ds_read_b64_tr_b16 a[198:199], v17 offset:37376\n"
      "ds_read_b64 a[206:207], v25 offset:26624\n"
      "s_add_u32 s88, s66, s72\n"
      "s_add_u32 m0, s85, 0x1c000\n"
      "v_mfma_f32_16x16x32_bf16 v[128:131], a[168:171], a[152:155], v[128:131]\n"
      "ds_read_b64 a[208:209], v24 offset:32768\n"
      "buffer_load_dwordx4 v192, s[20:23], s88 offen lds\n"
      "v_mfma_f32_16x16x32_bf16 v[128:131], a[172:175], a[164:167], v[128:131]\n"
      "ds_read_b64 a[210:211], v25 offset:32768\n"
      "s_waitcnt lgkmcnt(15)\n"
      "v_mfma_f32_16x16x32_bf16 v[132:135], a[176:179], a[144:147], v[132:135]\n"
      "ds_read_b64 a[212:213], v24 offset:34816\n"
      "buffer_load_dwordx4 v193, s[20:23], s88 offen offset:1024 lds\n"
      "s_waitcnt lgkmcnt(15)\n"
      "v_mfma_f32_16x16x32_bf16 v[132:135], a[180:183], a[156:159], v[132:135]\n"
      "ds_read_b64 a[214:215], v25 offset:34816\n"
      "v_mfma_f32_16x16x32_bf16 v[136:139], a[176:179], a[148:151], v[136:139]\n"
      "ds_read_b64 a[216:217], v24 offset:40960\n"
      "buffer_load_dwordx4 v194, s[20:23], s88 offen offset:2048 lds\n"
      "v_mfma_f32_16x16x32_bf16 v[136:139], a[180:183], a[160:163], v[136:139]\n"
      "ds_read_b64 a[218:219], v25 offset:40960\n"
      "v_mfma_f32_16x16x32_bf16 v[140:143], a[176:179], a[152:155], v[140:143]\n"
      "ds_read_b64 a[220:221], v24 offset:43008\n"
      "buffer_load_dwordx4 v195, s[20:23], s88 offen offset:3072 lds\n"
      "v_mfma_f32_16x16x32_bf16 v[140:143], a[180:183], a[164:167], v[140:143]\n"
      "ds_read_b64 a[222:223], v25 offset:43008\n"
      "s_barrier\n"
      "s_waitcnt lgkmcnt(15)\n"
      "v_mfma_f32_16x16x32_bf16 v[144:147], a[184:187], a[144:147], v[144:147]\n"
      "ds_read_b64_tr_b16 a[168:169], v200 offset:32768\n"
      "v_cvt_pk_bf16_f32 v168, v120, v121\n"
      "v_mfma_f32_16x16x32_bf16 v[144:147], a[188:191], a[156:159], v[144:147]\n"
      "ds_read_b64_tr_b16 a[170:171], v200 offset:33280\n"
      "v_cvt_pk_bf16_f32 v169, v122, v123\n"
      "v_mfma_f32_16x16x32_bf16 v[148:151], a[184:187], a[148:151], v[148:151]\n"
      "ds_read_b64_tr_b16 a[172:173], v200 offset:36864\n"
      "v_cvt_pk_bf16_f32 v172, v124, v125\n"
      "v_mfma_f32_16x16x32_bf16 v[148:151], a[188:191], a[160:163], v[148:151]\n"
      "ds_read_b64_tr_b16 a[174:175], v200 offset:37376\n"
      "v_cvt_pk_bf16_f32 v173, v126, v127\n"
      "v_mfma_f32_16x16x32_bf16 v[152:155], a[184:187], a[152:155], v[152:155]\n"
      "ds_read_b64_tr_b16 a[176:177], v201 offset:32768\n"
      "v_cvt_pk_bf16_f32 v176, v128, v129\n"
      "s_add_u32 s88, s67, s72\n"
      "s_add_u32 m0, s85, 0x10000\n"
      "v_mfma_f32_16x16x32_bf16 v[152:155], a[188:191], a[164:167], v[152:155]\n"
      "ds_read_b64_tr_b16 a[178:179], v201 offset:33280\n"
      "v_cvt_pk_bf16_f32 v177, v130, v131\n"
      "buffer_load_dwordx4 v192, s[20:23], s88 offen lds\n"
      "s_waitcnt lgkmcnt(15)\n"
      "v_mfma_f32_16x16x32_bf16 v[156:159], a[192:195], a[144:147], v[156:159]\n"
      "ds_read_b64_tr_b16 a[180:181], v201 offset:36864\n"
      "v_cvt_pk_bf16_f32 v170, v132, v133\n"
      "v_mfma_f32_16x16x32_bf16 v[156:159], a[196:199], a[156:159], v[156:159]\n"
      "ds_read_b64_tr_b16 a[182:183], v201 offset:37376\n"
      "v_cvt_pk_bf16_f32 v171, v134, v135\n"
      "buffer_load_dwordx4 v193, s[20:23], s88 offen offset:1024 lds\n"
      "v_mfma_f32_16x16x32_bf16 v[160:163], a[192:195], a[148:151], v[160:163]\n"
      "ds_read_b64_tr_b16 a[184:185], v202 offset:32768\n"
      "v_cvt_pk_bf16_f32 v174, v136, v137\n"
      "v_mfma_f32_16x16x32_bf16 v[160:163], a[196:199], a[160:163], v[160:163]\n"
      "ds_read_b64_tr_b16 a[186:187], v202 offset:33280\n"
      "v_cvt_pk_bf16_f32 v175, v138, v139\n"
      "buffer_load_dwordx4 v194, s[20:23], s88 offen offset:2048 lds\n"
      "v_mfma_f32_16x16x32_bf16 v[164:167], a[192:195], a[152:155], v[164:167]\n"
      "ds_read_b64_tr_b16 a[188:189], v202 offset:36864\n"
      "v_cvt_pk_bf16_f32 v178, v140, v141\n"
      "v_mfma_f32_16x16x32_bf16 v[164:167], a[196:199], a[164:167], v[164:167]\n"
      "ds_read_b64_tr_b16 a[190:191], v202 offset:37376\n"
      "v_cvt_pk_bf16_f32 v179, v142, v143\n"
      "buffer_load_dwordx4 v195, s[20:23], s88 offen offset:3072 lds\n"
      "s_waitcnt lgkmcnt(10)\n"
      "v_mfma_f32_16x16x32_bf16 v[120:123], a[168:171], a[0:3], 0\n"
      "ds_read_b64_tr_b16 a[192:193], v203 offset:32768\n"
      "v_cvt_pk_bf16_f32 v180, v144, v145\n"
      "ds_read_b64 a[224:225], v26 offset:24576\n"
      "s_waitcnt lgkmcnt(10)\n"
      "v_mfma_f32_16x16x32_bf16 v[120:123], a[172:175], a[12:15], v[120:123]\n"
      "ds_read_b64_tr_b16 a[194:195], v203 offset:33280\n"
      "v_cvt_pk_bf16_f32 v181, v146, v147\n"
      "ds_read_b64 a[226:227], v27 offset:24576\n"
      "v_mfma_f32_16x16x32_bf16 v[124:127], a[168:171], a[4:7], 0\n"
      "ds_read_b64_tr_b16 a[196:197], v203 offset:36864\n"
      "v_cvt_pk_bf16_f32 v184, v148, v149\n"
      "ds_read_b64 a[228:229], v26 offset:26624\n"
      "v_mfma_f32_16x16x32_bf16 v[124:127], a[172:175], a[16:19], v[124:127]\n"
      "ds_read_b64_tr_b16 a[198:199], v203 offset:37376\n"
      "v_cvt_pk_bf16_f32 v185, v150, v151\n"
      "ds_read_b64 a[230:231], v27 offset:26624\n"
      "s_add_u32 s88, s68, s72\n"
      "s_add_u32 m0, s85, 0x20000\n"
      "v_mfma_f32_16x16x32_bf16 v[128:131], a[168:171], a[8:11], 0\n"
      "v_cvt_pk_bf16_f32 v188, v152, v153\n"
      "ds_read_b64 a[232:233], v26 offset:32768\n"
      "buffer_load_dwordx4 v192, s[20:23], s88 offen lds\n"
      "v_mfma_f32_16x16x32_bf16 v[128:131], a[172:175], a[20:23], v[128:131]\n"
      "v_cvt_pk_bf16_f32 v189, v154, v155\n"
      "ds_read_b64 a[234:235], v27 offset:32768\n"
      "s_waitcnt lgkmcnt(15)\n"
      "v_mfma_f32_16x16x32_bf16 v[132:135], a[176:179], a[0:3], 0\n"
      "v_cvt_pk_bf16_f32 v182, v156, v157\n"
      "ds_read_b64 a[236:237], v26 offset:34816\n"
      "buffer_load_dwordx4 v193, s[20:23], s88 offen offset:1024 lds\n"
      "s_waitcnt lgkmcnt(15)\n"
      "v_mfma_f32_16x16x32_bf16 v[132:135], a[180:183], a[12:15], v[132:135]\n"
      "v_cvt_pk_bf16_f32 v183, v158, v159\n"
      "ds_read_b64 a[238:239], v27 offset:34816\n"
      "v_mfma_f32_16x16x32_bf16 v[136:139], a[176:179], a[4:7], 0\n"
      "v_cvt_pk_bf16_f32 v186, v160, v161\n"
      "ds_read_b64 a[240:241], v26 offset:40960\n"
      "buffer_load_dwordx4 v194, s[20:23], s88 offen offset:2048 lds\n"
      "v_mfma_f32_16x16x32_bf16 v[136:139], a[180:183], a[16:19], v[136:139]\n"
      "v_cvt_pk_bf16_f32 v187, v162, v163\n"
      "ds_read_b64 a[242:243], v27 offset:40960\n"
      "v_mfma_f32_16x16x32_bf16 v[140:143], a[176:179], a[8:11], 0\n"
      "v_cvt_pk_bf16_f32 v190, v164, v165\n"
      "ds_read_b64 a[244:245], v26 offset:43008\n"
      "buffer_load_dwordx4 v195, s[20:23], s88 offen offset:3072 lds\n"
      "v_mfma_f32_16x16x32_bf16 v[140:143], a[180:183], a[20:23], v[140:143]\n"
      "v_cvt_pk_bf16_f32 v191, v166, v167\n"
      "ds_read_b64 a[246:247], v27 offset:43008\n"
      "s_waitcnt vmcnt(16) lgkmcnt(0)\n"
      "s_barrier\n"
      "v_mfma_f32_16x16x32_bf16 v[144:147], a[184:187], a[0:3], 0\n"
      "ds_read_b64_tr_b16 a[168:169], v14 offset:49152\n"
      "v_mfma_f32_16x16x32_bf16 v[144:147], a[188:191], a[12:15], v[144:147]\n"
      "ds_read_b64_tr_b16 a[170:171], v14 offset:49664\n"
      "v_mfma_f32_16x16x32_bf16 v[148:151], a[184:187], a[4:7], 0\n"
      "ds_read_b64_tr_b16 a[172:173], v14 offset:53248\n"
      "v_mfma_f32_16x16x32_bf16 v[148:151], a[188:191], a[16:19], v[148:151]\n"
      "ds_read_b64_tr_b16 a[174:175], v14 offset:53760\n"
      "v_mfma_f32_16x16x32_bf16 v[152:155], a[184:187], a[8:11], 0\n"
      "ds_read_b64_tr_b16 a[176:177], v15 offset:49152\n"
      "v_mfma_f32_16x16x32_bf16 v[152:155], a[188:191], a[20:23], v[152:155]\n"
      "ds_read_b64_tr_b16 a[178:179], v15 offset:49664\n"
      "v_mfma_f32_16x16x32_bf16 v[156:159], a[192:195], a[0:3], 0\n"
      "ds_read_b64_tr_b16 a[180:181], v15 offset:53248\n"
      "v_mfma_f32_16x16x32_bf16 v[156:159], a[196:199], a[12:15], v[156:159]\n"
      "ds_read_b64_tr_b16 a[182:183], v15 offset:53760\n"
      "v_mfma_f32_16x16x32_bf16 v[160:163], a[192:195], a[4:7], 0\n"
      "ds_read_b64_tr_b16 a[184:185], v16 offset:49152\n"
      "v_mfma_f32_16x16x32_bf16 v[160:163], a[196:199], a[16:19], v[160:163]\n"
      "ds_read_b64_tr_b16 a[186:187], v16 offset:49664\n"
      "v_mfma_f32_16x16x32_bf16 v[164:167], a[192:195], a[8:11], 0\n"
      "ds_read_b64_tr_b16 a[188:189], v16 offset:53248\n"
      "v_mfma_f32_16x16x32_bf16 v[164:167], a[196:199], a[20:23], v[164:167]\n"
      "ds_read_b64_tr_b16 a[190:191], v16 offset:53760\n"
      "v_mfma_f32_16x16x32_bf16 v[48:51], a[200:203], v[168:171], v[48:51]\n"
      "ds_read_b64_tr_b16 a[192:193], v17 offset:49152\n"
      "v_mfma_f32_16x16x32_bf16 v[48:51], a[224:227], v[180:183], v[48:51]\n"
      "ds_read_b64_tr_b16 a[194:195], v17 offset:49664\n"
      "v_mfma_f32_16x16x32_bf16 v[52:55], a[200:203], v[172:175], v[52:55]\n"
      "ds_read_b64_tr_b16 a[196:197], v17 offset:53248\n"
      "v_mfma_f32_16x16x32_bf16 v[52:55], a[224:227], v[184:187], v[52:55]\n"
      "ds_read_b64_tr_b16 a[198:199], v17 offset:53760\n"
      "s_add_u32 s88, s69, s72\n"
      "s_add_u32 m0, s85, 0x14000\n"
      "v_mfma_f32_16x16x32_bf16 v[56:59], a[200:203], v[176:179], v[56:59]\n"
      "buffer_load_dwordx4 v192, s[20:23], s88 offen lds\n"
      "v_mfma_f32_16x16x32_bf16 v[56:59], a[224:227], v[188:191], v[56:59]\n"
      "ds_read_b64 a[200:201], v24\n"
      "v_mfma_f32_16x16x32_bf16 v[60:63], a[204:207], v[168:171], v[60:63]\n"
      "ds_read_b64 a[202:203], v25\n"
      "v_mfma_f32_16x16x32_bf16 v[60:63], a[228:231], v[180:183], v[60:63]\n"
      "ds_read_b64 a[224:225], v26\n"
      "v_mfma_f32_16x16x32_bf16 v[64:67], a[204:207], v[172:175], v[64:67]\n"
      "ds_read_b64 a[226:227], v27\n"
      "v_mfma_f32_16x16x32_bf16 v[64:67], a[228:231], v[184:187], v[64:67]\n"
      "v_mfma_f32_16x16x32_bf16 v[68:71], a[204:207], v[176:179], v[68:71]\n"
      "buffer_load_dwordx4 v193, s[20:23], s88 offen offset:1024 lds\n"
      "v_mfma_f32_16x16x32_bf16 v[68:71], a[228:231], v[188:191], v[68:71]\n"
      "ds_read_b64 a[204:205], v24 offset:2048\n"
      "v_mfma_f32_16x16x32_bf16 v[72:75], a[208:211], v[168:171], v[72:75]\n"
      "ds_read_b64 a[206:207], v25 offset:2048\n"
      "v_mfma_f32_16x16x32_bf16 v[72:75], a[232:235], v[180:183], v[72:75]\n"
      "ds_read_b64 a[228:229], v26 offset:2048\n"
      "v_mfma_f32_16x16x32_bf16 v[76:79], a[208:211], v[172:175], v[76:79]\n"
      "ds_read_b64 a[230:231], v27 offset:2048\n"
      "v_mfma_f32_16x16x32_bf16 v[76:79], a[232:235], v[184:187], v[76:79]\n"
      "v_mfma_f32_16x16x32_bf16 v[80:83], a[208:211], v[176:179], v[80:83]\n"
      "buffer_load_dwordx4 v194, s[20:23], s88 offen offset:2048 lds\n"
      "v_mfma_f32_16x16x32_bf16 v[80:83], a[232:235], v[188:191], v[80:83]\n"
      "ds_read_b64 a[208:209], v24 offset:8192\n"
      "v_mfma_f32_16x16x32_bf16 v[84:87], a[212:215], v[168:171], v[84:87]\n"
      "ds_read_b64 a[210:211], v25 offset:8192\n"
      "v_mfma_f32_16x16x32_bf16 v[84:87], a[236:239], v[180:183], v[84:87]\n"
      "ds_read_b64 a[232:233], v26 offset:8192\n"
      "v_mfma_f32_16x16x32_bf16 v[88:91], a[212:215], v[172:175], v[88:91]\n"
      "ds_read_b64 a[234:235], v27 offset:8192\n"
      "v_mfma_f32_16x16x32_bf16 v[88:91], a[236:239], v[184:187], v[88:91]\n"
      "v_mfma_f32_16x16x32_bf16 v[92:95], a[212:215], v[176:179], v[92:95]\n"
      "buffer_load_dwordx4 v195, s[20:23], s88 offen offset:3072 lds\n"
      "v_mfma_f32_16x16x32_bf16 v[92:95], a[236:239], v[188:191], v[92:95]\n"
      "ds_read_b64 a[212:213], v24 offset:10240\n"
      "v_mfma_f32_16x16x32_bf16 v[96:99], a[216:219], v[168:171], v[96:99]\n"
      "ds_read_b64 a[214:215], v25 offset:10240\n"
      "v_mfma_f32_16x16x32_bf16 v[96:99], a[240:243], v[180:183], v[96:99]\n"
      "ds_read_b64 a[236:237], v26 offset:10240\n"
      "v_mfma_f32_16x16x32_bf16 v[100:103], a[216:219], v[172:175], v[100:103]\n"
      "ds_read_b64 a[238:239], v27 offset:10240\n"
      "v_mfma_f32_16x16x32_bf16 v[100:103], a[240:243], v[184:187], v[100:103]\n"
      "s_add_u32 s88, s63, s73\n"
      "s_add_u32 m0, s85, 0x24000\n"
      "v_mfma_f32_16x16x32_bf16 v[104:107], a[216:219], v[176:179], v[104:107]\n"
      "buffer_load_dwordx4 v192, s[20:23], s88 offen lds\n"
      "v_mfma_f32_16x16x32_bf16 v[104:107], a[240:243], v[188:191], v[104:107]\n"
      "ds_read_b64 a[216:217], v24 offset:16384\n"
      "v_mfma_f32_16x16x32_bf16 v[108:111], a[220:223], v[168:171], v[108:111]\n"
      "ds_read_b64 a[218:219], v25 offset:16384\n"
      "buffer_load_dwordx4 v193, s[20:23], s88 offen offset:1024 lds\n"
      "v_mfma_f32_16x16x32_bf16 v[108:111], a[244:247], v[180:183], v[108:111]\n"
      "ds_read_b64 a[240:241], v26 offset:16384\n"
      "v_mfma_f32_16x16x32_bf16 v[112:115], a[220:223], v[172:175], v[112:115]\n"
      "ds_read_b64 a[242:243], v27 offset:16384\n"
      "buffer_load_dwordx4 v194, s[20:23], s88 offen offset:2048 lds\n"
      "v_mfma_f32_16x16x32_bf16 v[112:115], a[244:247], v[184:187], v[112:115]\n"
      "v_mfma_f32_16x16x32_bf16 v[116:119], a[220:223], v[176:179], v[116:119]\n"
      "buffer_load_dwordx4 v195, s[20:23], s88 offen offset:3072 lds\n"
      "v_mfma_f32_16x16x32_bf16 v[116:119], a[244:247], v[188:191], v[116:119]\n"
      "ds_read_b64 a[220:221], v24 offset:18432\n"
      "s_add_u32 s89, s73, 0\n"
      "s_add_u32 m0, s86, 0x6000\n"
      "s_waitcnt lgkmcnt(15)\n"
      "v_mfma_f32_16x16x32_bf16 v[120:123], a[168:171], a[24:27], v[120:123]\n"
      "ds_read_b64 a[222:223], v25 offset:18432\n"
      "buffer_load_dwordx4 v204, s[12:15], s89 offen nt lds\n"
      "v_mfma_f32_16x16x32_bf16 v[120:123], a[172:175], a[36:39], v[120:123]\n"
      "ds_read_b64 a[244:245], v26 offset:18432\n"
      "v_mfma_f32_16x16x32_bf16 v[124:127], a[168:171], a[28:31], v[124:127]\n"
      "ds_read_b64 a[246:247], v27 offset:18432\n"
      "buffer_load_dwordx4 v205, s[12:15], s89 offen offset:1024 nt lds\n"
      "v_mfma_f32_16x16x32_bf16 v[124:127], a[172:175], a[40:43], v[124:127]\n"
      "s_add_u32 s89, s73, 0x4000\n"
      "s_add_u32 m0, s86, 0x8000\n"
      "v_mfma_f32_16x16x32_bf16 v[128:131], a[168:171], a[32:35], v[128:131]\n"
      "buffer_load_dwordx4 v204, s[12:15], s89 offen nt lds\n"
      "v_mfma_f32_16x16x32_bf16 v[128:131], a[172:175], a[44:47], v[128:131]\n"
      "v_mfma_f32_16x16x32_bf16 v[132:135], a[176:179], a[24:27], v[132:135]\n"
      "buffer_load_dwordx4 v205, s[12:15], s89 offen offset:1024 nt lds\n"
      "v_mfma_f32_16x16x32_bf16 v[132:135], a[180:183], a[36:39], v[132:135]\n"
      "s_add_u32 s89, s73, 0x8000\n"
      "s_add_u32 m0, s86, 0xa000\n"
      "v_mfma_f32_16x16x32_bf16 v[136:139], a[176:179], a[28:31], v[136:139]\n"
      "buffer_load_dwordx4 v204, s[12:15], s89 offen nt lds\n"
      "v_mfma_f32_16x16x32_bf16 v[136:139], a[180:183], a[40:43], v[136:139]\n"
      "v_mfma_f32_16x16x32_bf16 v[140:143], a[176:179], a[32:35], v[140:143]\n"
      "buffer_load_dwordx4 v205, s[12:15], s89 offen offset:1024 nt lds\n"
      "v_mfma_f32_16x16x32_bf16 v[140:143], a[180:183], a[44:47], v[140:143]\n"
      "s_waitcnt vmcnt(26) lgkmcnt(15)\n"
      "s_barrier\n"
      "v_mfma_f32_16x16x32_bf16 v[144:147], a[184:187], a[24:27], v[144:147]\n"
      "ds_read_b64_tr_b16 a[168:169], v14\n"
      "v_mfma_f32_16x16x32_bf16 v[144:147], a[188:191], a[36:39], v[144:147]\n"
      "ds_read_b64_tr_b16 a[170:171], v14 offset:512\n"
      "v_mfma_f32_16x16x32_bf16 v[148:151], a[184:187], a[28:31], v[148:151]\n"
      "ds_read_b64_tr_b16 a[172:173], v14 offset:4096\n"
      "v_mfma_f32_16x16x32_bf16 v[148:151], a[188:191], a[40:43], v[148:151]\n"
      "ds_read_b64_tr_b16 a[174:175], v14 offset:4608\n"
      "v_mfma_f32_16x16x32_bf16 v[152:155], a[184:187], a[32:35], v[152:155]\n"
      "ds_read_b64_tr_b16 a[176:177], v15\n"
      "v_mfma_f32_16x16x32_bf16 v[152:155], a[188:191], a[44:47], v[152:155]\n"
      "ds_read_b64_tr_b16 a[178:179], v15 offset:512\n"
      "v_mfma_f32_16x16x32_bf16 v[156:159], a[192:195], a[24:27], v[156:159]\n"
      "ds_read_b64_tr_b16 a[180:181], v15 offset:4096\n"
      "v_mfma_f32_16x16x32_bf16 v[156:159], a[196:199], a[36:39], v[156:159]\n"
      "ds_read_b64_tr_b16 a[182:183], v15 offset:4608\n"
      "v_mfma_f32_16x16x32_bf16 v[160:163], a[192:195], a[28:31], v[160:163]\n"
      "ds_read_b64_tr_b16 a[184:185], v16\n"
      "v_mfma_f32_16x16x32_bf16 v[160:163], a[196:199], a[40:43], v[160:163]\n"
      "ds_read_b64_tr_b16 a[186:187], v16 offset:512\n"
      "v_mfma_f32_16x16x32_bf16 v[164:167], a[192:195], a[32:35], v[164:167]\n"
      "ds_read_b64_tr_b16 a[188:189], v16 offset:4096\n"
      "v_mfma_f32_16x16x32_bf16 v[164:167], a[196:199], a[44:47], v[164:167]\n"
      "ds_read_b64_tr_b16 a[190:191], v16 offset:4608\n"
      "s_waitcnt lgkmcnt(10)\n"
      "v_mfma_f32_16x16x32_bf16 v[120:123], a[168:171], a[48:51], v[120:123]\n"
      "ds_read_b64_tr_b16 a[192:193], v17\n"
      "s_waitcnt lgkmcnt(9)\n"
      "v_mfma_f32_16x16x32_bf16 v[120:123], a[172:175], a[60:63], v[120:123]\n"
      "ds_read_b64_tr_b16 a[194:195], v17 offset:512\n"
      "v_mfma_f32_16x16x32_bf16 v[124:127], a[168:171], a[52:55], v[124:127]\n"
      "ds_read_b64_tr_b16 a[196:197], v17 offset:4096\n"
      "v_mfma_f32_16x16x32_bf16 v[124:127], a[172:175], a[64:67], v[124:127]\n"
      "ds_read_b64_tr_b16 a[198:199], v17 offset:4608\n"
      "v_mfma_f32_16x16x32_bf16 v[128:131], a[168:171], a[56:59], v[128:131]\n"
      "v_mfma_f32_16x16x32_bf16 v[128:131], a[172:175], a[68:71], v[128:131]\n"
      "s_waitcnt lgkmcnt(10)\n"
      "v_mfma_f32_16x16x32_bf16 v[132:135], a[176:179], a[48:51], v[132:135]\n"
      "s_waitcnt lgkmcnt(8)\n"
      "v_mfma_f32_16x16x32_bf16 v[132:135], a[180:183], a[60:63], v[132:135]\n"
      "s_add_u32 s89, s70, 0\n"
      "s_add_u32 m0, s86, 0\n"
      "v_mfma_f32_16x16x32_bf16 v[136:139], a[176:179], a[52:55], v[136:139]\n"
      "buffer_load_dwordx4 v216, s[16:19], s89 offen nt lds\n"
      "v_mfma_f32_16x16x32_bf16 v[136:139], a[180:183], a[64:67], v[136:139]\n"
      "v_mfma_f32_16x16x32_bf16 v[140:143], a[176:179], a[56:59], v[140:143]\n"
      "buffer_load_dwordx4 v217, s[16:19], s89 offen offset:1024 nt lds\n"
      "v_mfma_f32_16x16x32_bf16 v[140:143], a[180:183], a[68:71], v[140:143]\n"
      "s_waitcnt vmcnt(24)\n"
      "s_barrier\n"
      "s_waitcnt lgkmcnt(6)\n"
      "v_mfma_f32_16x16x32_bf16 v[144:147], a[184:187], a[48:51], v[144:147]\n"
      "ds_read_b64_tr_b16 a[168:169], v200\n"
      "s_waitcnt lgkmcnt(5)\n"
      "v_mfma_f32_16x16x32_bf16 v[144:147], a[188:191], a[60:63], v[144:147]\n"
      "ds_read_b64_tr_b16 a[170:171], v200 offset:512\n"
      "v_mfma_f32_16x16x32_bf16 v[148:151], a[184:187], a[52:55], v[148:151]\n"
      "ds_read_b64_tr_b16 a[172:173], v200 offset:4096\n"
      "v_mfma_f32_16x16x32_bf16 v[148:151], a[188:191], a[64:67], v[148:151]\n"
      "ds_read_b64_tr_b16 a[174:175], v200 offset:4608\n"
      "v_mfma_f32_16x16x32_bf16 v[152:155], a[184:187], a[56:59], v[152:155]\n"
      "ds_read_b64_tr_b16 a[176:177], v201\n"
      "v_mfma_f32_16x16x32_bf16 v[152:155], a[188:191], a[68:71], v[152:155]\n"
      "ds_read_b64_tr_b16 a[178:179], v201 offset:512\n"
      "s_waitcnt lgkmcnt(8)\n"
      "v_mfma_f32_16x16x32_bf16 v[156:159], a[192:195], a[48:51], v[156:159]\n"
      "ds_read_b64_tr_b16 a[180:181], v201 offset:4096\n"
      "s_waitcnt lgkmcnt(7)\n"
      "v_mfma_f32_16x16x32_bf16 v[156:159], a[196:199], a[60:63], v[156:159]\n"
      "ds_read_b64_tr_b16 a[182:183], v201 offset:4608\n"
      "v_mfma_f32_16x16x32_bf16 v[160:163], a[192:195], a[52:55], v[160:163]\n"
      "ds_read_b64_tr_b16 a[184:185], v202\n"
      "v_mfma_f32_16x16x32_bf16 v[160:163], a[196:199], a[64:67], v[160:163]\n"
      "ds_read_b64_tr_b16 a[186:187], v202 offset:512\n"
      "v_mfma_f32_16x16x32_bf16 v[164:167], a[192:195], a[56:59], v[164:167]\n"
      "ds_read_b64_tr_b16 a[188:189], v202 offset:4096\n"
      "v_mfma_f32_16x16x32_bf16 v[164:167], a[196:199], a[68:71], v[164:167]\n"
      "ds_read_b64_tr_b16 a[190:191], v202 offset:4608\n"
      "s_waitcnt lgkmcnt(10)\n"
      "v_mfma_f32_16x16x32_bf16 v[120:123], a[168:171], a[72:75], v[120:123]\n"
      "ds_read_b64_tr_b16 a[192:193], v203\n"
      "s_waitcnt lgkmcnt(9)\n"
      "v_mfma_f32_16x16x32_bf16 v[120:123], a[172:175], a[84:87], v[120:123]\n"
      "ds_read_b64_tr_b16 a[194:195], v203 offset:512\n"
      "v_mfma_f32_16x16x32_bf16 v[124:127], a[168:171], a[76:79], v[124:127]\n"
      "ds_read_b64_tr_b16 a[196:197], v203 offset:4096\n"
      "v_mfma_f32_16x16x32_bf16 v[124:127], a[172:175], a[88:91], v[124:127]\n"
      "ds_read_b64_tr_b16 a[198:199], v203 offset:4608\n"
      "v_mfma_f32_16x16x32_bf16 v[128:131], a[168:171], a[80:83], v[128:131]\n"
      "v_mfma_f32_16x16x32_bf16 v[128:131], a[172:175], a[92:95], v[128:131]\n"
      "s_waitcnt lgkmcnt(10)\n"
      "v_mfma_f32_16x16x32_bf16 v[132:135], a[176:179], a[72:75], v[132:135]\n"
      "s_waitcnt lgkmcnt(8)\n"
      "v_mfma_f32_16x16x32_bf16 v[132:135], a[180:183], a[84:87], v[132:135]\n"
      "s_add_u32 s89, s70, 0x4000\n"
      "s_add_u32 m0, s86, 0x2000\n"
      "v_mfma_f32_16x16x32_bf16 v[136:139], a[176:179], a[76:79], v[136:139]\n"
      "buffer_load_dwordx4 v216, s[16:19], s89 offen nt lds\n"
      "v_mfma_f32_16x16x32_bf16 v[136:139], a[180:183], a[88:91], v[136:139]\n"
      "v_mfma_f32_16x16x32_bf16 v[140:143], a[176:179], a[80:83], v[140:143]\n"
      "buffer_load_dwordx4 v217, s[16:19], s89 offen offset:1024 nt lds\n"
      "v_mfma_f32_16x16x32_bf16 v[140:143], a[180:183], a[92:95], v[140:143]\n"
      "s_waitcnt vmcnt(22)\n"
      "s_barrier\n"
      "s_waitcnt lgkmcnt(6)\n"
      "v_mfma_f32_16x16x32_bf16 v[144:147], a[184:187], a[72:75], v[144:147]\n"
      "ds_read_b64_tr_b16 a[168:169], v14 offset:16384\n"
      "s_waitcnt lgkmcnt(5)\n"
      "v_mfma_f32_16x16x32_bf16 v[144:147], a[188:191], a[84:87], v[144:147]\n"
      "ds_read_b64_tr_b16 a[170:171], v14 offset:16896\n"
      "v_mfma_f32_16x16x32_bf16 v[148:151], a[184:187], a[76:79], v[148:151]\n"
      "ds_read_b64_tr_b16 a[172:173], v14 offset:20480\n"
      "v_mfma_f32_16x16x32_bf16 v[148:151], a[188:191], a[88:91], v[148:151]\n"
      "ds_read_b64_tr_b16 a[174:175], v14 offset:20992\n"
      "v_mfma_f32_16x16x32_bf16 v[152:155], a[184:187], a[80:83], v[152:155]\n"
      "ds_read_b64_tr_b16 a[176:177], v15 offset:16384\n"
      "v_mfma_f32_16x16x32_bf16 v[152:155], a[188:191], a[92:95], v[152:155]\n"
      "ds_read_b64_tr_b16 a[178:179], v15 offset:16896\n"
      "s_waitcnt lgkmcnt(8)\n"
      "v_mfma_f32_16x16x32_bf16 v[156:159], a[192:195], a[72:75], v[156:159]\n"
      "ds_read_b64_tr_b16 a[180:181], v15 offset:20480\n"
      "s_waitcnt lgkmcnt(7)\n"
      "v_mfma_f32_16x16x32_bf16 v[156:159], a[196:199], a[84:87], v[156:159]\n"
      "ds_read_b64_tr_b16 a[182:183], v15 offset:20992\n"
      "v_mfma_f32_16x16x32_bf16 v[160:163], a[192:195], a[76:79], v[160:163]\n"
      "ds_read_b64_tr_b16 a[184:185], v16 offset:16384\n"
      "v_mfma_f32_16x16x32_bf16 v[160:163], a[196:199], a[88:91], v[160:163]\n"
      "ds_read_b64_tr_b16 a[186:187], v16 offset:16896\n"
      "v_mfma_f32_16x16x32_bf16 v[164:167], a[192:195], a[80:83], v[164:167]\n"
      "ds_read_b64_tr_b16 a[188:189], v16 offset:20480\n"
      "v_mfma_f32_16x16x32_bf16 v[164:167], a[196:199], a[92:95], v[164:167]\n"
      "ds_read_b64_tr_b16 a[190:191], v16 offset:20992\n"
      "s_waitcnt lgkmcnt(10)\n"
      "v_mfma_f32_16x16x32_bf16 v[120:123], a[168:171], a[96:99], v[120:123]\n"
      "ds_read_b64_tr_b16 a[192:193], v17 offset:16384\n"
      "s_waitcnt lgkmcnt(9)\n"
      "v_mfma_f32_16x16x32_bf16 v[120:123], a[172:175], a[108:111], v[120:123]\n"
      "ds_read_b64_tr_b16 a[194:195], v17 offset:16896\n"
      "v_mfma_f32_16x16x32_bf16 v[124:127], a[168:171], a[100:103], v[124:127]\n"
      "ds_read_b64_tr_b16 a[196:197], v17 offset:20480\n"
      "v_mfma_f32_16x16x32_bf16 v[124:127], a[172:175], a[112:115], v[124:127]\n"
      "ds_read_b64_tr_b16 a[198:199], v17 offset:20992\n"
      "v_mfma_f32_16x16x32_bf16 v[128:131], a[168:171], a[104:107], v[128:131]\n"
      "v_mfma_f32_16x16x32_bf16 v[128:131], a[172:175], a[116:119], v[128:131]\n"
      "s_waitcnt lgkmcnt(10)\n"
      "v_mfma_f32_16x16x32_bf16 v[132:135], a[176:179], a[96:99], v[132:135]\n"
      "s_waitcnt lgkmcnt(8)\n"
      "v_mfma_f32_16x16x32_bf16 v[132:135], a[180:183], a[108:111], v[132:135]\n"
      "s_add_u32 s89, s70, 0x8000\n"
      "s_add_u32 m0, s86, 0x4000\n"
      "v_mfma_f32_16x16x32_bf16 v[136:139], a[176:179], a[100:103], v[136:139]\n"
      "buffer_load_dwordx4 v216, s[16:19], s89 offen nt lds\n"
      "v_mfma_f32_16x16x32_bf16 v[136:139], a[180:183], a[112:115], v[136:139]\n"
      "v_mfma_f32_16x16x32_bf16 v[140:143], a[176:179], a[104:107], v[140:143]\n"
      "buffer_load_dwordx4 v217, s[16:19], s89 offen offset:1024 nt lds\n"
      "v_mfma_f32_16x16x32_bf16 v[140:143], a[180:183], a[116:119], v[140:143]\n"
      "s_waitcnt vmcnt(20)\n"
      "s_barrier\n"
      "s_waitcnt lgkmcnt(6)\n"
      "v_mfma_f32_16x16x32_bf16 v[144:147], a[184:187], a[96:99], v[144:147]\n"
      "ds_read_b64_tr_b16 a[168:169], v200 offset:16384\n"
      "s_waitcnt lgkmcnt(5)\n"
      "v_mfma_f32_16x16x32_bf16 v[144:147], a[188:191], a[108:111], v[144:147]\n"
      "ds_read_b64_tr_b16 a[170:171], v200 offset:16896\n"
      "v_mfma_f32_16x16x32_bf16 v[148:151], a[184:187], a[100:103], v[148:151]\n"
      "ds_read_b64_tr_b16 a[172:173], v200 offset:20480\n"
      "v_mfma_f32_16x16x32_bf16 v[148:151], a[188:191], a[112:115], v[148:151]\n"
      "ds_read_b64_tr_b16 a[174:175], v200 offset:20992\n"
      "v_mfma_f32_16x16x32_bf16 v[152:155], a[184:187], a[104:107], v[152:155]\n"
      "ds_read_b64_tr_b16 a[176:177], v201 offset:16384\n"
      "v_mfma_f32_16x16x32_bf16 v[152:155], a[188:191], a[116:119], v[152:155]\n"
      "ds_read_b64_tr_b16 a[178:179], v201 offset:16896\n"
      "s_waitcnt lgkmcnt(8)\n"
      "v_mfma_f32_16x16x32_bf16 v[156:159], a[192:195], a[96:99], v[156:159]\n"
      "ds_read_b64_tr_b16 a[180:181], v201 offset:20480\n"
      "s_waitcnt lgkmcnt(7)\n"
      "v_mfma_f32_16x16x32_bf16 v[156:159], a[196:199], a[108:111], v[156:159]\n"
      "ds_read_b64_tr_b16 a[182:183], v201 offset:20992\n"
      "v_mfma_f32_16x16x32_bf16 v[160:163], a[192:195], a[100:103], v[160:163]\n"
      "ds_read_b64_tr_b16 a[184:185], v202 offset:16384\n"
      "v_mfma_f32_16x16x32_bf16 v[160:163], a[196:199], a[112:115], v[160:163]\n"
      "ds_read_b64_tr_b16 a[186:187], v202 offset:16896\n"
      "v_mfma_f32_16x16x32_bf16 v[164:167], a[192:195], a[104:107], v[164:167]\n"
      "ds_read_b64_tr_b16 a[188:189], v202 offset:20480\n"
      "v_mfma_f32_16x16x32_bf16 v[164:167], a[196:199], a[116:119], v[164:167]\n"
      "ds_read_b64_tr_b16 a[190:191], v202 offset:20992\n"
      "s_waitcnt lgkmcnt(10)\n"
      "v_mfma_f32_16x16x32_bf16 v[120:123], a[168:171], a[120:123], v[120:123]\n"
      "ds_read_b64_tr_b16 a[192:193], v203 offset:16384\n"
      "s_waitcnt lgkmcnt(9)\n"
      "v_mfma_f32_16x16x32_bf16 v[120:123], a[172:175], a[132:135], v[120:123]\n"
      "ds_read_b64_tr_b16 a[194:195], v203 offset:16896\n"
      "v_mfma_f32_16x16x32_bf16 v[124:127], a[168:171], a[124:127], v[124:127]\n"
      "ds_read_b64_tr_b16 a[196:197], v203 offset:20480\n"
      "v_mfma_f32_16x16x32_bf16 v[124:127], a[172:175], a[136:139], v[124:127]\n"
      "ds_read_b64_tr_b16 a[198:199], v203 offset:20992\n"
      "s_add_u32 s88, s64, s73\n"
      "s_add_u32 m0, s85, 0x18000\n"
      "v_mfma_f32_16x16x32_bf16 v[128:131], a[168:171], a[128:131], v[128:131]\n"
      "buffer_load_dwordx4 v192, s[20:23], s88 offen lds\n"
      "v_mfma_f32_16x16x32_bf16 v[128:131], a[172:175], a[140:143], v[128:131]\n"
      "s_waitcnt lgkmcnt(10)\n"
      "v_mfma_f32_16x16x32_bf16 v[132:135], a[176:179], a[120:123], v[132:135]\n"
      "buffer_load_dwordx4 v193, s[20:23], s88 offen offset:1024 lds\n"
      "s_waitcnt lgkmcnt(8)\n"
      "v_mfma_f32_16x16x32_bf16 v[132:135], a[180:183], a[132:135], v[132:135]\n"
      "v_mfma_f32_16x16x32_bf16 v[136:139], a[176:179], a[124:127], v[136:139]\n"
      "buffer_load_dwordx4 v194, s[20:23], s88 offen offset:2048 lds\n"
      "v_mfma_f32_16x16x32_bf16 v[136:139], a[180:183], a[136:139], v[136:139]\n"
      "v_mfma_f32_16x16x32_bf16 v[140:143], a[176:179], a[128:131], v[140:143]\n"
      "buffer_load_dwordx4 v195, s[20:23], s88 offen offset:3072 lds\n"
      "v_mfma_f32_16x16x32_bf16 v[140:143], a[180:183], a[140:143], v[140:143]\n"
      "s_waitcnt vmcnt(20)\n"
      "s_barrier\n"
      "s_waitcnt lgkmcnt(6)\n"
      "v_mfma_f32_16x16x32_bf16 v[144:147], a[184:187], a[120:123], v[144:147]\n"
      "ds_read_b64_tr_b16 a[168:169], v14 offset:32768\n"
      "s_waitcnt lgkmcnt(5)\n"
      "v_mfma_f32_16x16x32_bf16 v[144:147], a[188:191], a[132:135], v[144:147]\n"
      "ds_read_b64_tr_b16 a[170:171], v14 offset:33280\n"
      "v_mfma_f32_16x16x32_bf16 v[148:151], a[184:187], a[124:127], v[148:151]\n"
      "ds_read_b64_tr_b16 a[172:173], v14 offset:36864\n"
      "v_mfma_f32_16x16x32_bf16 v[148:151], a[188:191], a[136:139], v[148:151]\n"
      "ds_read_b64_tr_b16 a[174:175], v14 offset:37376\n"
      "v_mfma_f32_16x16x32_bf16 v[152:155], a[184:187], a[128:131], v[152:155]\n"
      "ds_read_b64_tr_b16 a[176:177], v15 offset:32768\n"
      "v_mfma_f32_16x16x32_bf16 v[152:155], a[188:191], a[140:143], v[152:155]\n"
      "ds_read_b64_tr_b16 a[178:179], v15 offset:33280\n"
      "s_waitcnt lgkmcnt(8)\n"
      "v_mfma_f32_16x16x32_bf16 v[156:159], a[192:195], a[120:123], v[156:159]\n"
      "ds_read_b64_tr_b16 a[180:181], v15 offset:36864\n"
      "s_waitcnt lgkmcnt(7)\n"
      "v_mfma_f32_16x16x32_bf16 v[156:159], a[196:199], a[132:135], v[156:159]\n"
      "ds_read_b64_tr_b16 a[182:183], v15 offset:37376\n"
      "s_add_u32 s88, s65, s73\n"
      "s_add_u32 m0, s85, 0xc000\n"
      "v_mfma_f32_16x16x32_bf16 v[160:163], a[192:195], a[124:127], v[160:163]\n"
      "ds_read_b64_tr_b16 a[184:185], v16 offset:32768\n"
      "buffer_load_dwordx4 v192, s[20:23], s88 offen lds\n"
      "v_mfma_f32_16x16x32_bf16 v[160:163], a[196:199], a[136:139], v[160:163]\n"
      "ds_read_b64_tr_b16 a[186:187], v16 offset:33280\n"
      "v_mfma_f32_16x16x32_bf16 v[164:167], a[192:195], a[128:131], v[164:167]\n"
      "ds_read_b64_tr_b16 a[188:189], v16 offset:36864\n"
      "buffer_load_dwordx4 v193, s[20:23], s88 offen offset:1024 lds\n"
      "v_mfma_f32_16x16x32_bf16 v[164:167], a[196:199], a[140:143], v[164:167]\n"
      "ds_read_b64_tr_b16 a[190:191], v16 offset:37376\n"
      "s_waitcnt lgkmcnt(10)\n"
      "v_mfma_f32_16x16x32_bf16 v[120:123], a[168:171], a[144:147], v[120:123]\n"
      "ds_read_b64_tr_b16 a[192:193], v17 offset:32768\n"
      "buffer_load_dwordx4 v194, s[20:23], s88 offen offset:2048 lds\n"
      "s_waitcnt lgkmcnt(9)\n"
      "v_mfma_f32_16x16x32_bf16 v[120:123], a[172:175], a[156:159], v[120:123]\n"
      "ds_read_b64_tr_b16 a[194:195], v17 offset:33280\n"
      "v_mfma_f32_16x16x32_bf16 v[124:127], a[168:171], a[148:151], v[124:127]\n"
      "ds_read_b64_tr_b16 a[196:197], v17 offset:36864\n"
      "buffer_load_dwordx4 v195, s[20:23], s88 offen offset:3072 lds\n"
      "v_mfma_f32_16x16x32_bf16 v[124:127], a[172:175], a[160:163], v[124:127]\n"
      "ds_read_b64_tr_b16 a[198:199], v17 offset:37376\n"
      "s_add_u32 s88, s66, s73\n"
      "s_add_u32 m0, s85, 0x1c000\n"
      "v_mfma_f32_16x16x32_bf16 v[128:131], a[168:171], a[152:155], v[128:131]\n"
      "buffer_load_dwordx4 v192, s[20:23], s88 offen lds\n"
      "v_mfma_f32_16x16x32_bf16 v[128:131], a[172:175], a[164:167], v[128:131]\n"
      "s_waitcnt lgkmcnt(10)\n"
      "v_mfma_f32_16x16x32_bf16 v[132:135], a[176:179], a[144:147], v[132:135]\n"
      "buffer_load_dwordx4 v193, s[20:23], s88 offen offset:1024 lds\n"
      "s_waitcnt lgkmcnt(8)\n"
      "v_mfma_f32_16x16x32_bf16 v[132:135], a[180:183], a[156:159], v[132:135]\n"
      "v_mfma_f32_16x16x32_bf16 v[136:139], a[176:179], a[148:151], v[136:139]\n"
      "buffer_load_dwordx4 v194, s[20:23], s88 offen offset:2048 lds\n"
      "v_mfma_f32_16x16x32_bf16 v[136:139], a[180:183], a[160:163], v[136:139]\n"
      "v_mfma_f32_16x16x32_bf16 v[140:143], a[176:179], a[152:155], v[140:143]\n"
      "buffer_load_dwordx4 v195, s[20:23], s88 offen offset:3072 lds\n"
      "v_mfma_f32_16x16x32_bf16 v[140:143], a[180:183], a[164:167], v[140:143]\n"
      "s_waitcnt vmcnt(24)\n"
      "s_barrier\n"
      "s_waitcnt lgkmcnt(6)\n"
      "v_mfma_f32_16x16x32_bf16 v[144:147], a[184:187], a[144:147], v[144:147]\n"
      "ds_read_b64_tr_b16 a[168:169], v200 offset:32768\n"
      "v_cvt_pk_bf16_f32 v220, v120, v121\n"
      "s_waitcnt lgkmcnt(5)\n"
      "v_mfma_f32_16x16x32_bf16 v[144:147], a[188:191], a[156:159], v[144:147]\n"
      "ds_read_b64_tr_b16 a[170:171], v200 offset:33280\n"
      "v_cvt_pk_bf16_f32 v221, v122, v123\n"
      "v_mfma_f32_16x16x32_bf16 v[148:151], a[184:187], a[148:151], v[148:151]\n"
      "ds_read_b64_tr_b16 a[172:173], v200 offset:36864\n"
      "v_cvt_pk_bf16_f32 v224, v124, v125\n"
      "v_mfma_f32_16x16x32_bf16 v[148:151], a[188:191], a[160:163], v[148:151]\n"
      "ds_read_b64_tr_b16 a[174:175], v200 offset:37376\n"
      "v_cvt_pk_bf16_f32 v225, v126, v127\n"
      "v_mfma_f32_16x16x32_bf16 v[152:155], a[184:187], a[152:155], v[152:155]\n"
      "ds_read_b64_tr_b16 a[176:177], v201 offset:32768\n"
      "v_cvt_pk_bf16_f32 v228, v128, v129\n"
      "v_mfma_f32_16x16x32_bf16 v[152:155], a[188:191], a[164:167], v[152:155]\n"
      "ds_read_b64_tr_b16 a[178:179], v201 offset:33280\n"
      "v_cvt_pk_bf16_f32 v229, v130, v131\n"
      "s_waitcnt lgkmcnt(8)\n"
      "v_mfma_f32_16x16x32_bf16 v[156:159], a[192:195], a[144:147], v[156:159]\n"
      "ds_read_b64_tr_b16 a[180:181], v201 offset:36864\n"
      "v_cvt_pk_bf16_f32 v222, v132, v133\n"
      "s_waitcnt lgkmcnt(7)\n"
      "v_mfma_f32_16x16x32_bf16 v[156:159], a[196:199], a[156:159], v[156:159]\n"
      "ds_read_b64_tr_b16 a[182:183], v201 offset:37376\n"
      "v_cvt_pk_bf16_f32 v223, v134, v135\n"
      "s_add_u32 s88, s67, s73\n"
      "s_add_u32 m0, s85, 0x10000\n"
      "v_mfma_f32_16x16x32_bf16 v[160:163], a[192:195], a[148:151], v[160:163]\n"
      "ds_read_b64_tr_b16 a[184:185], v202 offset:32768\n"
      "v_cvt_pk_bf16_f32 v226, v136, v137\n"
      "buffer_load_dwordx4 v192, s[20:23], s88 offen lds\n"
      "v_mfma_f32_16x16x32_bf16 v[160:163], a[196:199], a[160:163], v[160:163]\n"
      "ds_read_b64_tr_b16 a[186:187], v202 offset:33280\n"
      "v_cvt_pk_bf16_f32 v227, v138, v139\n"
      "v_mfma_f32_16x16x32_bf16 v[164:167], a[192:195], a[152:155], v[164:167]\n"
      "ds_read_b64_tr_b16 a[188:189], v202 offset:36864\n"
      "v_cvt_pk_bf16_f32 v230, v140, v141\n"
      "buffer_load_dwordx4 v193, s[20:23], s88 offen offset:1024 lds\n"
      "v_mfma_f32_16x16x32_bf16 v[164:167], a[196:199], a[164:167], v[164:167]\n"
      "ds_read_b64_tr_b16 a[190:191], v202 offset:37376\n"
      "v_cvt_pk_bf16_f32 v231, v142, v143\n"
      "s_waitcnt lgkmcnt(10)\n"
      "v_mfma_f32_16x16x32_bf16 v[120:123], a[168:171], a[0:3], 0\n"
      "ds_read_b64_tr_b16 a[192:193], v203 offset:32768\n"
      "v_cvt_pk_bf16_f32 v232, v144, v145\n"
      "buffer_load_dwordx4 v194, s[20:23], s88 offen offset:2048 lds\n"
      "s_waitcnt lgkmcnt(9)\n"
      "v_mfma_f32_16x16x32_bf16 v[120:123], a[172:175], a[12:15], v[120:123]\n"
      "ds_read_b64_tr_b16 a[194:195], v203 offset:33280\n"
      "v_cvt_pk_bf16_f32 v233, v146, v147\n"
      "v_mfma_f32_16x16x32_bf16 v[124:127], a[168:171], a[4:7], 0\n"
      "ds_read_b64_tr_b16 a[196:197], v203 offset:36864\n"
      "v_cvt_pk_bf16_f32 v236, v148, v149\n"
      "buffer_load_dwordx4 v195, s[20:23], s88 offen offset:3072 lds\n"
      "v_mfma_f32_16x16x32_bf16 v[124:127], a[172:175], a[16:19], v[124:127]\n"
      "ds_read_b64_tr_b16 a[198:199], v203 offset:37376\n"
      "v_cvt_pk_bf16_f32 v237, v150, v151\n"
      "s_add_u32 s88, s68, s73\n"
      "s_add_u32 m0, s85, 0x20000\n"
      "v_mfma_f32_16x16x32_bf16 v[128:131], a[168:171], a[8:11], 0\n"
      "v_cvt_pk_bf16_f32 v240, v152, v153\n"
      "buffer_load_dwordx4 v192, s[20:23], s88 offen lds\n"
      "v_mfma_f32_16x16x32_bf16 v[128:131], a[172:175], a[20:23], v[128:131]\n"
      "v_cvt_pk_bf16_f32 v241, v154, v155\n"
      "s_waitcnt lgkmcnt(10)\n"
      "v_mfma_f32_16x16x32_bf16 v[132:135], a[176:179], a[0:3], 0\n"
      "v_cvt_pk_bf16_f32 v234, v156, v157\n"
      "buffer_load_dwordx4 v193, s[20:23], s88 offen offset:1024 lds\n"
      "s_waitcnt lgkmcnt(8)\n"
      "v_mfma_f32_16x16x32_bf16 v[132:135], a[180:183], a[12:15], v[132:135]\n"
      "v_cvt_pk_bf16_f32 v235, v158, v159\n"
      "v_mfma_f32_16x16x32_bf16 v[136:139], a[176:179], a[4:7], 0\n"
      "v_cvt_pk_bf16_f32 v238, v160, v161\n"
      "buffer_load_dwordx4 v194, s[20:23], s88 offen offset:2048 lds\n"
      "v_mfma_f32_16x16x32_bf16 v[136:139], a[180:183], a[16:19], v[136:139]\n"
      "v_cvt_pk_bf16_f32 v239, v162, v163\n"
      "v_mfma_f32_16x16x32_bf16 v[140:143], a[176:179], a[8:11], 0\n"
      "v_cvt_pk_bf16_f32 v242, v164, v165\n"
      "buffer_load_dwordx4 v195, s[20:23], s88 offen offset:3072 lds\n"
      "v_mfma_f32_16x16x32_bf16 v[140:143], a[180:183], a[20:23], v[140:143]\n"
      "v_cvt_pk_bf16_f32 v243, v166, v167\n"
      "s_waitcnt vmcnt(16) lgkmcnt(0)\n"
      "s_barrier\n"
      "v_mfma_f32_16x16x32_bf16 v[144:147], a[184:187], a[0:3], 0\n"
      "ds_read_b64_tr_b16 a[168:169], v14 offset:49152\n"
      "v_mfma_f32_16x16x32_bf16 v[144:147], a[188:191], a[12:15], v[144:147]\n"
      "ds_read_b64_tr_b16 a[170:171], v14 offset:49664\n"
      "v_mfma_f32_16x16x32_bf16 v[148:151], a[184:187], a[4:7], 0\n"
      "ds_read_b64_tr_b16 a[172:173], v14 offset:53248\n"
      "v_mfma_f32_16x16x32_bf16 v[148:151], a[188:191], a[16:19], v[148:151]\n"
      "ds_read_b64_tr_b16 a[174:175], v14 offset:53760\n"
      "v_mfma_f32_16x16x32_bf16 v[152:155], a[184:187], a[8:11], 0\n"
      "ds_read_b64_tr_b16 a[176:177], v15 offset:49152\n"
      "v_mfma_f32_16x16x32_bf16 v[152:155], a[188:191], a[20:23], v[152:155]\n"
      "ds_read_b64_tr_b16 a[178:179], v15 offset:49664\n"
      "v_mfma_f32_16x16x32_bf16 v[156:159], a[192:195], a[0:3], 0\n"
      "ds_read_b64_tr_b16 a[180:181], v15 offset:53248\n"
      "v_mfma_f32_16x16x32_bf16 v[156:159], a[196:199], a[12:15], v[156:159]\n"
      "ds_read_b64_tr_b16 a[182:183], v15 offset:53760\n"
      "v_mfma_f32_16x16x32_bf16 v[160:163], a[192:195], a[4:7], 0\n"
      "ds_read_b64_tr_b16 a[184:185], v16 offset:49152\n"
      "v_mfma_f32_16x16x32_bf16 v[160:163], a[196:199], a[16:19], v[160:163]\n"
      "ds_read_b64_tr_b16 a[186:187], v16 offset:49664\n"
      "v_mfma_f32_16x16x32_bf16 v[164:167], a[192:195], a[8:11], 0\n"
      "ds_read_b64_tr_b16 a[188:189], v16 offset:53248\n"
      "v_mfma_f32_16x16x32_bf16 v[164:167], a[196:199], a[20:23], v[164:167]\n"
      "ds_read_b64_tr_b16 a[190:191], v16 offset:53760\n"
      "v_mfma_f32_16x16x32_bf16 v[48:51], a[200:203], v[220:223], v[48:51]\n"
      "ds_read_b64_tr_b16 a[192:193], v17 offset:49152\n"
      "v_mfma_f32_16x16x32_bf16 v[48:51], a[224:227], v[232:235], v[48:51]\n"
      "ds_read_b64_tr_b16 a[194:195], v17 offset:49664\n"
      "v_mfma_f32_16x16x32_bf16 v[52:55], a[200:203], v[224:227], v[52:55]\n"
      "ds_read_b64_tr_b16 a[196:197], v17 offset:53248\n"
      "v_mfma_f32_16x16x32_bf16 v[52:55], a[224:227], v[236:239], v[52:55]\n"
      "ds_read_b64_tr_b16 a[198:199], v17 offset:53760\n"
      "v_mfma_f32_16x16x32_bf16 v[56:59], a[200:203], v[228:231], v[56:59]\n"
      "v_mfma_f32_16x16x32_bf16 v[56:59], a[224:227], v[240:243], v[56:59]\n"
      "ds_read_b64 a[200:201], v24 offset:24576\n"
      "v_mfma_f32_16x16x32_bf16 v[60:63], a[204:207], v[220:223], v[60:63]\n"
      "ds_read_b64 a[202:203], v25 offset:24576\n"
      "v_mfma_f32_16x16x32_bf16 v[60:63], a[228:231], v[232:235], v[60:63]\n"
      "ds_read_b64 a[224:225], v26 offset:24576\n"
      "v_mfma_f32_16x16x32_bf16 v[64:67], a[204:207], v[224:227], v[64:67]\n"
      "ds_read_b64 a[226:227], v27 offset:24576\n"
      "v_mfma_f32_16x16x32_bf16 v[64:67], a[228:231], v[236:239], v[64:67]\n"
      "v_mfma_f32_16x16x32_bf16 v[68:71], a[204:207], v[228:231], v[68:71]\n"
      "v_mfma_f32_16x16x32_bf16 v[68:71], a[228:231], v[240:243], v[68:71]\n"
      "ds_read_b64 a[204:205], v24 offset:26624\n"
      "v_mfma_f32_16x16x32_bf16 v[72:75], a[208:211], v[220:223], v[72:75]\n"
      "ds_read_b64 a[206:207], v25 offset:26624\n"
      "v_mfma_f32_16x16x32_bf16 v[72:75], a[232:235], v[232:235], v[72:75]\n"
      "ds_read_b64 a[228:229], v26 offset:26624\n"
      "v_mfma_f32_16x16x32_bf16 v[76:79], a[208:211], v[224:227], v[76:79]\n"
      "ds_read_b64 a[230:231], v27 offset:26624\n"
      "v_mfma_f32_16x16x32_bf16 v[76:79], a[232:235], v[236:239], v[76:79]\n"
      "v_mfma_f32_16x16x32_bf16 v[80:83], a[208:211], v[228:231], v[80:83]\n"
      "v_mfma_f32_16x16x32_bf16 v[80:83], a[232:235], v[240:243], v[80:83]\n"
      "ds_read_b64 a[208:209], v24 offset:32768\n"
      "v_mfma_f32_16x16x32_bf16 v[84:87], a[212:215], v[220:223], v[84:87]\n"
      "ds_read_b64 a[210:211], v25 offset:32768\n"
      "v_mfma_f32_16x16x32_bf16 v[84:87], a[236:239], v[232:235], v[84:87]\n"
      "ds_read_b64 a[232:233], v26 offset:32768\n"
      "v_mfma_f32_16x16x32_bf16 v[88:91], a[212:215], v[224:227], v[88:91]\n"
      "ds_read_b64 a[234:235], v27 offset:32768\n"
      "v_mfma_f32_16x16x32_bf16 v[88:91], a[236:239], v[236:239], v[88:91]\n"
      "v_mfma_f32_16x16x32_bf16 v[92:95], a[212:215], v[228:231], v[92:95]\n"
      "v_mfma_f32_16x16x32_bf16 v[92:95], a[236:239], v[240:243], v[92:95]\n"
      "ds_read_b64 a[212:213], v24 offset:34816\n"
      "v_mfma_f32_16x16x32_bf16 v[96:99], a[216:219], v[220:223], v[96:99]\n"
      "ds_read_b64 a[214:215], v25 offset:34816\n"
      "v_mfma_f32_16x16x32_bf16 v[96:99], a[240:243], v[232:235], v[96:99]\n"
      "ds_read_b64 a[236:237], v26 offset:34816\n"
      "v_mfma_f32_16x16x32_bf16 v[100:103], a[216:219], v[224:227], v[100:103]\n"
      "ds_read_b64 a[238:239], v27 offset:34816\n"
      "v_mfma_f32_16x16x32_bf16 v[100:103], a[240:243], v[236:239], v[100:103]\n"
      "s_add_u32 s88, s69, s73\n"
      "s_add_u32 m0, s85, 0x14000\n"
      "v_mfma_f32_16x16x32_bf16 v[104:107], a[216:219], v[228:231], v[104:107]\n"
      "buffer_load_dwordx4 v192, s[20:23], s88 offen lds\n"
      "v_mfma_f32_16x16x32_bf16 v[104:107], a[240:243], v[240:243], v[104:107]\n"
      "ds_read_b64 a[216:217], v24 offset:40960\n"
      "v_mfma_f32_16x16x32_bf16 v[108:111], a[220:223], v[220:223], v[108:111]\n"
      "ds_read_b64 a[218:219], v25 offset:40960\n"
      "v_mfma_f32_16x16x32_bf16 v[108:111], a[244:247], v[232:235], v[108:111]\n"
      "ds_read_b64 a[240:241], v26 offset:40960\n"
      "v_mfma_f32_16x16x32_bf16 v[112:115], a[220:223], v[224:227], v[112:115]\n"
      "ds_read_b64 a[242:243], v27 offset:40960\n"
      "v_mfma_f32_16x16x32_bf16 v[112:115], a[244:247], v[236:239], v[112:115]\n"
      "v_mfma_f32_16x16x32_bf16 v[116:119], a[220:223], v[228:231], v[116:119]\n"
      "buffer_load_dwordx4 v193, s[20:23], s88 offen offset:1024 lds\n"
      "v_mfma_f32_16x16x32_bf16 v[116:119], a[244:247], v[240:243], v[116:119]\n"
      "ds_read_b64 a[220:221], v24 offset:43008\n"
      "s_waitcnt lgkmcnt(15)\n"
      "v_mfma_f32_16x16x32_bf16 v[120:123], a[168:171], a[24:27], v[120:123]\n"
      "ds_read_b64 a[222:223], v25 offset:43008\n"
      "v_mfma_f32_16x16x32_bf16 v[120:123], a[172:175], a[36:39], v[120:123]\n"
      "ds_read_b64 a[244:245], v26 offset:43008\n"
      "v_mfma_f32_16x16x32_bf16 v[124:127], a[168:171], a[28:31], v[124:127]\n"
      "ds_read_b64 a[246:247], v27 offset:43008\n"
      "v_mfma_f32_16x16x32_bf16 v[124:127], a[172:175], a[40:43], v[124:127]\n"
      "v_mfma_f32_16x16x32_bf16 v[128:131], a[168:171], a[32:35], v[128:131]\n"
      "buffer_load_dwordx4 v194, s[20:23], s88 offen offset:2048 lds\n"
      "v_mfma_f32_16x16x32_bf16 v[128:131], a[172:175], a[44:47], v[128:131]\n"
      "v_mfma_f32_16x16x32_bf16 v[132:135], a[176:179], a[24:27], v[132:135]\n"
      "buffer_load_dwordx4 v195, s[20:23], s88 offen offset:3072 lds\n"
      "v_mfma_f32_16x16x32_bf16 v[132:135], a[180:183], a[36:39], v[132:135]\n"
      "s_add_u32 s89, s71, 0\n"
      "s_add_u32 m0, s86, 0x24000\n"
      "v_mfma_f32_16x16x32_bf16 v[136:139], a[176:179], a[28:31], v[136:139]\n"
      "buffer_load_dwordx4 v216, s[16:19], s89 offen nt lds\n"
      "v_mfma_f32_16x16x32_bf16 v[136:139], a[180:183], a[40:43], v[136:139]\n"
      "v_mfma_f32_16x16x32_bf16 v[140:143], a[176:179], a[32:35], v[140:143]\n"
      "buffer_load_dwordx4 v217, s[16:19], s89 offen offset:1024 nt lds\n"
      "v_mfma_f32_16x16x32_bf16 v[140:143], a[180:183], a[44:47], v[140:143]\n"
      "s_waitcnt vmcnt(18)\n"
      "s_barrier\n"
      "v_mfma_f32_16x16x32_bf16 v[144:147], a[184:187], a[24:27], v[144:147]\n"
      "ds_read_b64_tr_b16 a[168:169], v14\n"
      "v_mfma_f32_16x16x32_bf16 v[144:147], a[188:191], a[36:39], v[144:147]\n"
      "ds_read_b64_tr_b16 a[170:171], v14 offset:512\n"
      "v_mfma_f32_16x16x32_bf16 v[148:151], a[184:187], a[28:31], v[148:151]\n"
      "ds_read_b64_tr_b16 a[172:173], v14 offset:4096\n"
      "v_mfma_f32_16x16x32_bf16 v[148:151], a[188:191], a[40:43], v[148:151]\n"
      "ds_read_b64_tr_b16 a[174:175], v14 offset:4608\n"
      "v_mfma_f32_16x16x32_bf16 v[152:155], a[184:187], a[32:35], v[152:155]\n"
      "ds_read_b64_tr_b16 a[176:177], v15\n"
      "v_mfma_f32_16x16x32_bf16 v[152:155], a[188:191], a[44:47], v[152:155]\n"
      "ds_read_b64_tr_b16 a[178:179], v15 offset:512\n"
      "v_mfma_f32_16x16x32_bf16 v[156:159], a[192:195], a[24:27], v[156:159]\n"
      "ds_read_b64_tr_b16 a[180:181], v15 offset:4096\n"
      "v_mfma_f32_16x16x32_bf16 v[156:159], a[196:199], a[36:39], v[156:159]\n"
      "ds_read_b64_tr_b16 a[182:183], v15 offset:4608\n"
      "v_mfma_f32_16x16x32_bf16 v[160:163], a[192:195], a[28:31], v[160:163]\n"
      "ds_read_b64_tr_b16 a[184:185], v16\n"
      "v_mfma_f32_16x16x32_bf16 v[160:163], a[196:199], a[40:43], v[160:163]\n"
      "ds_read_b64_tr_b16 a[186:187], v16 offset:512\n"
      "v_mfma_f32_16x16x32_bf16 v[164:167], a[192:195], a[32:35], v[164:167]\n"
      "ds_read_b64_tr_b16 a[188:189], v16 offset:4096\n"
      "v_mfma_f32_16x16x32_bf16 v[164:167], a[196:199], a[44:47], v[164:167]\n"
      "ds_read_b64_tr_b16 a[190:191], v16 offset:4608\n"
      "s_waitcnt lgkmcnt(10)\n"
      "v_mfma_f32_16x16x32_bf16 v[120:123], a[168:171], a[48:51], v[120:123]\n"
      "ds_read_b64_tr_b16 a[192:193], v17\n"
      "s_waitcnt lgkmcnt(9)\n"
      "v_mfma_f32_16x16x32_bf16 v[120:123], a[172:175], a[60:63], v[120:123]\n"
      "ds_read_b64_tr_b16 a[194:195], v17 offset:512\n"
      "v_mfma_f32_16x16x32_bf16 v[124:127], a[168:171], a[52:55], v[124:127]\n"
      "ds_read_b64_tr_b16 a[196:197], v17 offset:4096\n"
      "v_mfma_f32_16x16x32_bf16 v[124:127], a[172:175], a[64:67], v[124:127]\n"
      "ds_read_b64_tr_b16 a[198:199], v17 offset:4608\n"
      "s_add_u32 s89, s71, 0x4000\n"
      "s_add_u32 m0, s86, 0x26000\n"
      "v_mfma_f32_16x16x32_bf16 v[128:131], a[168:171], a[56:59], v[128:131]\n"
      "buffer_load_dwordx4 v216, s[16:19], s89 offen nt lds\n"
      "v_mfma_f32_16x16x32_bf16 v[128:131], a[172:175], a[68:71], v[128:131]\n"
      "s_waitcnt lgkmcnt(10)\n"
      "v_mfma_f32_16x16x32_bf16 v[132:135], a[176:179], a[48:51], v[132:135]\n"
      "buffer_load_dwordx4 v217, s[16:19], s89 offen offset:1024 nt lds\n"
      "s_waitcnt lgkmcnt(8)\n"
      "v_mfma_f32_16x16x32_bf16 v[132:135], a[180:183], a[60:63], v[132:135]\n"
      "s_add_u32 s89, s71, 0x8000\n"
      "s_add_u32 m0, s86, 0x18000\n"
      "v_mfma_f32_16x16x32_bf16 v[136:139], a[176:179], a[52:55], v[136:139]\n"
      "buffer_load_dwordx4 v216, s[16:19], s89 offen nt lds\n"
      "v_mfma_f32_16x16x32_bf16 v[136:139], a[180:183], a[64:67], v[136:139]\n"
      "v_mfma_f32_16x16x32_bf16 v[140:143], a[176:179], a[56:59], v[140:143]\n"
      "buffer_load_dwordx4 v217, s[16:19], s89 offen offset:1024 nt lds\n"
      "v_mfma_f32_16x16x32_bf16 v[140:143], a[180:183], a[68:71], v[140:143]\n"
      "s_waitcnt vmcnt(18)\n"
      "s_barrier\n"
      "s_waitcnt lgkmcnt(6)\n"
      "v_mfma_f32_16x16x32_bf16 v[144:147], a[184:187], a[48:51], v[144:147]\n"
      "ds_read_b64_tr_b16 a[168:169], v200\n"
      "s_waitcnt lgkmcnt(5)\n"
      "v_mfma_f32_16x16x32_bf16 v[144:147], a[188:191], a[60:63], v[144:147]\n"
      "ds_read_b64_tr_b16 a[170:171], v200 offset:512\n"
      "v_mfma_f32_16x16x32_bf16 v[148:151], a[184:187], a[52:55], v[148:151]\n"
      "ds_read_b64_tr_b16 a[172:173], v200 offset:4096\n"
      "v_mfma_f32_16x16x32_bf16 v[148:151], a[188:191], a[64:67], v[148:151]\n"
      "ds_read_b64_tr_b16 a[174:175], v200 offset:4608\n"
      "v_mfma_f32_16x16x32_bf16 v[152:155], a[184:187], a[56:59], v[152:155]\n"
      "ds_read_b64_tr_b16 a[176:177], v201\n"
      "v_mfma_f32_16x16x32_bf16 v[152:155], a[188:191], a[68:71], v[152:155]\n"
      "ds_read_b64_tr_b16 a[178:179], v201 offset:512\n"
      "s_waitcnt lgkmcnt(8)\n"
      "v_mfma_f32_16x16x32_bf16 v[156:159], a[192:195], a[48:51], v[156:159]\n"
      "ds_read_b64_tr_b16 a[180:181], v201 offset:4096\n"
      "s_waitcnt lgkmcnt(7)\n"
      "v_mfma_f32_16x16x32_bf16 v[156:159], a[196:199], a[60:63], v[156:159]\n"
      "ds_read_b64_tr_b16 a[182:183], v201 offset:4608\n"
      "v_mfma_f32_16x16x32_bf16 v[160:163], a[192:195], a[52:55], v[160:163]\n"
      "ds_read_b64_tr_b16 a[184:185], v202\n"
      "v_mfma_f32_16x16x32_bf16 v[160:163], a[196:199], a[64:67], v[160:163]\n"
      "ds_read_b64_tr_b16 a[186:187], v202 offset:512\n"
      "v_mfma_f32_16x16x32_bf16 v[164:167], a[192:195], a[56:59], v[164:167]\n"
      "ds_read_b64_tr_b16 a[188:189], v202 offset:4096\n"
      "v_mfma_f32_16x16x32_bf16 v[164:167], a[196:199], a[68:71], v[164:167]\n"
      "ds_read_b64_tr_b16 a[190:191], v202 offset:4608\n"
      "s_waitcnt lgkmcnt(10)\n"
      "v_mfma_f32_16x16x32_bf16 v[120:123], a[168:171], a[72:75], v[120:123]\n"
      "ds_read_b64_tr_b16 a[192:193], v203\n"
      "s_waitcnt lgkmcnt(9)\n"
      "v_mfma_f32_16x16x32_bf16 v[120:123], a[172:175], a[84:87], v[120:123]\n"
      "ds_read_b64_tr_b16 a[194:195], v203 offset:512\n"
      "v_mfma_f32_16x16x32_bf16 v[124:127], a[168:171], a[76:79], v[124:127]\n"
      "ds_read_b64_tr_b16 a[196:197], v203 offset:4096\n"
      "v_mfma_f32_16x16x32_bf16 v[124:127], a[172:175], a[88:91], v[124:127]\n"
      "ds_read_b64_tr_b16 a[198:199], v203 offset:4608\n"
      "s_add_u32 s89, s72, 0x4000\n"
      "s_add_u32 m0, s86, 0x6000\n"
      "v_mfma_f32_16x16x32_bf16 v[128:131], a[168:171], a[80:83], v[128:131]\n"
      "buffer_load_dwordx4 v216, s[16:19], s89 offen nt lds\n"
      "v_mfma_f32_16x16x32_bf16 v[128:131], a[172:175], a[92:95], v[128:131]\n"
      "s_waitcnt lgkmcnt(10)\n"
      "v_mfma_f32_16x16x32_bf16 v[132:135], a[176:179], a[72:75], v[132:135]\n"
      "buffer_load_dwordx4 v217, s[16:19], s89 offen offset:1024 nt lds\n"
      "s_waitcnt lgkmcnt(8)\n"
      "v_mfma_f32_16x16x32_bf16 v[132:135], a[180:183], a[84:87], v[132:135]\n"
      "s_add_u32 s89, s72, 0\n"
      "s_add_u32 m0, s86, 0x1a000\n"
      "v_mfma_f32_16x16x32_bf16 v[136:139], a[176:179], a[76:79], v[136:139]\n"
      "buffer_load_dwordx4 v216, s[16:19], s89 offen nt lds\n"
      "v_mfma_f32_16x16x32_bf16 v[136:139], a[180:183], a[88:91], v[136:139]\n"
      "v_mfma_f32_16x16x32_bf16 v[140:143], a[176:179], a[80:83], v[140:143]\n"
      "buffer_load_dwordx4 v217, s[16:19], s89 offen offset:1024 nt lds\n"
      "v_mfma_f32_16x16x32_bf16 v[140:143], a[180:183], a[92:95], v[140:143]\n"
      "s_waitcnt vmcnt(18)\n"
      "s_barrier\n"
      "s_waitcnt lgkmcnt(6)\n"
      "v_mfma_f32_16x16x32_bf16 v[144:147], a[184:187], a[72:75], v[144:147]\n"
      "ds_read_b64_tr_b16 a[168:169], v14 offset:16384\n"
      "s_waitcnt lgkmcnt(5)\n"
      "v_mfma_f32_16x16x32_bf16 v[144:147], a[188:191], a[84:87], v[144:147]\n"
      "ds_read_b64_tr_b16 a[170:171], v14 offset:16896\n"
      "v_mfma_f32_16x16x32_bf16 v[148:151], a[184:187], a[76:79], v[148:151]\n"
      "ds_read_b64_tr_b16 a[172:173], v14 offset:20480\n"
      "v_mfma_f32_16x16x32_bf16 v[148:151], a[188:191], a[88:91], v[148:151]\n"
      "ds_read_b64_tr_b16 a[174:175], v14 offset:20992\n"
      "v_mfma_f32_16x16x32_bf16 v[152:155], a[184:187], a[80:83], v[152:155]\n"
      "ds_read_b64_tr_b16 a[176:177], v15 offset:16384\n"
      "v_mfma_f32_16x16x32_bf16 v[152:155], a[188:191], a[92:95], v[152:155]\n"
      "ds_read_b64_tr_b16 a[178:179], v15 offset:16896\n"
      "s_waitcnt lgkmcnt(8)\n"
      "v_mfma_f32_16x16x32_bf16 v[156:159], a[192:195], a[72:75], v[156:159]\n"
      "ds_read_b64_tr_b16 a[180:181], v15 offset:20480\n"
      "s_waitcnt lgkmcnt(7)\n"
      "v_mfma_f32_16x16x32_bf16 v[156:159], a[196:199], a[84:87], v[156:159]\n"
      "ds_read_b64_tr_b16 a[182:183], v15 offset:20992\n"
      "v_mfma_f32_16x16x32_bf16 v[160:163], a[192:195], a[76:79], v[160:163]\n"
      "ds_read_b64_tr_b16 a[184:185], v16 offset:16384\n"
      "v_mfma_f32_16x16x32_bf16 v[160:163], a[196:199], a[88:91], v[160:163]\n"
      "ds_read_b64_tr_b16 a[186:187], v16 offset:16896\n"
      "v_mfma_f32_16x16x32_bf16 v[164:167], a[192:195], a[80:83], v[164:167]\n"
      "ds_read_b64_tr_b16 a[188:189], v16 offset:20480\n"
      "v_mfma_f32_16x16x32_bf16 v[164:167], a[196:199], a[92:95], v[164:167]\n"
      "ds_read_b64_tr_b16 a[190:191], v16 offset:20992\n"
      "s_waitcnt lgkmcnt(10)\n"
      "v_mfma_f32_16x16x32_bf16 v[120:123], a[168:171], a[96:99], v[120:123]\n"
      "ds_read_b64_tr_b16 a[192:193], v17 offset:16384\n"
      "s_waitcnt lgkmcnt(9)\n"
      "v_mfma_f32_16x16x32_bf16 v[120:123], a[172:175], a[108:111], v[120:123]\n"
      "ds_read_b64_tr_b16 a[194:195], v17 offset:16896\n"
      "v_mfma_f32_16x16x32_bf16 v[124:127], a[168:171], a[100:103], v[124:127]\n"
      "ds_read_b64_tr_b16 a[196:197], v17 offset:20480\n"
      "v_mfma_f32_16x16x32_bf16 v[124:127], a[172:175], a[112:115], v[124:127]\n"
      "ds_read_b64_tr_b16 a[198:199], v17 offset:20992\n"
      "v_mfma_f32_16x16x32_bf16 v[128:131], a[168:171], a[104:107], v[128:131]\n"
      "v_mfma_f32_16x16x32_bf16 v[128:131], a[172:175], a[116:119], v[128:131]\n"
      "s_waitcnt lgkmcnt(10)\n"
      "v_mfma_f32_16x16x32_bf16 v[132:135], a[176:179], a[96:99], v[132:135]\n"
      "s_waitcnt lgkmcnt(8)\n"
      "v_mfma_f32_16x16x32_bf16 v[132:135], a[180:183], a[108:111], v[132:135]\n"
      "v_mfma_f32_16x16x32_bf16 v[136:139], a[176:179], a[100:103], v[136:139]\n"
      "v_mfma_f32_16x16x32_bf16 v[136:139], a[180:183], a[112:115], v[136:139]\n"
      "v_mfma_f32_16x16x32_bf16 v[140:143], a[176:179], a[104:107], v[140:143]\n"
      "v_mfma_f32_16x16x32_bf16 v[140:143], a[180:183], a[116:119], v[140:143]\n"
      "s_waitcnt vmcnt(14)\n"
      "s_barrier\n"
      "s_waitcnt lgkmcnt(6)\n"
      "v_mfma_f32_16x16x32_bf16 v[144:147], a[184:187], a[96:99], v[144:147]\n"
      "ds_read_b64_tr_b16 a[168:169], v200 offset:16384\n"
      "s_waitcnt lgkmcnt(5)\n"
      "v_mfma_f32_16x16x32_bf16 v[144:147], a[188:191], a[108:111], v[144:147]\n"
      "ds_read_b64_tr_b16 a[170:171], v200 offset:16896\n"
      "v_mfma_f32_16x16x32_bf16 v[148:151], a[184:187], a[100:103], v[148:151]\n"
      "ds_read_b64_tr_b16 a[172:173], v200 offset:20480\n"
      "v_mfma_f32_16x16x32_bf16 v[148:151], a[188:191], a[112:115], v[148:151]\n"
      "ds_read_b64_tr_b16 a[174:175], v200 offset:20992\n"
      "v_mfma_f32_16x16x32_bf16 v[152:155], a[184:187], a[104:107], v[152:155]\n"
      "ds_read_b64_tr_b16 a[176:177], v201 offset:16384\n"
      "v_mfma_f32_16x16x32_bf16 v[152:155], a[188:191], a[116:119], v[152:155]\n"
      "ds_read_b64_tr_b16 a[178:179], v201 offset:16896\n"
      "s_waitcnt lgkmcnt(8)\n"
      "v_mfma_f32_16x16x32_bf16 v[156:159], a[192:195], a[96:99], v[156:159]\n"
      "ds_read_b64_tr_b16 a[180:181], v201 offset:20480\n"
      "s_waitcnt lgkmcnt(7)\n"
      "v_mfma_f32_16x16x32_bf16 v[156:159], a[196:199], a[108:111], v[156:159]\n"
      "ds_read_b64_tr_b16 a[182:183], v201 offset:20992\n"
      "v_mfma_f32_16x16x32_bf16 v[160:163], a[192:195], a[100:103], v[160:163]\n"
      "ds_read_b64_tr_b16 a[184:185], v202 offset:16384\n"
      "v_mfma_f32_16x16x32_bf16 v[160:163], a[196:199], a[112:115], v[160:163]\n"
      "ds_read_b64_tr_b16 a[186:187], v202 offset:16896\n"
      "v_mfma_f32_16x16x32_bf16 v[164:167], a[192:195], a[104:107], v[164:167]\n"
      "ds_read_b64_tr_b16 a[188:189], v202 offset:20480\n"
      "v_mfma_f32_16x16x32_bf16 v[164:167], a[196:199], a[116:119], v[164:167]\n"
      "ds_read_b64_tr_b16 a[190:191], v202 offset:20992\n"
      "s_waitcnt lgkmcnt(10)\n"
      "v_mfma_f32_16x16x32_bf16 v[120:123], a[168:171], a[120:123], v[120:123]\n"
      "ds_read_b64_tr_b16 a[192:193], v203 offset:16384\n"
      "s_waitcnt lgkmcnt(9)\n"
      "v_mfma_f32_16x16x32_bf16 v[120:123], a[172:175], a[132:135], v[120:123]\n"
      "ds_read_b64_tr_b16 a[194:195], v203 offset:16896\n"
      "v_mfma_f32_16x16x32_bf16 v[124:127], a[168:171], a[124:127], v[124:127]\n"
      "ds_read_b64_tr_b16 a[196:197], v203 offset:20480\n"
      "v_mfma_f32_16x16x32_bf16 v[124:127], a[172:175], a[136:139], v[124:127]\n"
      "ds_read_b64_tr_b16 a[198:199], v203 offset:20992\n"
      "v_mfma_f32_16x16x32_bf16 v[128:131], a[168:171], a[128:131], v[128:131]\n"
      "v_mfma_f32_16x16x32_bf16 v[128:131], a[172:175], a[140:143], v[128:131]\n"
      "s_waitcnt lgkmcnt(10)\n"
      "v_mfma_f32_16x16x32_bf16 v[132:135], a[176:179], a[120:123], v[132:135]\n"
      "s_waitcnt lgkmcnt(8)\n"
      "v_mfma_f32_16x16x32_bf16 v[132:135], a[180:183], a[132:135], v[132:135]\n"
      "s_add_u32 s89, s72, 0x8000\n"
      "s_add_u32 m0, s86, 0x8000\n"
      "v_mfma_f32_16x16x32_bf16 v[136:139], a[176:179], a[124:127], v[136:139]\n"
      "buffer_load_dwordx4 v216, s[16:19], s89 offen nt lds\n"
      "v_mfma_f32_16x16x32_bf16 v[136:139], a[180:183], a[136:139], v[136:139]\n"
      "v_mfma_f32_16x16x32_bf16 v[140:143], a[176:179], a[128:131], v[140:143]\n"
      "buffer_load_dwordx4 v217, s[16:19], s89 offen offset:1024 nt lds\n"
      "v_mfma_f32_16x16x32_bf16 v[140:143], a[180:183], a[140:143], v[140:143]\n"
      "s_waitcnt vmcnt(12)\n"
      "s_barrier\n"
      "s_waitcnt lgkmcnt(6)\n"
      "v_mfma_f32_16x16x32_bf16 v[144:147], a[184:187], a[120:123], v[144:147]\n"
      "ds_read_b64_tr_b16 a[168:169], v14 offset:32768\n"
      "s_waitcnt lgkmcnt(5)\n"
      "v_mfma_f32_16x16x32_bf16 v[144:147], a[188:191], a[132:135], v[144:147]\n"
      "ds_read_b64_tr_b16 a[170:171], v14 offset:33280\n"
      "v_mfma_f32_16x16x32_bf16 v[148:151], a[184:187], a[124:127], v[148:151]\n"
      "ds_read_b64_tr_b16 a[172:173], v14 offset:36864\n"
      "v_mfma_f32_16x16x32_bf16 v[148:151], a[188:191], a[136:139], v[148:151]\n"
      "ds_read_b64_tr_b16 a[174:175], v14 offset:37376\n"
      "v_mfma_f32_16x16x32_bf16 v[152:155], a[184:187], a[128:131], v[152:155]\n"
      "ds_read_b64_tr_b16 a[176:177], v15 offset:32768\n"
      "v_mfma_f32_16x16x32_bf16 v[152:155], a[188:191], a[140:143], v[152:155]\n"
      "ds_read_b64_tr_b16 a[178:179], v15 offset:33280\n"
      "s_waitcnt lgkmcnt(8)\n"
      "v_mfma_f32_16x16x32_bf16 v[156:159], a[192:195], a[120:123], v[156:159]\n"
      "ds_read_b64_tr_b16 a[180:181], v15 offset:36864\n"
      "s_waitcnt lgkmcnt(7)\n"
      "v_mfma_f32_16x16x32_bf16 v[156:159], a[196:199], a[132:135], v[156:159]\n"
      "ds_read_b64_tr_b16 a[182:183], v15 offset:37376\n"
      "v_mfma_f32_16x16x32_bf16 v[160:163], a[192:195], a[124:127], v[160:163]\n"
      "ds_read_b64_tr_b16 a[184:185], v16 offset:32768\n"
      "v_mfma_f32_16x16x32_bf16 v[160:163], a[196:199], a[136:139], v[160:163]\n"
      "ds_read_b64_tr_b16 a[186:187], v16 offset:33280\n"
      "v_mfma_f32_16x16x32_bf16 v[164:167], a[192:195], a[128:131], v[164:167]\n"
      "ds_read_b64_tr_b16 a[188:189], v16 offset:36864\n"
      "v_mfma_f32_16x16x32_bf16 v[164:167], a[196:199], a[140:143], v[164:167]\n"
      "ds_read_b64_tr_b16 a[190:191], v16 offset:37376\n"
      "s_waitcnt lgkmcnt(10)\n"
      "v_mfma_f32_16x16x32_bf16 v[120:123], a[168:171], a[144:147], v[120:123]\n"
      "ds_read_b64_tr_b16 a[192:193], v17 offset:32768\n"
      "s_waitcnt lgkmcnt(9)\n"
      "v_mfma_f32_16x16x32_bf16 v[120:123], a[172:175], a[156:159], v[120:123]\n"
      "ds_read_b64_tr_b16 a[194:195], v17 offset:33280\n"
      "v_mfma_f32_16x16x32_bf16 v[124:127], a[168:171], a[148:151], v[124:127]\n"
      "ds_read_b64_tr_b16 a[196:197], v17 offset:36864\n"
      "v_mfma_f32_16x16x32_bf16 v[124:127], a[172:175], a[160:163], v[124:127]\n"
      "ds_read_b64_tr_b16 a[198:199], v17 offset:37376\n"
      "v_mfma_f32_16x16x32_bf16 v[128:131], a[168:171], a[152:155], v[128:131]\n"
      "v_mfma_f32_16x16x32_bf16 v[128:131], a[172:175], a[164:167], v[128:131]\n"
      "s_waitcnt lgkmcnt(10)\n"
      "v_mfma_f32_16x16x32_bf16 v[132:135], a[176:179], a[144:147], v[132:135]\n"
      "s_waitcnt lgkmcnt(8)\n"
      "v_mfma_f32_16x16x32_bf16 v[132:135], a[180:183], a[156:159], v[132:135]\n"
      "v_mfma_f32_16x16x32_bf16 v[136:139], a[176:179], a[148:151], v[136:139]\n"
      "v_mfma_f32_16x16x32_bf16 v[136:139], a[180:183], a[160:163], v[136:139]\n"
      "v_mfma_f32_16x16x32_bf16 v[140:143], a[176:179], a[152:155], v[140:143]\n"
      "v_mfma_f32_16x16x32_bf16 v[140:143], a[180:183], a[164:167], v[140:143]\n"
      "s_branch 4f\n"
      "3:\n"
      "s_waitcnt vmcnt(8)\n"
      "s_barrier\n"
      "ds_read_b64_tr_b16 a[168:169], v14 offset:49152\n"
      "ds_read_b64_tr_b16 a[170:171], v14 offset:49664\n"
      "ds_read_b64_tr_b16 a[172:173], v14 offset:53248\n"
      "ds_read_b64_tr_b16 a[174:175], v14 offset:53760\n"
      "v_add_u32 v28, 0x24000, v28\n"
      "ds_read_b128 a[0:3], v28\n"
      "v_add_u32 v29, 0x24000, v29\n"
      "ds_read_b128 a[12:15], v29\n"
      "ds_read_b128 a[4:7], v28 offset:2048\n"
      "ds_read_b128 a[16:19], v29 offset:2048\n"
      "ds_read_b128 a[8:11], v28 offset:4096\n"
      "ds_read_b128 a[20:23], v29 offset:4096\n"
      "ds_read_b64_tr_b16 a[176:177], v15 offset:49152\n"
      "ds_read_b64_tr_b16 a[178:179], v15 offset:49664\n"
      "ds_read_b64_tr_b16 a[180:181], v15 offset:53248\n"
      "ds_read_b64_tr_b16 a[182:183], v15 offset:53760\n"
      "ds_read_b64_tr_b16 a[184:185], v16 offset:49152\n"
      "ds_read_b64_tr_b16 a[186:187], v16 offset:49664\n"
      "ds_read_b64_tr_b16 a[188:189], v16 offset:53248\n"
      "ds_read_b64_tr_b16 a[190:191], v16 offset:53760\n"
      "ds_read_b64_tr_b16 a[192:193], v17 offset:49152\n"
      "ds_read_b64_tr_b16 a[194:195], v17 offset:49664\n"
      "ds_read_b64_tr_b16 a[196:197], v17 offset:53248\n"
      "ds_read_b64_tr_b16 a[198:199], v17 offset:53760\n"
      "s_waitcnt lgkmcnt(15)\n"
      "v_mfma_f32_16x16x32_bf16 v[120:123], a[168:171], a[0:3], 0\n"
      "s_add_u32 m0, s84, 0x9000\n"
      "v_mfma_f32_16x16x32_bf16 v[120:123], a[172:175], a[12:15], v[120:123]\n"
      "buffer_load_dwordx4 v196, s[8:11], s58 offen lds\n"
      "v_mfma_f32_16x16x32_bf16 v[124:127], a[168:171], a[4:7], 0\n"
      "buffer_load_dwordx4 v197, s[8:11], s58 offen offset:1024 lds\n"
      "s_waitcnt lgkmcnt(14)\n"
      "v_mfma_f32_16x16x32_bf16 v[124:127], a[172:175], a[16:19], v[124:127]\n"
      "buffer_load_dwordx4 v198, s[8:11], s58 offen offset:2048 lds\n"
      "s_add_u32 s88, s65, s70\n"
      "s_add_u32 m0, s85, 0x20000\n"
      "s_waitcnt lgkmcnt(13)\n"
      "v_mfma_f32_16x16x32_bf16 v[128:131], a[168:171], a[8:11], 0\n"
      "buffer_load_dwordx4 v192, s[20:23], s88 offen lds\n"
      "s_waitcnt lgkmcnt(12)\n"
      "v_mfma_f32_16x16x32_bf16 v[128:131], a[172:175], a[20:23], v[128:131]\n"
      "buffer_load_dwordx4 v193, s[20:23], s88 offen offset:1024 lds\n"
      "s_waitcnt lgkmcnt(10)\n"
      "v_mfma_f32_16x16x32_bf16 v[132:135], a[176:179], a[0:3], 0\n"
      "buffer_load_dwordx4 v194, s[20:23], s88 offen offset:2048 lds\n"
      "s_waitcnt lgkmcnt(8)\n"
      "v_mfma_f32_16x16x32_bf16 v[132:135], a[180:183], a[12:15], v[132:135]\n"
      "buffer_load_dwordx4 v195, s[20:23], s88 offen offset:3072 lds\n"
      "s_add_u32 m0, s84, 0xc000\n"
      "v_mfma_f32_16x16x32_bf16 v[136:139], a[176:179], a[4:7], 0\n"
      "buffer_load_dwordx4 v196, s[8:11], s59 offen lds\n"
      "v_mfma_f32_16x16x32_bf16 v[136:139], a[180:183], a[16:19], v[136:139]\n"
      "buffer_load_dwordx4 v197, s[8:11], s59 offen offset:1024 lds\n"
      "v_mfma_f32_16x16x32_bf16 v[140:143], a[176:179], a[8:11], 0\n"
      "buffer_load_dwordx4 v198, s[8:11], s59 offen offset:2048 lds\n"
      "v_mfma_f32_16x16x32_bf16 v[140:143], a[180:183], a[20:23], v[140:143]\n"
      "s_waitcnt vmcnt(10) lgkmcnt(0)\n"
      "s_barrier\n"
      "v_mfma_f32_16x16x32_bf16 v[144:147], a[184:187], a[0:3], 0\n"
      "ds_read_b64_tr_b16 a[168:169], v200\n"
      "v_add_u32 v28, 0xfffe2000, v28\n"
      "ds_read_b128 a[24:27], v28\n"
      "s_add_u32 s88, s66, s70\n"
      "s_add_u32 m0, s85, 0x24000\n"
      "v_mfma_f32_16x16x32_bf16 v[144:147], a[188:191], a[12:15], v[144:147]\n"
      "ds_read_b64_tr_b16 a[170:171], v200 offset:512\n"
      "v_add_u32 v29, 0xfffe2000, v29\n"
      "ds_read_b128 a[36:39], v29\n"
      "buffer_load_dwordx4 v192, s[20:23], s88 offen lds\n"
      "v_mfma_f32_16x16x32_bf16 v[148:151], a[184:187], a[4:7], 0\n"
      "ds_read_b64_tr_b16 a[172:173], v200 offset:4096\n"
      "ds_read_b128 a[28:31], v28 offset:2048\n"
      "v_mfma_f32_16x16x32_bf16 v[148:151], a[188:191], a[16:19], v[148:151]\n"
      "ds_read_b64_tr_b16 a[174:175], v200 offset:4608\n"
      "ds_read_b128 a[40:43], v29 offset:2048\n"
      "buffer_load_dwordx4 v193, s[20:23], s88 offen offset:1024 lds\n"
      "v_mfma_f32_16x16x32_bf16 v[152:155], a[184:187], a[8:11], 0\n"
      "ds_read_b64_tr_b16 a[176:177], v201\n"
      "ds_read_b128 a[32:35], v28 offset:4096\n"
      "v_mfma_f32_16x16x32_bf16 v[152:155], a[188:191], a[20:23], v[152:155]\n"
      "ds_read_b64_tr_b16 a[178:179], v201 offset:512\n"
      "ds_read_b128 a[44:47], v29 offset:4096\n"
      "buffer_load_dwordx4 v194, s[20:23], s88 offen offset:2048 lds\n"
      "v_mfma_f32_16x16x32_bf16 v[156:159], a[192:195], a[0:3], 0\n"
      "ds_read_b64_tr_b16 a[180:181], v201 offset:4096\n"
      "v_mfma_f32_16x16x32_bf16 v[156:159], a[196:199], a[12:15], v[156:159]\n"
      "ds_read_b64_tr_b16 a[182:183], v201 offset:4608\n"
      "buffer_load_dwordx4 v195, s[20:23], s88 offen offset:3072 lds\n"
      "v_mfma_f32_16x16x32_bf16 v[160:163], a[192:195], a[4:7], 0\n"
      "ds_read_b64_tr_b16 a[184:185], v202\n"
      "s_add_u32 s88, s67, s70\n"
      "s_add_u32 m0, s85, 0x18000\n"
      "v_mfma_f32_16x16x32_bf16 v[160:163], a[196:199], a[16:19], v[160:163]\n"
      "ds_read_b64_tr_b16 a[186:187], v202 offset:512\n"
      "buffer_load_dwordx4 v192, s[20:23], s88 offen lds\n"
      "v_mfma_f32_16x16x32_bf16 v[164:167], a[192:195], a[8:11], 0\n"
      "ds_read_b64_tr_b16 a[188:189], v202 offset:4096\n"
      "v_mfma_f32_16x16x32_bf16 v[164:167], a[196:199], a[20:23], v[164:167]\n"
      "ds_read_b64_tr_b16 a[190:191], v202 offset:4608\n"
      "buffer_load_dwordx4 v193, s[20:23], s88 offen offset:1024 lds\n"
      "s_waitcnt lgkmcnt(15)\n"
      "v_mfma_f32_16x16x32_bf16 v[120:123], a[168:171], a[24:27], v[120:123]\n"
      "ds_read_b64_tr_b16 a[192:193], v203\n"
      "s_waitcnt lgkmcnt(12)\n"
      "v_mfma_f32_16x16x32_bf16 v[120:123], a[172:175], a[36:39], v[120:123]\n"
      "ds_read_b64_tr_b16 a[194:195], v203 offset:512\n"
      "buffer_load_dwordx4 v194, s[20:23], s88 offen offset:2048 lds\n"
      "v_mfma_f32_16x16x32_bf16 v[124:127], a[168:171], a[28:31], v[124:127]\n"
      "ds_read_b64_tr_b16 a[196:197], v203 offset:4096\n"
      "s_waitcnt lgkmcnt(13)\n"
      "v_mfma_f32_16x16x32_bf16 v[124:127], a[172:175], a[40:43], v[124:127]\n"
      "ds_read_b64_tr_b16 a[198:199], v203 offset:4608\n"
      "buffer_load_dwordx4 v195, s[20:23], s88 offen offset:3072 lds\n"
      "s_waitcnt lgkmcnt(12)\n"
      "v_mfma_f32_16x16x32_bf16 v[128:131], a[168:171], a[32:35], v[128:131]\n"
      "s_add_u32 m0, s84, 0xf000\n"
      "s_waitcnt lgkmcnt(10)\n"
      "v_mfma_f32_16x16x32_bf16 v[128:131], a[172:175], a[44:47], v[128:131]\n"
      "buffer_load_dwordx4 v196, s[8:11], s60 offen lds\n"
      "v_mfma_f32_16x16x32_bf16 v[132:135], a[176:179], a[24:27], v[132:135]\n"
      "buffer_load_dwordx4 v197, s[8:11], s60 offen offset:1024 lds\n"
      "s_waitcnt lgkmcnt(8)\n"
      "v_mfma_f32_16x16x32_bf16 v[132:135], a[180:183], a[36:39], v[132:135]\n"
      "buffer_load_dwordx4 v198, s[8:11], s60 offen offset:2048 lds\n"
      "s_add_u32 m0, s84, 0x12000\n"
      "v_mfma_f32_16x16x32_bf16 v[136:139], a[176:179], a[28:31], v[136:139]\n"
      "buffer_load_dwordx4 v196, s[8:11], s61 offen lds\n"
      "v_mfma_f32_16x16x32_bf16 v[136:139], a[180:183], a[40:43], v[136:139]\n"
      "buffer_load_dwordx4 v197, s[8:11], s61 offen offset:1024 lds\n"
      "v_mfma_f32_16x16x32_bf16 v[140:143], a[176:179], a[32:35], v[140:143]\n"
      "buffer_load_dwordx4 v198, s[8:11], s61 offen offset:2048 lds\n"
      "v_mfma_f32_16x16x32_bf16 v[140:143], a[180:183], a[44:47], v[140:143]\n"
      "s_waitcnt vmcnt(17) lgkmcnt(0)\n"
      "s_barrier\n"
      "v_mfma_f32_16x16x32_bf16 v[144:147], a[184:187], a[24:27], v[144:147]\n"
      "ds_read_b64_tr_b16 a[168:169], v200 offset:16384\n"
      "ds_read_b128 a[48:51], v28 offset:12288\n"
      "s_add_u32 s88, s68, s70\n"
      "s_add_u32 m0, s85, 0x1c000\n"
      "v_mfma_f32_16x16x32_bf16 v[144:147], a[188:191], a[36:39], v[144:147]\n"
      "ds_read_b64_tr_b16 a[170:171], v200 offset:16896\n"
      "ds_read_b128 a[60:63], v29 offset:12288\n"
      "buffer_load_dwordx4 v192, s[20:23], s88 offen lds\n"
      "v_mfma_f32_16x16x32_bf16 v[148:151], a[184:187], a[28:31], v[148:151]\n"
      "ds_read_b64_tr_b16 a[172:173], v200 offset:20480\n"
      "ds_read_b128 a[52:55], v28 offset:14336\n"
      "v_mfma_f32_16x16x32_bf16 v[148:151], a[188:191], a[40:43], v[148:151]\n"
      "ds_read_b64_tr_b16 a[174:175], v200 offset:20992\n"
      "ds_read_b128 a[64:67], v29 offset:14336\n"
      "buffer_load_dwordx4 v193, s[20:23], s88 offen offset:1024 lds\n"
      "v_mfma_f32_16x16x32_bf16 v[152:155], a[184:187], a[32:35], v[152:155]\n"
      "ds_read_b64_tr_b16 a[176:177], v201 offset:16384\n"
      "ds_read_b128 a[56:59], v28 offset:16384\n"
      "v_mfma_f32_16x16x32_bf16 v[152:155], a[188:191], a[44:47], v[152:155]\n"
      "ds_read_b64_tr_b16 a[178:179], v201 offset:16896\n"
      "ds_read_b128 a[68:71], v29 offset:16384\n"
      "buffer_load_dwordx4 v194, s[20:23], s88 offen offset:2048 lds\n"
      "v_mfma_f32_16x16x32_bf16 v[156:159], a[192:195], a[24:27], v[156:159]\n"
      "ds_read_b64_tr_b16 a[180:181], v201 offset:20480\n"
      "v_mfma_f32_16x16x32_bf16 v[156:159], a[196:199], a[36:39], v[156:159]\n"
      "ds_read_b64_tr_b16 a[182:183], v201 offset:20992\n"
      "buffer_load_dwordx4 v195, s[20:23], s88 offen offset:3072 lds\n"
      "v_mfma_f32_16x16x32_bf16 v[160:163], a[192:195], a[28:31], v[160:163]\n"
      "ds_read_b64_tr_b16 a[184:185], v202 offset:16384\n"
      "s_add_u32 m0, s84, 0x15000\n"
      "v_mfma_f32_16x16x32_bf16 v[160:163], a[196:199], a[40:43], v[160:163]\n"
      "ds_read_b64_tr_b16 a[186:187], v202 offset:16896\n"
      "buffer_load_dwordx4 v196, s[8:11], s62 offen lds\n"
      "v_mfma_f32_16x16x32_bf16 v[164:167], a[192:195], a[32:35], v[164:167]\n"
      "ds_read_b64_tr_b16 a[188:189], v202 offset:20480\n"
      "v_mfma_f32_16x16x32_bf16 v[164:167], a[196:199], a[44:47], v[164:167]\n"
      "ds_read_b64_tr_b16 a[190:191], v202 offset:20992\n"
      "buffer_load_dwordx4 v197, s[8:11], s62 offen offset:1024 lds\n"
      "s_waitcnt lgkmcnt(15)\n"
      "v_mfma_f32_16x16x32_bf16 v[120:123], a[168:171], a[48:51], v[120:123]\n"
      "ds_read_b64_tr_b16 a[192:193], v203 offset:16384\n"
      "s_waitcnt lgkmcnt(12)\n"
      "v_mfma_f32_16x16x32_bf16 v[120:123], a[172:175], a[60:63], v[120:123]\n"
      "ds_read_b64_tr_b16 a[194:195], v203 offset:16896\n"
      "buffer_load_dwordx4 v198, s[8:11], s62 offen offset:2048 lds\n"
      "v_mfma_f32_16x16x32_bf16 v[124:127], a[168:171], a[52:55], v[124:127]\n"
      "ds_read_b64_tr_b16 a[196:197], v203 offset:20480\n"
      "s_add_u32 s89, s70, 0\n"
      "s_add_u32 m0, s86, 0\n"
      "s_waitcnt lgkmcnt(13)\n"
      "v_mfma_f32_16x16x32_bf16 v[124:127], a[172:175], a[64:67], v[124:127]\n"
      "ds_read_b64_tr_b16 a[198:199], v203 offset:20992\n"
      "buffer_load_dwordx4 v204, s[12:15], s89 offen nt lds\n"
      "s_waitcnt lgkmcnt(12)\n"
      "v_mfma_f32_16x16x32_bf16 v[128:131], a[168:171], a[56:59], v[128:131]\n"
      "s_waitcnt lgkmcnt(10)\n"
      "v_mfma_f32_16x16x32_bf16 v[128:131], a[172:175], a[68:71], v[128:131]\n"
      "buffer_load_dwordx4 v205, s[12:15], s89 offen offset:1024 nt lds\n"
      "v_mfma_f32_16x16x32_bf16 v[132:135], a[176:179], a[48:51], v[132:135]\n"
      "s_add_u32 s89, s70, 0x4000\n"
      "s_add_u32 m0, s86, 0x2000\n"
      "s_waitcnt lgkmcnt(8)\n"
      "v_mfma_f32_16x16x32_bf16 v[132:135], a[180:183], a[60:63], v[132:135]\n"
      "buffer_load_dwordx4 v204, s[12:15], s89 offen nt lds\n"
      "v_mfma_f32_16x16x32_bf16 v[136:139], a[176:179], a[52:55], v[136:139]\n"
      "buffer_load_dwordx4 v205, s[12:15], s89 offen offset:1024 nt lds\n"
      "s_add_u32 s89, s70, 0x8000\n"
      "s_add_u32 m0, s86, 0x4000\n"
      "v_mfma_f32_16x16x32_bf16 v[136:139], a[180:183], a[64:67], v[136:139]\n"
      "buffer_load_dwordx4 v204, s[12:15], s89 offen nt lds\n"
      "v_mfma_f32_16x16x32_bf16 v[140:143], a[176:179], a[56:59], v[140:143]\n"
      "buffer_load_dwordx4 v205, s[12:15], s89 offen offset:1024 nt lds\n"
      "v_mfma_f32_16x16x32_bf16 v[140:143], a[180:183], a[68:71], v[140:143]\n"
      "s_waitcnt vmcnt(23) lgkmcnt(0)\n"
      "s_barrier\n"
      "v_mfma_f32_16x16x32_bf16 v[144:147], a[184:187], a[48:51], v[144:147]\n"
      "ds_read_b64_tr_b16 a[168:169], v200 offset:32768\n"
      "ds_read_b128 a[72:75], v28 offset:24576\n"
      "v_mfma_f32_16x16x32_bf16 v[144:147], a[188:191], a[60:63], v[144:147]\n"
      "ds_read_b64_tr_b16 a[170:171], v200 offset:33280\n"
      "ds_read_b128 a[84:87], v29 offset:24576\n"
      "v_mfma_f32_16x16x32_bf16 v[148:151], a[184:187], a[52:55], v[148:151]\n"
      "ds_read_b64_tr_b16 a[172:173], v200 offset:36864\n"
      "ds_read_b128 a[76:79], v28 offset:26624\n"
      "v_mfma_f32_16x16x32_bf16 v[148:151], a[188:191], a[64:67], v[148:151]\n"
      "ds_read_b64_tr_b16 a[174:175], v200 offset:37376\n"
      "ds_read_b128 a[88:91], v29 offset:26624\n"
      "v_mfma_f32_16x16x32_bf16 v[152:155], a[184:187], a[56:59], v[152:155]\n"
      "ds_read_b64_tr_b16 a[176:177], v201 offset:32768\n"
      "ds_read_b128 a[80:83], v28 offset:28672\n"
      "v_mfma_f32_16x16x32_bf16 v[152:155], a[188:191], a[68:71], v[152:155]\n"
      "ds_read_b64_tr_b16 a[178:179], v201 offset:33280\n"
      "ds_read_b128 a[92:95], v29 offset:28672\n"
      "v_mfma_f32_16x16x32_bf16 v[156:159], a[192:195], a[48:51], v[156:159]\n"
      "ds_read_b64_tr_b16 a[180:181], v201 offset:36864\n"
      "v_mfma_f32_16x16x32_bf16 v[156:159], a[196:199], a[60:63], v[156:159]\n"
      "ds_read_b64_tr_b16 a[182:183], v201 offset:37376\n"
      "v_mfma_f32_16x16x32_bf16 v[160:163], a[192:195], a[52:55], v[160:163]\n"
      "ds_read_b64_tr_b16 a[184:185], v202 offset:32768\n"
      "v_mfma_f32_16x16x32_bf16 v[160:163], a[196:199], a[64:67], v[160:163]\n"
      "ds_read_b64_tr_b16 a[186:187], v202 offset:33280\n"
      "v_mfma_f32_16x16x32_bf16 v[164:167], a[192:195], a[56:59], v[164:167]\n"
      "ds_read_b64_tr_b16 a[188:189], v202 offset:36864\n"
      "v_mfma_f32_16x16x32_bf16 v[164:167], a[196:199], a[68:71], v[164:167]\n"
      "ds_read_b64_tr_b16 a[190:191], v202 offset:37376\n"
      "s_waitcnt lgkmcnt(15)\n"
      "v_mfma_f32_16x16x32_bf16 v[120:123], a[168:171], a[72:75], v[120:123]\n"
      "ds_read_b64_tr_b16 a[192:193], v203 offset:32768\n"
      "s_waitcnt lgkmcnt(12)\n"
      "v_mfma_f32_16x16x32_bf16 v[120:123], a[172:175], a[84:87], v[120:123]\n"
      "ds_read_b64_tr_b16 a[194:195], v203 offset:33280\n"
      "v_mfma_f32_16x16x32_bf16 v[124:127], a[168:171], a[76:79], v[124:127]\n"
      "ds_read_b64_tr_b16 a[196:197], v203 offset:36864\n"
      "s_waitcnt lgkmcnt(13)\n"
      "v_mfma_f32_16x16x32_bf16 v[124:127], a[172:175], a[88:91], v[124:127]\n"
      "ds_read_b64_tr_b16 a[198:199], v203 offset:37376\n"
      "s_add_u32 s88, s69, s70\n"
      "s_add_u32 m0, s85, 0x20000\n"
      "s_waitcnt lgkmcnt(12)\n"
      "v_mfma_f32_16x16x32_bf16 v[128:131], a[168:171], a[80:83], v[128:131]\n"
      "buffer_load_dwordx4 v192, s[20:23], s88 offen lds\n"
      "s_waitcnt lgkmcnt(10)\n"
      "v_mfma_f32_16x16x32_bf16 v[128:131], a[172:175], a[92:95], v[128:131]\n"
      "v_mfma_f32_16x16x32_bf16 v[132:135], a[176:179], a[72:75], v[132:135]\n"
      "buffer_load_dwordx4 v193, s[20:23], s88 offen offset:1024 lds\n"
      "s_waitcnt lgkmcnt(8)\n"
      "v_mfma_f32_16x16x32_bf16 v[132:135], a[180:183], a[84:87], v[132:135]\n"
      "v_mfma_f32_16x16x32_bf16 v[136:139], a[176:179], a[76:79], v[136:139]\n"
      "buffer_load_dwordx4 v194, s[20:23], s88 offen offset:2048 lds\n"
      "v_mfma_f32_16x16x32_bf16 v[136:139], a[180:183], a[88:91], v[136:139]\n"
      "v_mfma_f32_16x16x32_bf16 v[140:143], a[176:179], a[80:83], v[140:143]\n"
      "buffer_load_dwordx4 v195, s[20:23], s88 offen offset:3072 lds\n"
      "v_mfma_f32_16x16x32_bf16 v[140:143], a[180:183], a[92:95], v[140:143]\n"
      "s_waitcnt vmcnt(20) lgkmcnt(0)\n"
      "s_barrier\n"
      "v_mfma_f32_16x16x32_bf16 v[144:147], a[184:187], a[72:75], v[144:147]\n"
      "ds_read_b64_tr_b16 a[168:169], v14 offset:49152\n"
      "ds_read_b128 a[96:99], v28 offset:36864\n"
      "v_mfma_f32_16x16x32_bf16 v[144:147], a[188:191], a[84:87], v[144:147]\n"
      "ds_read_b64_tr_b16 a[170:171], v14 offset:49664\n"
      "ds_read_b128 a[108:111], v29 offset:36864\n"
      "v_mfma_f32_16x16x32_bf16 v[148:151], a[184:187], a[76:79], v[148:151]\n"
      "ds_read_b64_tr_b16 a[172:173], v14 offset:53248\n"
      "ds_read_b128 a[100:103], v28 offset:38912\n"
      "v_mfma_f32_16x16x32_bf16 v[148:151], a[188:191], a[88:91], v[148:151]\n"
      "ds_read_b64_tr_b16 a[174:175], v14 offset:53760\n"
      "ds_read_b128 a[112:115], v29 offset:38912\n"
      "v_mfma_f32_16x16x32_bf16 v[152:155], a[184:187], a[80:83], v[152:155]\n"
      "ds_read_b64_tr_b16 a[176:177], v15 offset:49152\n"
      "ds_read_b128 a[104:107], v28 offset:40960\n"
      "v_mfma_f32_16x16x32_bf16 v[152:155], a[188:191], a[92:95], v[152:155]\n"
      "ds_read_b64_tr_b16 a[178:179], v15 offset:49664\n"
      "ds_read_b128 a[116:119], v29 offset:40960\n"
      "v_mfma_f32_16x16x32_bf16 v[156:159], a[192:195], a[72:75], v[156:159]\n"
      "ds_read_b64_tr_b16 a[180:181], v15 offset:53248\n"
      "v_mfma_f32_16x16x32_bf16 v[156:159], a[196:199], a[84:87], v[156:159]\n"
      "ds_read_b64_tr_b16 a[182:183], v15 offset:53760\n"
      "v_mfma_f32_16x16x32_bf16 v[160:163], a[192:195], a[76:79], v[160:163]\n"
      "ds_read_b64_tr_b16 a[184:185], v16 offset:49152\n"
      "v_mfma_f32_16x16x32_bf16 v[160:163], a[196:199], a[88:91], v[160:163]\n"
      "ds_read_b64_tr_b16 a[186:187], v16 offset:49664\n"
      "v_mfma_f32_16x16x32_bf16 v[164:167], a[192:195], a[80:83], v[164:167]\n"
      "ds_read_b64_tr_b16 a[188:189], v16 offset:53248\n"
      "v_mfma_f32_16x16x32_bf16 v[164:167], a[196:199], a[92:95], v[164:167]\n"
      "ds_read_b64_tr_b16 a[190:191], v16 offset:53760\n"
      "s_waitcnt lgkmcnt(15)\n"
      "v_mfma_f32_16x16x32_bf16 v[120:123], a[168:171], a[96:99], v[120:123]\n"
      "ds_read_b64_tr_b16 a[192:193], v17 offset:49152\n"
      "s_waitcnt lgkmcnt(12)\n"
      "v_mfma_f32_16x16x32_bf16 v[120:123], a[172:175], a[108:111], v[120:123]\n"
      "ds_read_b64_tr_b16 a[194:195], v17 offset:49664\n"
      "v_mfma_f32_16x16x32_bf16 v[124:127], a[168:171], a[100:103], v[124:127]\n"
      "ds_read_b64_tr_b16 a[196:197], v17 offset:53248\n"
      "s_waitcnt lgkmcnt(13)\n"
      "v_mfma_f32_16x16x32_bf16 v[124:127], a[172:175], a[112:115], v[124:127]\n"
      "ds_read_b64_tr_b16 a[198:199], v17 offset:53760\n"
      "s_add_u32 s88, s63, s71\n"
      "s_add_u32 m0, s85, 0x24000\n"
      "s_waitcnt lgkmcnt(12)\n"
      "v_mfma_f32_16x16x32_bf16 v[128:131], a[168:171], a[104:107], v[128:131]\n"
      "buffer_load_dwordx4 v192, s[20:23], s88 offen lds\n"
      "s_waitcnt lgkmcnt(10)\n"
      "v_mfma_f32_16x16x32_bf16 v[128:131], a[172:175], a[116:119], v[128:131]\n"
      "v_mfma_f32_16x16x32_bf16 v[132:135], a[176:179], a[96:99], v[132:135]\n"
      "buffer_load_dwordx4 v193, s[20:23], s88 offen offset:1024 lds\n"
      "s_waitcnt lgkmcnt(8)\n"
      "v_mfma_f32_16x16x32_bf16 v[132:135], a[180:183], a[108:111], v[132:135]\n"
      "v_mfma_f32_16x16x32_bf16 v[136:139], a[176:179], a[100:103], v[136:139]\n"
      "buffer_load_dwordx4 v194, s[20:23], s88 offen offset:2048 lds\n"
      "v_mfma_f32_16x16x32_bf16 v[136:139], a[180:183], a[112:115], v[136:139]\n"
      "v_mfma_f32_16x16x32_bf16 v[140:143], a[176:179], a[104:107], v[140:143]\n"
      "buffer_load_dwordx4 v195, s[20:23], s88 offen offset:3072 lds\n"
      "v_mfma_f32_16x16x32_bf16 v[140:143], a[180:183], a[116:119], v[140:143]\n"
      "s_waitcnt vmcnt(17) lgkmcnt(0)\n"
      "s_barrier\n"
      "v_mfma_f32_16x16x32_bf16 v[144:147], a[184:187], a[96:99], v[144:147]\n"
      "ds_read_b64_tr_b16 a[168:169], v200\n"
      "ds_read_b128 a[120:123], v28 offset:49152\n"
      "v_mfma_f32_16x16x32_bf16 v[144:147], a[188:191], a[108:111], v[144:147]\n"
      "ds_read_b64_tr_b16 a[170:171], v200 offset:512\n"
      "ds_read_b128 a[132:135], v29 offset:49152\n"
      "v_mfma_f32_16x16x32_bf16 v[148:151], a[184:187], a[100:103], v[148:151]\n"
      "ds_read_b64_tr_b16 a[172:173], v200 offset:4096\n"
      "ds_read_b128 a[124:127], v28 offset:51200\n"
      "v_mfma_f32_16x16x32_bf16 v[148:151], a[188:191], a[112:115], v[148:151]\n"
      "ds_read_b64_tr_b16 a[174:175], v200 offset:4608\n"
      "ds_read_b128 a[136:139], v29 offset:51200\n"
      "v_mfma_f32_16x16x32_bf16 v[152:155], a[184:187], a[104:107], v[152:155]\n"
      "ds_read_b64_tr_b16 a[176:177], v201\n"
      "ds_read_b128 a[128:131], v28 offset:53248\n"
      "v_mfma_f32_16x16x32_bf16 v[152:155], a[188:191], a[116:119], v[152:155]\n"
      "ds_read_b64_tr_b16 a[178:179], v201 offset:512\n"
      "ds_read_b128 a[140:143], v29 offset:53248\n"
      "v_mfma_f32_16x16x32_bf16 v[156:159], a[192:195], a[96:99], v[156:159]\n"
      "ds_read_b64_tr_b16 a[180:181], v201 offset:4096\n"
      "v_mfma_f32_16x16x32_bf16 v[156:159], a[196:199], a[108:111], v[156:159]\n"
      "ds_read_b64_tr_b16 a[182:183], v201 offset:4608\n"
      "s_add_u32 s88, s64, s71\n"
      "s_add_u32 m0, s85, 0x18000\n"
      "v_mfma_f32_16x16x32_bf16 v[160:163], a[192:195], a[100:103], v[160:163]\n"
      "ds_read_b64_tr_b16 a[184:185], v202\n"
      "buffer_load_dwordx4 v192, s[20:23], s88 offen lds\n"
      "v_mfma_f32_16x16x32_bf16 v[160:163], a[196:199], a[112:115], v[160:163]\n"
      "ds_read_b64_tr_b16 a[186:187], v202 offset:512\n"
      "v_mfma_f32_16x16x32_bf16 v[164:167], a[192:195], a[104:107], v[164:167]\n"
      "ds_read_b64_tr_b16 a[188:189], v202 offset:4096\n"
      "buffer_load_dwordx4 v193, s[20:23], s88 offen offset:1024 lds\n"
      "v_mfma_f32_16x16x32_bf16 v[164:167], a[196:199], a[116:119], v[164:167]\n"
      "ds_read_b64_tr_b16 a[190:191], v202 offset:4608\n"
      "s_waitcnt lgkmcnt(15)\n"
      "v_mfma_f32_16x16x32_bf16 v[120:123], a[168:171], a[120:123], v[120:123]\n"
      "ds_read_b64_tr_b16 a[192:193], v203\n"
      "buffer_load_dwordx4 v194, s[20:23], s88 offen offset:2048 lds\n"
      "s_waitcnt lgkmcnt(12)\n"
      "v_mfma_f32_16x16x32_bf16 v[120:123], a[172:175], a[132:135], v[120:123]\n"
      "ds_read_b64_tr_b16 a[194:195], v203 offset:512\n"
      "v_mfma_f32_16x16x32_bf16 v[124:127], a[168:171], a[124:127], v[124:127]\n"
      "ds_read_b64_tr_b16 a[196:197], v203 offset:4096\n"
      "buffer_load_dwordx4 v195, s[20:23], s88 offen offset:3072 lds\n"
      "s_waitcnt lgkmcnt(13)\n"
      "v_mfma_f32_16x16x32_bf16 v[124:127], a[172:175], a[136:139], v[124:127]\n"
      "ds_read_b64_tr_b16 a[198:199], v203 offset:4608\n"
      "s_add_u32 s88, s65, s71\n"
      "s_add_u32 m0, s85, 0xc000\n"
      "s_waitcnt lgkmcnt(12)\n"
      "v_mfma_f32_16x16x32_bf16 v[128:131], a[168:171], a[128:131], v[128:131]\n"
      "buffer_load_dwordx4 v192, s[20:23], s88 offen lds\n"
      "s_waitcnt lgkmcnt(10)\n"
      "v_mfma_f32_16x16x32_bf16 v[128:131], a[172:175], a[140:143], v[128:131]\n"
      "v_mfma_f32_16x16x32_bf16 v[132:135], a[176:179], a[120:123], v[132:135]\n"
      "buffer_load_dwordx4 v193, s[20:23], s88 offen offset:1024 lds\n"
      "s_waitcnt lgkmcnt(8)\n"
      "v_mfma_f32_16x16x32_bf16 v[132:135], a[180:183], a[132:135], v[132:135]\n"
      "v_mfma_f32_16x16x32_bf16 v[136:139], a[176:179], a[124:127], v[136:139]\n"
      "buffer_load_dwordx4 v194, s[20:23], s88 offen offset:2048 lds\n"
      "v_mfma_f32_16x16x32_bf16 v[136:139], a[180:183], a[136:139], v[136:139]\n"
      "v_mfma_f32_16x16x32_bf16 v[140:143], a[176:179], a[128:131], v[140:143]\n"
      "buffer_load_dwordx4 v195, s[20:23], s88 offen offset:3072 lds\n"
      "v_mfma_f32_16x16x32_bf16 v[140:143], a[180:183], a[140:143], v[140:143]\n"
      "s_waitcnt vmcnt(12) lgkmcnt(0)\n"
      "s_barrier\n"
      "v_mfma_f32_16x16x32_bf16 v[144:147], a[184:187], a[120:123], v[144:147]\n"
      "ds_read_b64_tr_b16 a[168:169], v200 offset:16384\n"
      "ds_read_b128 a[144:147], v28 offset:61440\n"
      "v_mfma_f32_16x16x32_bf16 v[144:147], a[188:191], a[132:135], v[144:147]\n"
      "ds_read_b64_tr_b16 a[170:171], v200 offset:16896\n"
      "ds_read_b128 a[156:159], v29 offset:61440\n"
      "v_mfma_f32_16x16x32_bf16 v[148:151], a[184:187], a[124:127], v[148:151]\n"
      "ds_read_b64_tr_b16 a[172:173], v200 offset:20480\n"
      "ds_read_b128 a[148:151], v28 offset:63488\n"
      "v_mfma_f32_16x16x32_bf16 v[148:151], a[188:191], a[136:139], v[148:151]\n"
      "ds_read_b64_tr_b16 a[174:175], v200 offset:20992\n"
      "ds_read_b128 a[160:163], v29 offset:63488\n"
      "v_mfma_f32_16x16x32_bf16 v[152:155], a[184:187], a[128:131], v[152:155]\n"
      "ds_read_b64_tr_b16 a[176:177], v201 offset:16384\n"
      "v_add_u32 v28, 0x10000, v28\n"
      "ds_read_b128 a[152:155], v28\n"
      "v_mfma_f32_16x16x32_bf16 v[152:155], a[188:191], a[140:143], v[152:155]\n"
      "ds_read_b64_tr_b16 a[178:179], v201 offset:16896\n"
      "v_add_u32 v29, 0x10000, v29\n"
      "ds_read_b128 a[164:167], v29\n"
      "v_mfma_f32_16x16x32_bf16 v[156:159], a[192:195], a[120:123], v[156:159]\n"
      "ds_read_b64_tr_b16 a[180:181], v201 offset:20480\n"
      "s_add_u32 s88, s66, s71\n"
      "s_add_u32 m0, s85, 0x1c000\n"
      "v_mfma_f32_16x16x32_bf16 v[156:159], a[196:199], a[132:135], v[156:159]\n"
      "ds_read_b64_tr_b16 a[182:183], v201 offset:20992\n"
      "buffer_load_dwordx4 v192, s[20:23], s88 offen lds\n"
      "v_mfma_f32_16x16x32_bf16 v[160:163], a[192:195], a[124:127], v[160:163]\n"
      "ds_read_b64_tr_b16 a[184:185], v202 offset:16384\n"
      "v_mfma_f32_16x16x32_bf16 v[160:163], a[196:199], a[136:139], v[160:163]\n"
      "ds_read_b64_tr_b16 a[186:187], v202 offset:16896\n"
      "buffer_load_dwordx4 v193, s[20:23], s88 offen offset:1024 lds\n"
      "v_mfma_f32_16x16x32_bf16 v[164:167], a[192:195], a[128:131], v[164:167]\n"
      "ds_read_b64_tr_b16 a[188:189], v202 offset:20480\n"
      "v_mfma_f32_16x16x32_bf16 v[164:167], a[196:199], a[140:143], v[164:167]\n"
      "ds_read_b64_tr_b16 a[190:191], v202 offset:20992\n"
      "buffer_load_dwordx4 v194, s[20:23], s88 offen offset:2048 lds\n"
      "s_waitcnt lgkmcnt(15)\n"
      "v_mfma_f32_16x16x32_bf16 v[120:123], a[168:171], a[144:147], v[120:123]\n"
      "ds_read_b64_tr_b16 a[192:193], v203 offset:16384\n"
      "ds_read_b64 a[200:201], v24\n"
      "s_waitcnt lgkmcnt(13)\n"
      "v_mfma_f32_16x16x32_bf16 v[120:123], a[172:175], a[156:159], v[120:123]\n"
      "ds_read_b64_tr_b16 a[194:195], v203 offset:16896\n"
      "ds_read_b64 a[202:203], v25\n"
      "v_mfma_f32_16x16x32_bf16 v[124:127], a[168:171], a[148:151], v[124:127]\n"
      "ds_read_b64_tr_b16 a[196:197], v203 offset:20480\n"
      "ds_read_b64 a[204:205], v24 offset:2048\n"
      "buffer_load_dwordx4 v195, s[20:23], s88 offen offset:3072 lds\n"
      "s_waitcnt lgkmcnt(15)\n"
      "v_mfma_f32_16x16x32_bf16 v[124:127], a[172:175], a[160:163], v[124:127]\n"
      "ds_read_b64_tr_b16 a[198:199], v203 offset:20992\n"
      "ds_read_b64 a[206:207], v25 offset:2048\n"
      "s_add_u32 s88, s67, s71\n"
      "s_add_u32 m0, s85, 0x10000\n"
      "s_waitcnt lgkmcnt(15)\n"
      "v_mfma_f32_16x16x32_bf16 v[128:131], a[168:171], a[152:155], v[128:131]\n"
      "ds_read_b64 a[208:209], v24 offset:8192\n"
      "buffer_load_dwordx4 v192, s[20:23], s88 offen lds\n"
      "s_waitcnt lgkmcnt(15)\n"
      "v_mfma_f32_16x16x32_bf16 v[128:131], a[172:175], a[164:167], v[128:131]\n"
      "ds_read_b64 a[210:211], v25 offset:8192\n"
      "v_mfma_f32_16x16x32_bf16 v[132:135], a[176:179], a[144:147], v[132:135]\n"
      "ds_read_b64 a[212:213], v24 offset:10240\n"
      "buffer_load_dwordx4 v193, s[20:23], s88 offen offset:1024 lds\n"
      "s_waitcnt lgkmcnt(15)\n"
      "v_mfma_f32_16x16x32_bf16 v[132:135], a[180:183], a[156:159], v[132:135]\n"
      "ds_read_b64 a[214:215], v25 offset:10240\n"
      "v_mfma_f32_16x16x32_bf16 v[136:139], a[176:179], a[148:151], v[136:139]\n"
      "ds_read_b64 a[216:217], v24 offset:16384\n"
      "buffer_load_dwordx4 v194, s[20:23], s88 offen offset:2048 lds\n"
      "v_mfma_f32_16x16x32_bf16 v[136:139], a[180:183], a[160:163], v[136:139]\n"
      "ds_read_b64 a[218:219], v25 offset:16384\n"
      "v_mfma_f32_16x16x32_bf16 v[140:143], a[176:179], a[152:155], v[140:143]\n"
      "ds_read_b64 a[220:221], v24 offset:18432\n"
      "buffer_load_dwordx4 v195, s[20:23], s88 offen offset:3072 lds\n"
      "v_mfma_f32_16x16x32_bf16 v[140:143], a[180:183], a[164:167], v[140:143]\n"
      "ds_read_b64 a[222:223], v25 offset:18432\n"
      "s_waitcnt vmcnt(16) lgkmcnt(9)\n"
      "s_barrier\n"
      "v_mfma_f32_16x16x32_bf16 v[144:147], a[184:187], a[144:147], v[144:147]\n"
      "ds_read_b64_tr_b16 a[168:169], v200 offset:32768\n"
      "v_cvt_pk_bf16_f32 v220, v120, v121\n"
      "v_mfma_f32_16x16x32_bf16 v[144:147], a[188:191], a[156:159], v[144:147]\n"
      "ds_read_b64_tr_b16 a[170:171], v200 offset:33280\n"
      "v_cvt_pk_bf16_f32 v221, v122, v123\n"
      "v_mfma_f32_16x16x32_bf16 v[148:151], a[184:187], a[148:151], v[148:151]\n"
      "ds_read_b64_tr_b16 a[172:173], v200 offset:36864\n"
      "v_cvt_pk_bf16_f32 v224, v124, v125\n"
      "v_mfma_f32_16x16x32_bf16 v[148:151], a[188:191], a[160:163], v[148:151]\n"
      "ds_read_b64_tr_b16 a[174:175], v200 offset:37376\n"
      "v_cvt_pk_bf16_f32 v225, v126, v127\n"
      "v_mfma_f32_16x16x32_bf16 v[152:155], a[184:187], a[152:155], v[152:155]\n"
      "ds_read_b64_tr_b16 a[176:177], v201 offset:32768\n"
      "v_cvt_pk_bf16_f32 v228, v128, v129\n"
      "s_add_u32 s88, s68, s71\n"
      "s_add_u32 m0, s85, 0x20000\n"
      "v_mfma_f32_16x16x32_bf16 v[152:155], a[188:191], a[164:167], v[152:155]\n"
      "ds_read_b64_tr_b16 a[178:179], v201 offset:33280\n"
      "v_cvt_pk_bf16_f32 v229, v130, v131\n"
      "buffer_load_dwordx4 v192, s[20:23], s88 offen lds\n"
      "v_mfma_f32_16x16x32_bf16 v[156:159], a[192:195], a[144:147], v[156:159]\n"
      "ds_read_b64_tr_b16 a[180:181], v201 offset:36864\n"
      "v_cvt_pk_bf16_f32 v222, v132, v133\n"
      "v_mfma_f32_16x16x32_bf16 v[156:159], a[196:199], a[156:159], v[156:159]\n"
      "ds_read_b64_tr_b16 a[182:183], v201 offset:37376\n"
      "v_cvt_pk_bf16_f32 v223, v134, v135\n"
      "buffer_load_dwordx4 v193, s[20:23], s88 offen offset:1024 lds\n"
      "v_mfma_f32_16x16x32_bf16 v[160:163], a[192:195], a[148:151], v[160:163]\n"
      "ds_read_b64_tr_b16 a[184:185], v202 offset:32768\n"
      "v_cvt_pk_bf16_f32 v226, v136, v137\n"
      "v_mfma_f32_16x16x32_bf16 v[160:163], a[196:199], a[160:163], v[160:163]\n"
      "ds_read_b64_tr_b16 a[186:187], v202 offset:33280\n"
      "v_cvt_pk_bf16_f32 v227, v138, v139\n"
      "buffer_load_dwordx4 v194, s[20:23], s88 offen offset:2048 lds\n"
      "v_mfma_f32_16x16x32_bf16 v[164:167], a[192:195], a[152:155], v[164:167]\n"
      "ds_read_b64_tr_b16 a[188:189], v202 offset:36864\n"
      "v_cvt_pk_bf16_f32 v230, v140, v141\n"
      "v_mfma_f32_16x16x32_bf16 v[164:167], a[196:199], a[164:167], v[164:167]\n"
      "ds_read_b64_tr_b16 a[190:191], v202 offset:37376\n"
      "v_cvt_pk_bf16_f32 v231, v142, v143\n"
      "buffer_load_dwordx4 v195, s[20:23], s88 offen offset:3072 lds\n"
      "s_waitcnt lgkmcnt(10)\n"
      "v_mfma_f32_16x16x32_bf16 v[120:123], a[168:171], a[0:3], 0\n"
      "ds_read_b64_tr_b16 a[192:193], v203 offset:32768\n"
      "v_cvt_pk_bf16_f32 v232, v144, v145\n"
      "ds_read_b64 a[224:225], v26\n"
      "s_waitcnt lgkmcnt(10)\n"
      "v_mfma_f32_16x16x32_bf16 v[120:123], a[172:175], a[12:15], v[120:123]\n"
      "ds_read_b64_tr_b16 a[194:195], v203 offset:33280\n"
      "v_cvt_pk_bf16_f32 v233, v146, v147\n"
      "ds_read_b64 a[226:227], v27\n"
      "v_mfma_f32_16x16x32_bf16 v[124:127], a[168:171], a[4:7], 0\n"
      "ds_read_b64_tr_b16 a[196:197], v203 offset:36864\n"
      "v_cvt_pk_bf16_f32 v236, v148, v149\n"
      "ds_read_b64 a[228:229], v26 offset:2048\n"
      "v_mfma_f32_16x16x32_bf16 v[124:127], a[172:175], a[16:19], v[124:127]\n"
      "ds_read_b64_tr_b16 a[198:199], v203 offset:37376\n"
      "v_cvt_pk_bf16_f32 v237, v150, v151\n"
      "ds_read_b64 a[230:231], v27 offset:2048\n"
      "s_add_u32 s88, s69, s71\n"
      "s_add_u32 m0, s85, 0x14000\n"
      "v_mfma_f32_16x16x32_bf16 v[128:131], a[168:171], a[8:11], 0\n"
      "v_cvt_pk_bf16_f32 v240, v152, v153\n"
      "ds_read_b64 a[232:233], v26 offset:8192\n"
      "buffer_load_dwordx4 v192, s[20:23], s88 offen lds\n"
      "v_mfma_f32_16x16x32_bf16 v[128:131], a[172:175], a[20:23], v[128:131]\n"
      "v_cvt_pk_bf16_f32 v241, v154, v155\n"
      "ds_read_b64 a[234:235], v27 offset:8192\n"
      "s_waitcnt lgkmcnt(15)\n"
      "v_mfma_f32_16x16x32_bf16 v[132:135], a[176:179], a[0:3], 0\n"
      "v_cvt_pk_bf16_f32 v234, v156, v157\n"
      "ds_read_b64 a[236:237], v26 offset:10240\n"
      "buffer_load_dwordx4 v193, s[20:23], s88 offen offset:1024 lds\n"
      "s_waitcnt lgkmcnt(15)\n"
      "v_mfma_f32_16x16x32_bf16 v[132:135], a[180:183], a[12:15], v[132:135]\n"
      "v_cvt_pk_bf16_f32 v235, v158, v159\n"
      "ds_read_b64 a[238:239], v27 offset:10240\n"
      "v_mfma_f32_16x16x32_bf16 v[136:139], a[176:179], a[4:7], 0\n"
      "v_cvt_pk_bf16_f32 v238, v160, v161\n"
      "ds_read_b64 a[240:241], v26 offset:16384\n"
      "buffer_load_dwordx4 v194, s[20:23], s88 offen offset:2048 lds\n"
      "v_mfma_f32_16x16x32_bf16 v[136:139], a[180:183], a[16:19], v[136:139]\n"
      "v_cvt_pk_bf16_f32 v239, v162, v163\n"
      "ds_read_b64 a[242:243], v27 offset:16384\n"
      "v_mfma_f32_16x16x32_bf16 v[140:143], a[176:179], a[8:11], 0\n"
      "v_cvt_pk_bf16_f32 v242, v164, v165\n"
      "ds_read_b64 a[244:245], v26 offset:18432\n"
      "buffer_load_dwordx4 v195, s[20:23], s88 offen offset:3072 lds\n"
      "v_mfma_f32_16x16x32_bf16 v[140:143], a[180:183], a[20:23], v[140:143]\n"
      "v_cvt_pk_bf16_f32 v243, v166, v167\n"
      "ds_read_b64 a[246:247], v27 offset:18432\n"
      "s_waitcnt vmcnt(20) lgkmcnt(9)\n"
      "s_barrier\n"
      "v_mfma_f32_16x16x32_bf16 v[144:147], a[184:187], a[0:3], 0\n"
      "ds_read_b64_tr_b16 a[168:169], v14 offset:49152\n"
      "v_mfma_f32_16x16x32_bf16 v[144:147], a[188:191], a[12:15], v[144:147]\n"
      "ds_read_b64_tr_b16 a[170:171], v14 offset:49664\n"
      "v_mfma_f32_16x16x32_bf16 v[148:151], a[184:187], a[4:7], 0\n"
      "ds_read_b64_tr_b16 a[172:173], v14 offset:53248\n"
      "v_mfma_f32_16x16x32_bf16 v[148:151], a[188:191], a[16:19], v[148:151]\n"
      "ds_read_b64_tr_b16 a[174:175], v14 offset:53760\n"
      "v_mfma_f32_16x16x32_bf16 v[152:155], a[184:187], a[8:11], 0\n"
      "ds_read_b64_tr_b16 a[176:177], v15 offset:49152\n"
      "v_mfma_f32_16x16x32_bf16 v[152:155], a[188:191], a[20:23], v[152:155]\n"
      "ds_read_b64_tr_b16 a[178:179], v15 offset:49664\n"
      "v_mfma_f32_16x16x32_bf16 v[156:159], a[192:195], a[0:3], 0\n"
      "ds_read_b64_tr_b16 a[180:181], v15 offset:53248\n"
      "v_mfma_f32_16x16x32_bf16 v[156:159], a[196:199], a[12:15], v[156:159]\n"
      "ds_read_b64_tr_b16 a[182:183], v15 offset:53760\n"
      "v_mfma_f32_16x16x32_bf16 v[160:163], a[192:195], a[4:7], 0\n"
      "ds_read_b64_tr_b16 a[184:185], v16 offset:49152\n"
      "v_mfma_f32_16x16x32_bf16 v[160:163], a[196:199], a[16:19], v[160:163]\n"
      "ds_read_b64_tr_b16 a[186:187], v16 offset:49664\n"
      "v_mfma_f32_16x16x32_bf16 v[164:167], a[192:195], a[8:11], 0\n"
      "ds_read_b64_tr_b16 a[188:189], v16 offset:53248\n"
      "v_mfma_f32_16x16x32_bf16 v[164:167], a[196:199], a[20:23], v[164:167]\n"
      "ds_read_b64_tr_b16 a[190:191], v16 offset:53760\n"
      "v_mfma_f32_16x16x32_bf16 v[48:51], a[200:203], v[220:223], 0\n"
      "ds_read_b64_tr_b16 a[192:193], v17 offset:49152\n"
      "v_mfma_f32_16x16x32_bf16 v[48:51], a[224:227], v[232:235], v[48:51]\n"
      "ds_read_b64_tr_b16 a[194:195], v17 offset:49664\n"
      "v_mfma_f32_16x16x32_bf16 v[52:55], a[200:203], v[224:227], 0\n"
      "ds_read_b64_tr_b16 a[196:197], v17 offset:53248\n"
      "v_mfma_f32_16x16x32_bf16 v[52:55], a[224:227], v[236:239], v[52:55]\n"
      "ds_read_b64_tr_b16 a[198:199], v17 offset:53760\n"
      "v_mfma_f32_16x16x32_bf16 v[56:59], a[200:203], v[228:231], 0\n"
      "v_mfma_f32_16x16x32_bf16 v[56:59], a[224:227], v[240:243], v[56:59]\n"
      "v_mfma_f32_16x16x32_bf16 v[60:63], a[204:207], v[220:223], 0\n"
      "s_waitcnt lgkmcnt(15)\n"
      "v_mfma_f32_16x16x32_bf16 v[60:63], a[228:231], v[232:235], v[60:63]\n"
      "v_mfma_f32_16x16x32_bf16 v[64:67], a[204:207], v[224:227], 0\n"
      "v_mfma_f32_16x16x32_bf16 v[64:67], a[228:231], v[236:239], v[64:67]\n"
      "v_mfma_f32_16x16x32_bf16 v[68:71], a[204:207], v[228:231], 0\n"
      "v_mfma_f32_16x16x32_bf16 v[68:71], a[228:231], v[240:243], v[68:71]\n"
      "v_mfma_f32_16x16x32_bf16 v[72:75], a[208:211], v[220:223], 0\n"
      "v_mfma_f32_16x16x32_bf16 v[72:75], a[232:235], v[232:235], v[72:75]\n"
      "v_mfma_f32_16x16x32_bf16 v[76:79], a[208:211], v[224:227], 0\n"
      "v_mfma_f32_16x16x32_bf16 v[76:79], a[232:235], v[236:239], v[76:79]\n"
      "v_mfma_f32_16x16x32_bf16 v[80:83], a[208:211], v[228:231], 0\n"
      "v_mfma_f32_16x16x32_bf16 v[80:83], a[232:235], v[240:243], v[80:83]\n"
      "v_mfma_f32_16x16x32_bf16 v[84:87], a[212:215], v[220:223], 0\n"
      "v_mfma_f32_16x16x32_bf16 v[84:87], a[236:239], v[232:235], v[84:87]\n"
      "v_mfma_f32_16x16x32_bf16 v[88:91], a[212:215], v[224:227], 0\n"
      "v_mfma_f32_16x16x32_bf16 v[88:91], a[236:239], v[236:239], v[88:91]\n"
      "v_mfma_f32_16x16x32_bf16 v[92:95], a[212:215], v[228:231], 0\n"
      "v_mfma_f32_16x16x32_bf16 v[92:95], a[236:239], v[240:243], v[92:95]\n"
      "v_mfma_f32_16x16x32_bf16 v[96:99], a[216:219], v[220:223], 0\n"
      "v_mfma_f32_16x16x32_bf16 v[96:99], a[240:243], v[232:235], v[96:99]\n"
      "v_mfma_f32_16x16x32_bf16 v[100:103], a[216:219], v[224:227], 0\n"
      "v_mfma_f32_16x16x32_bf16 v[100:103], a[240:243], v[236:239], v[100:103]\n"
      "v_mfma_f32_16x16x32_bf16 v[104:107], a[216:219], v[228:231], 0\n"
      "v_mfma_f32_16x16x32_bf16 v[104:107], a[240:243], v[240:243], v[104:107]\n"
      "v_mfma_f32_16x16x32_bf16 v[108:111], a[220:223], v[220:223], 0\n"
      "v_mfma_f32_16x16x32_bf16 v[108:111], a[244:247], v[232:235], v[108:111]\n"
      "v_mfma_f32_16x16x32_bf16 v[112:115], a[220:223], v[224:227], 0\n"
      "v_mfma_f32_16x16x32_bf16 v[112:115], a[244:247], v[236:239], v[112:115]\n"
      "v_mfma_f32_16x16x32_bf16 v[116:119], a[220:223], v[228:231], 0\n"
      "v_mfma_f32_16x16x32_bf16 v[116:119], a[244:247], v[240:243], v[116:119]\n"
      "s_waitcnt lgkmcnt(14)\n"
      "v_mfma_f32_16x16x32_bf16 v[120:123], a[168:171], a[24:27], v[120:123]\n"
      "s_waitcnt lgkmcnt(12)\n"
      "v_mfma_f32_16x16x32_bf16 v[120:123], a[172:175], a[36:39], v[120:123]\n"
      "v_mfma_f32_16x16x32_bf16 v[124:127], a[168:171], a[28:31], v[124:127]\n"
      "v_mfma_f32_16x16x32_bf16 v[124:127], a[172:175], a[40:43], v[124:127]\n"
      "s_add_u32 s88, s63, s72\n"
      "s_add_u32 m0, s85, 0x24000\n"
      "v_mfma_f32_16x16x32_bf16 v[128:131], a[168:171], a[32:35], v[128:131]\n"
      "buffer_load_dwordx4 v192, s[20:23], s88 offen lds\n"
      "v_mfma_f32_16x16x32_bf16 v[128:131], a[172:175], a[44:47], v[128:131]\n"
      "s_waitcnt lgkmcnt(10)\n"
      "v_mfma_f32_16x16x32_bf16 v[132:135], a[176:179], a[24:27], v[132:135]\n"
      "buffer_load_dwordx4 v193, s[20:23], s88 offen offset:1024 lds\n"
      "s_waitcnt lgkmcnt(8)\n"
      "v_mfma_f32_16x16x32_bf16 v[132:135], a[180:183], a[36:39], v[132:135]\n"
      "v_mfma_f32_16x16x32_bf16 v[136:139], a[176:179], a[28:31], v[136:139]\n"
      "buffer_load_dwordx4 v194, s[20:23], s88 offen offset:2048 lds\n"
      "v_mfma_f32_16x16x32_bf16 v[136:139], a[180:183], a[40:43], v[136:139]\n"
      "v_mfma_f32_16x16x32_bf16 v[140:143], a[176:179], a[32:35], v[140:143]\n"
      "buffer_load_dwordx4 v195, s[20:23], s88 offen offset:3072 lds\n"
      "v_mfma_f32_16x16x32_bf16 v[140:143], a[180:183], a[44:47], v[140:143]\n"
      "s_waitcnt vmcnt(20) lgkmcnt(0)\n"
      "s_barrier\n"
      "v_mfma_f32_16x16x32_bf16 v[144:147], a[184:187], a[24:27], v[144:147]\n"
      "ds_read_b64_tr_b16 a[168:169], v14\n"
      "v_mfma_f32_16x16x32_bf16 v[144:147], a[188:191], a[36:39], v[144:147]\n"
      "ds_read_b64_tr_b16 a[170:171], v14 offset:512\n"
      "v_mfma_f32_16x16x32_bf16 v[148:151], a[184:187], a[28:31], v[148:151]\n"
      "ds_read_b64_tr_b16 a[172:173], v14 offset:4096\n"
      "v_mfma_f32_16x16x32_bf16 v[148:151], a[188:191], a[40:43], v[148:151]\n"
      "ds_read_b64_tr_b16 a[174:175], v14 offset:4608\n"
      "v_mfma_f32_16x16x32_bf16 v[152:155], a[184:187], a[32:35], v[152:155]\n"
      "ds_read_b64_tr_b16 a[176:177], v15\n"
      "v_mfma_f32_16x16x32_bf16 v[152:155], a[188:191], a[44:47], v[152:155]\n"
      "ds_read_b64_tr_b16 a[178:179], v15 offset:512\n"
      "v_mfma_f32_16x16x32_bf16 v[156:159], a[192:195], a[24:27], v[156:159]\n"
      "ds_read_b64_tr_b16 a[180:181], v15 offset:4096\n"
      "v_mfma_f32_16x16x32_bf16 v[156:159], a[196:199], a[36:39], v[156:159]\n"
      "ds_read_b64_tr_b16 a[182:183], v15 offset:4608\n"
      "v_mfma_f32_16x16x32_bf16 v[160:163], a[192:195], a[28:31], v[160:163]\n"
      "ds_read_b64_tr_b16 a[184:185], v16\n"
      "v_mfma_f32_16x16x32_bf16 v[160:163], a[196:199], a[40:43], v[160:163]\n"
      "ds_read_b64_tr_b16 a[186:187], v16 offset:512\n"
      "v_mfma_f32_16x16x32_bf16 v[164:167], a[192:195], a[32:35], v[164:167]\n"
      "ds_read_b64_tr_b16 a[188:189], v16 offset:4096\n"
      "v_mfma_f32_16x16x32_bf16 v[164:167], a[196:199], a[44:47], v[164:167]\n"
      "ds_read_b64_tr_b16 a[190:191], v16 offset:4608\n"
      "s_waitcnt lgkmcnt(10)\n"
      "v_mfma_f32_16x16x32_bf16 v[120:123], a[168:171], a[48:51], v[120:123]\n"
      "ds_read_b64_tr_b16 a[192:193], v17\n"
      "s_waitcnt lgkmcnt(9)\n"
      "v_mfma_f32_16x16x32_bf16 v[120:123], a[172:175], a[60:63], v[120:123]\n"
      "ds_read_b64_tr_b16 a[194:195], v17 offset:512\n"
      "v_mfma_f32_16x16x32_bf16 v[124:127], a[168:171], a[52:55], v[124:127]\n"
      "ds_read_b64_tr_b16 a[196:197], v17 offset:4096\n"
      "v_mfma_f32_16x16x32_bf16 v[124:127], a[172:175], a[64:67], v[124:127]\n"
      "ds_read_b64_tr_b16 a[198:199], v17 offset:4608\n"
      "s_add_u32 s88, s64, s72\n"
      "s_add_u32 m0, s85, 0x18000\n"
      "v_mfma_f32_16x16x32_bf16 v[128:131], a[168:171], a[56:59], v[128:131]\n"
      "buffer_load_dwordx4 v192, s[20:23], s88 offen lds\n"
      "v_mfma_f32_16x16x32_bf16 v[128:131], a[172:175], a[68:71], v[128:131]\n"
      "s_waitcnt lgkmcnt(10)\n"
      "v_mfma_f32_16x16x32_bf16 v[132:135], a[176:179], a[48:51], v[132:135]\n"
      "buffer_load_dwordx4 v193, s[20:23], s88 offen offset:1024 lds\n"
      "s_waitcnt lgkmcnt(8)\n"
      "v_mfma_f32_16x16x32_bf16 v[132:135], a[180:183], a[60:63], v[132:135]\n"
      "v_mfma_f32_16x16x32_bf16 v[136:139], a[176:179], a[52:55], v[136:139]\n"
      "buffer_load_dwordx4 v194, s[20:23], s88 offen offset:2048 lds\n"
      "v_mfma_f32_16x16x32_bf16 v[136:139], a[180:183], a[64:67], v[136:139]\n"
      "v_mfma_f32_16x16x32_bf16 v[140:143], a[176:179], a[56:59], v[140:143]\n"
      "buffer_load_dwordx4 v195, s[20:23], s88 offen offset:3072 lds\n"
      "v_mfma_f32_16x16x32_bf16 v[140:143], a[180:183], a[68:71], v[140:143]\n"
      "s_waitcnt vmcnt(20) lgkmcnt(0)\n"
      "s_barrier\n"
      "v_mfma_f32_16x16x32_bf16 v[144:147], a[184:187], a[48:51], v[144:147]\n"
      "ds_read_b64_tr_b16 a[168:169], v200\n"
      "v_mfma_f32_16x16x32_bf16 v[144:147], a[188:191], a[60:63], v[144:147]\n"
      "ds_read_b64_tr_b16 a[170:171], v200 offset:512\n"
      "v_mfma_f32_16x16x32_bf16 v[148:151], a[184:187], a[52:55], v[148:151]\n"
      "ds_read_b64_tr_b16 a[172:173], v200 offset:4096\n"
      "v_mfma_f32_16x16x32_bf16 v[148:151], a[188:191], a[64:67], v[148:151]\n"
      "ds_read_b64_tr_b16 a[174:175], v200 offset:4608\n"
      "v_mfma_f32_16x16x32_bf16 v[152:155], a[184:187], a[56:59], v[152:155]\n"
      "ds_read_b64_tr_b16 a[176:177], v201\n"
      "v_mfma_f32_16x16x32_bf16 v[152:155], a[188:191], a[68:71], v[152:155]\n"
      "ds_read_b64_tr_b16 a[178:179], v201 offset:512\n"
      "v_mfma_f32_16x16x32_bf16 v[156:159], a[192:195], a[48:51], v[156:159]\n"
      "ds_read_b64_tr_b16 a[180:181], v201 offset:4096\n"
      "v_mfma_f32_16x16x32_bf16 v[156:159], a[196:199], a[60:63], v[156:159]\n"
      "ds_read_b64_tr_b16 a[182:183], v201 offset:4608\n"
      "v_mfma_f32_16x16x32_bf16 v[160:163], a[192:195], a[52:55], v[160:163]\n"
      "ds_read_b64_tr_b16 a[184:185], v202\n"
      "v_mfma_f32_16x16x32_bf16 v[160:163], a[196:199], a[64:67], v[160:163]\n"
      "ds_read_b64_tr_b16 a[186:187], v202 offset:512\n"
      "v_mfma_f32_16x16x32_bf16 v[164:167], a[192:195], a[56:59], v[164:167]\n"
      "ds_read_b64_tr_b16 a[188:189], v202 offset:4096\n"
      "v_mfma_f32_16x16x32_bf16 v[164:167], a[196:199], a[68:71], v[164:167]\n"
      "ds_read_b64_tr_b16 a[190:191], v202 offset:4608\n"
      "s_waitcnt lgkmcnt(10)\n"
      "v_mfma_f32_16x16x32_bf16 v[120:123], a[168:171], a[72:75], v[120:123]\n"
      "ds_read_b64_tr_b16 a[192:193], v203\n"
      "s_waitcnt lgkmcnt(9)\n"
      "v_mfma_f32_16x16x32_bf16 v[120:123], a[172:175], a[84:87], v[120:123]\n"
      "ds_read_b64_tr_b16 a[194:195], v203 offset:512\n"
      "v_mfma_f32_16x16x32_bf16 v[124:127], a[168:171], a[76:79], v[124:127]\n"
      "ds_read_b64_tr_b16 a[196:197], v203 offset:4096\n"
      "v_mfma_f32_16x16x32_bf16 v[124:127], a[172:175], a[88:91], v[124:127]\n"
      "ds_read_b64_tr_b16 a[198:199], v203 offset:4608\n"
      "s_add_u32 s88, s65, s72\n"
      "s_add_u32 m0, s85, 0xc000\n"
      "v_mfma_f32_16x16x32_bf16 v[128:131], a[168:171], a[80:83], v[128:131]\n"
      "buffer_load_dwordx4 v192, s[20:23], s88 offen lds\n"
      "v_mfma_f32_16x16x32_bf16 v[128:131], a[172:175], a[92:95], v[128:131]\n"
      "s_waitcnt lgkmcnt(10)\n"
      "v_mfma_f32_16x16x32_bf16 v[132:135], a[176:179], a[72:75], v[132:135]\n"
      "buffer_load_dwordx4 v193, s[20:23], s88 offen offset:1024 lds\n"
      "s_waitcnt lgkmcnt(8)\n"
      "v_mfma_f32_16x16x32_bf16 v[132:135], a[180:183], a[84:87], v[132:135]\n"
      "v_mfma_f32_16x16x32_bf16 v[136:139], a[176:179], a[76:79], v[136:139]\n"
      "buffer_load_dwordx4 v194, s[20:23], s88 offen offset:2048 lds\n"
      "v_mfma_f32_16x16x32_bf16 v[136:139], a[180:183], a[88:91], v[136:139]\n"
      "v_mfma_f32_16x16x32_bf16 v[140:143], a[176:179], a[80:83], v[140:143]\n"
      "buffer_load_dwordx4 v195, s[20:23], s88 offen offset:3072 lds\n"
      "v_mfma_f32_16x16x32_bf16 v[140:143], a[180:183], a[92:95], v[140:143]\n"
      "s_waitcnt vmcnt(20) lgkmcnt(0)\n"
      "s_barrier\n"
      "v_mfma_f32_16x16x32_bf16 v[144:147], a[184:187], a[72:75], v[144:147]\n"
      "ds_read_b64_tr_b16 a[168:169], v14 offset:16384\n"
      "v_mfma_f32_16x16x32_bf16 v[144:147], a[188:191], a[84:87], v[144:147]\n"
      "ds_read_b64_tr_b16 a[170:171], v14 offset:16896\n"
      "v_mfma_f32_16x16x32_bf16 v[148:151], a[184:187], a[76:79], v[148:151]\n"
      "ds_read_b64_tr_b16 a[172:173], v14 offset:20480\n"
      "v_mfma_f32_16x16x32_bf16 v[148:151], a[188:191], a[88:91], v[148:151]\n"
      "ds_read_b64_tr_b16 a[174:175], v14 offset:20992\n"
      "v_mfma_f32_16x16x32_bf16 v[152:155], a[184:187], a[80:83], v[152:155]\n"
      "ds_read_b64_tr_b16 a[176:177], v15 offset:16384\n"
      "v_mfma_f32_16x16x32_bf16 v[152:155], a[188:191], a[92:95], v[152:155]\n"
      "ds_read_b64_tr_b16 a[178:179], v15 offset:16896\n"
      "v_mfma_f32_16x16x32_bf16 v[156:159], a[192:195], a[72:75], v[156:159]\n"
      "ds_read_b64_tr_b16 a[180:181], v15 offset:20480\n"
      "v_mfma_f32_16x16x32_bf16 v[156:159], a[196:199], a[84:87], v[156:159]\n"
      "ds_read_b64_tr_b16 a[182:183], v15 offset:20992\n"
      "s_add_u32 s88, s66, s72\n"
      "s_add_u32 m0, s85, 0x1c000\n"
      "v_mfma_f32_16x16x32_bf16 v[160:163], a[192:195], a[76:79], v[160:163]\n"
      "ds_read_b64_tr_b16 a[184:185], v16 offset:16384\n"
      "buffer_load_dwordx4 v192, s[20:23], s88 offen lds\n"
      "v_mfma_f32_16x16x32_bf16 v[160:163], a[196:199], a[88:91], v[160:163]\n"
      "ds_read_b64_tr_b16 a[186:187], v16 offset:16896\n"
      "v_mfma_f32_16x16x32_bf16 v[164:167], a[192:195], a[80:83], v[164:167]\n"
      "ds_read_b64_tr_b16 a[188:189], v16 offset:20480\n"
      "buffer_load_dwordx4 v193, s[20:23], s88 offen offset:1024 lds\n"
      "v_mfma_f32_16x16x32_bf16 v[164:167], a[196:199], a[92:95], v[164:167]\n"
      "ds_read_b64_tr_b16 a[190:191], v16 offset:20992\n"
      "s_waitcnt lgkmcnt(10)\n"
      "v_mfma_f32_16x16x32_bf16 v[120:123], a[168:171], a[96:99], v[120:123]\n"
      "ds_read_b64_tr_b16 a[192:193], v17 offset:16384\n"
      "buffer_load_dwordx4 v194, s[20:23], s88 offen offset:2048 lds\n"
      "s_waitcnt lgkmcnt(9)\n"
      "v_mfma_f32_16x16x32_bf16 v[120:123], a[172:175], a[108:111], v[120:123]\n"
      "ds_read_b64_tr_b16 a[194:195], v17 offset:16896\n"
      "v_mfma_f32_16x16x32_bf16 v[124:127], a[168:171], a[100:103], v[124:127]\n"
      "ds_read_b64_tr_b16 a[196:197], v17 offset:20480\n"
      "buffer_load_dwordx4 v195, s[20:23], s88 offen offset:3072 lds\n"
      "v_mfma_f32_16x16x32_bf16 v[124:127], a[172:175], a[112:115], v[124:127]\n"
      "ds_read_b64_tr_b16 a[198:199], v17 offset:20992\n"
      "s_add_u32 s89, s71, 0\n"
      "s_add_u32 m0, s86, 0x6000\n"
      "v_mfma_f32_16x16x32_bf16 v[128:131], a[168:171], a[104:107], v[128:131]\n"
      "buffer_load_dwordx4 v204, s[12:15], s89 offen nt lds\n"
      "v_mfma_f32_16x16x32_bf16 v[128:131], a[172:175], a[116:119], v[128:131]\n"
      "s_waitcnt lgkmcnt(10)\n"
      "v_mfma_f32_16x16x32_bf16 v[132:135], a[176:179], a[96:99], v[132:135]\n"
      "buffer_load_dwordx4 v205, s[12:15], s89 offen offset:1024 nt lds\n"
      "s_waitcnt lgkmcnt(8)\n"
      "v_mfma_f32_16x16x32_bf16 v[132:135], a[180:183], a[108:111], v[132:135]\n"
      "s_add_u32 s89, s71, 0x4000\n"
      "s_add_u32 m0, s86, 0x8000\n"
      "v_mfma_f32_16x16x32_bf16 v[136:139], a[176:179], a[100:103], v[136:139]\n"
      "buffer_load_dwordx4 v204, s[12:15], s89 offen nt lds\n"
      "v_mfma_f32_16x16x32_bf16 v[136:139], a[180:183], a[112:115], v[136:139]\n"
      "v_mfma_f32_16x16x32_bf16 v[140:143], a[176:179], a[104:107], v[140:143]\n"
      "buffer_load_dwordx4 v205, s[12:15], s89 offen offset:1024 nt lds\n"
      "v_mfma_f32_16x16x32_bf16 v[140:143], a[180:183], a[116:119], v[140:143]\n"
      "s_waitcnt vmcnt(24)\n"
      "s_barrier\n"
      "s_waitcnt lgkmcnt(6)\n"
      "v_mfma_f32_16x16x32_bf16 v[144:147], a[184:187], a[96:99], v[144:147]\n"
      "ds_read_b64_tr_b16 a[168:169], v200 offset:16384\n"
      "s_waitcnt lgkmcnt(5)\n"
      "v_mfma_f32_16x16x32_bf16 v[144:147], a[188:191], a[108:111], v[144:147]\n"
      "ds_read_b64_tr_b16 a[170:171], v200 offset:16896\n"
      "v_mfma_f32_16x16x32_bf16 v[148:151], a[184:187], a[100:103], v[148:151]\n"
      "ds_read_b64_tr_b16 a[172:173], v200 offset:20480\n"
      "v_mfma_f32_16x16x32_bf16 v[148:151], a[188:191], a[112:115], v[148:151]\n"
      "ds_read_b64_tr_b16 a[174:175], v200 offset:20992\n"
      "v_mfma_f32_16x16x32_bf16 v[152:155], a[184:187], a[104:107], v[152:155]\n"
      "ds_read_b64_tr_b16 a[176:177], v201 offset:16384\n"
      "v_mfma_f32_16x16x32_bf16 v[152:155], a[188:191], a[116:119], v[152:155]\n"
      "ds_read_b64_tr_b16 a[178:179], v201 offset:16896\n"
      "s_waitcnt lgkmcnt(8)\n"
      "v_mfma_f32_16x16x32_bf16 v[156:159], a[192:195], a[96:99], v[156:159]\n"
      "ds_read_b64_tr_b16 a[180:181], v201 offset:20480\n"
      "s_waitcnt lgkmcnt(7)\n"
      "v_mfma_f32_16x16x32_bf16 v[156:159], a[196:199], a[108:111], v[156:159]\n"
      "ds_read_b64_tr_b16 a[182:183], v201 offset:20992\n"
      "v_mfma_f32_16x16x32_bf16 v[160:163], a[192:195], a[100:103], v[160:163]\n"
      "ds_read_b64_tr_b16 a[184:185], v202 offset:16384\n"
      "v_mfma_f32_16x16x32_bf16 v[160:163], a[196:199], a[112:115], v[160:163]\n"
      "ds_read_b64_tr_b16 a[186:187], v202 offset:16896\n"
      "v_mfma_f32_16x16x32_bf16 v[164:167], a[192:195], a[104:107], v[164:167]\n"
      "ds_read_b64_tr_b16 a[188:189], v202 offset:20480\n"
      "v_mfma_f32_16x16x32_bf16 v[164:167], a[196:199], a[116:119], v[164:167]\n"
      "ds_read_b64_tr_b16 a[190:191], v202 offset:20992\n"
      "s_waitcnt lgkmcnt(10)\n"
      "v_mfma_f32_16x16x32_bf16 v[120:123], a[168:171], a[120:123], v[120:123]\n"
      "ds_read_b64_tr_b16 a[192:193], v203 offset:16384\n"
      "s_waitcnt lgkmcnt(9)\n"
      "v_mfma_f32_16x16x32_bf16 v[120:123], a[172:175], a[132:135], v[120:123]\n"
      "ds_read_b64_tr_b16 a[194:195], v203 offset:16896\n"
      "v_mfma_f32_16x16x32_bf16 v[124:127], a[168:171], a[124:127], v[124:127]\n"
      "ds_read_b64_tr_b16 a[196:197], v203 offset:20480\n"
      "v_mfma_f32_16x16x32_bf16 v[124:127], a[172:175], a[136:139], v[124:127]\n"
      "ds_read_b64_tr_b16 a[198:199], v203 offset:20992\n"
      "v_mfma_f32_16x16x32_bf16 v[128:131], a[168:171], a[128:131], v[128:131]\n"
      "v_mfma_f32_16x16x32_bf16 v[128:131], a[172:175], a[140:143], v[128:131]\n"
      "s_waitcnt lgkmcnt(10)\n"
      "v_mfma_f32_16x16x32_bf16 v[132:135], a[176:179], a[120:123], v[132:135]\n"
      "s_waitcnt lgkmcnt(8)\n"
      "v_mfma_f32_16x16x32_bf16 v[132:135], a[180:183], a[132:135], v[132:135]\n"
      "s_add_u32 s89, s71, 0x8000\n"
      "s_add_u32 m0, s86, 0xa000\n"
      "v_mfma_f32_16x16x32_bf16 v[136:139], a[176:179], a[124:127], v[136:139]\n"
      "buffer_load_dwordx4 v204, s[12:15], s89 offen nt lds\n"
      "v_mfma_f32_16x16x32_bf16 v[136:139], a[180:183], a[136:139], v[136:139]\n"
      "v_mfma_f32_16x16x32_bf16 v[140:143], a[176:179], a[128:131], v[140:143]\n"
      "buffer_load_dwordx4 v205, s[12:15], s89 offen offset:1024 nt lds\n"
      "v_mfma_f32_16x16x32_bf16 v[140:143], a[180:183], a[140:143], v[140:143]\n"
      "s_waitcnt vmcnt(22)\n"
      "s_barrier\n"
      "s_waitcnt lgkmcnt(6)\n"
      "v_mfma_f32_16x16x32_bf16 v[144:147], a[184:187], a[120:123], v[144:147]\n"
      "ds_read_b64_tr_b16 a[168:169], v14 offset:32768\n"
      "s_waitcnt lgkmcnt(5)\n"
      "v_mfma_f32_16x16x32_bf16 v[144:147], a[188:191], a[132:135], v[144:147]\n"
      "ds_read_b64_tr_b16 a[170:171], v14 offset:33280\n"
      "v_mfma_f32_16x16x32_bf16 v[148:151], a[184:187], a[124:127], v[148:151]\n"
      "ds_read_b64_tr_b16 a[172:173], v14 offset:36864\n"
      "v_mfma_f32_16x16x32_bf16 v[148:151], a[188:191], a[136:139], v[148:151]\n"
      "ds_read_b64_tr_b16 a[174:175], v14 offset:37376\n"
      "v_mfma_f32_16x16x32_bf16 v[152:155], a[184:187], a[128:131], v[152:155]\n"
      "ds_read_b64_tr_b16 a[176:177], v15 offset:32768\n"
      "v_mfma_f32_16x16x32_bf16 v[152:155], a[188:191], a[140:143], v[152:155]\n"
      "ds_read_b64_tr_b16 a[178:179], v15 offset:33280\n"
      "s_waitcnt lgkmcnt(8)\n"
      "v_mfma_f32_16x16x32_bf16 v[156:159], a[192:195], a[120:123], v[156:159]\n"
      "ds_read_b64_tr_b16 a[180:181], v15 offset:36864\n"
      "s_waitcnt lgkmcnt(7)\n"
      "v_mfma_f32_16x16x32_bf16 v[156:159], a[196:199], a[132:135], v[156:159]\n"
      "ds_read_b64_tr_b16 a[182:183], v15 offset:37376\n"
      "v_mfma_f32_16x16x32_bf16 v[160:163], a[192:195], a[124:127], v[160:163]\n"
      "ds_read_b64_tr_b16 a[184:185], v16 offset:32768\n"
      "v_mfma_f32_16x16x32_bf16 v[160:163], a[196:199], a[136:139], v[160:163]\n"
      "ds_read_b64_tr_b16 a[186:187], v16 offset:33280\n"
      "v_mfma_f32_16x16x32_bf16 v[164:167], a[192:195], a[128:131], v[164:167]\n"
      "ds_read_b64_tr_b16 a[188:189], v16 offset:36864\n"
      "v_mfma_f32_16x16x32_bf16 v[164:167], a[196:199], a[140:143], v[164:167]\n"
      "ds_read_b64_tr_b16 a[190:191], v16 offset:37376\n"
      "s_waitcnt lgkmcnt(10)\n"
      "v_mfma_f32_16x16x32_bf16 v[120:123], a[168:171], a[144:147], v[120:123]\n"
      "ds_read_b64_tr_b16 a[192:193], v17 offset:32768\n"
      "s_waitcnt lgkmcnt(9)\n"
      "v_mfma_f32_16x16x32_bf16 v[120:123], a[172:175], a[156:159], v[120:123]\n"
      "ds_read_b64_tr_b16 a[194:195], v17 offset:33280\n"
      "v_mfma_f32_16x16x32_bf16 v[124:127], a[168:171], a[148:151], v[124:127]\n"
      "ds_read_b64_tr_b16 a[196:197], v17 offset:36864\n"
      "v_mfma_f32_16x16x32_bf16 v[124:127], a[172:175], a[160:163], v[124:127]\n"
      "ds_read_b64_tr_b16 a[198:199], v17 offset:37376\n"
      "s_add_u32 s89, s72, 0\n"
      "s_add_u32 m0, s86, 0\n"
      "v_mfma_f32_16x16x32_bf16 v[128:131], a[168:171], a[152:155], v[128:131]\n"
      "buffer_load_dwordx4 v204, s[12:15], s89 offen nt lds\n"
      "v_mfma_f32_16x16x32_bf16 v[128:131], a[172:175], a[164:167], v[128:131]\n"
      "s_waitcnt lgkmcnt(10)\n"
      "v_mfma_f32_16x16x32_bf16 v[132:135], a[176:179], a[144:147], v[132:135]\n"
      "buffer_load_dwordx4 v205, s[12:15], s89 offen offset:1024 nt lds\n"
      "s_waitcnt lgkmcnt(8)\n"
      "v_mfma_f32_16x16x32_bf16 v[132:135], a[180:183], a[156:159], v[132:135]\n"
      "s_add_u32 s89, s72, 0x4000\n"
      "s_add_u32 m0, s86, 0x2000\n"
      "v_mfma_f32_16x16x32_bf16 v[136:139], a[176:179], a[148:151], v[136:139]\n"
      "buffer_load_dwordx4 v204, s[12:15], s89 offen nt lds\n"
      "v_mfma_f32_16x16x32_bf16 v[136:139], a[180:183], a[160:163], v[136:139]\n"
      "v_mfma_f32_16x16x32_bf16 v[140:143], a[176:179], a[152:155], v[140:143]\n"
      "buffer_load_dwordx4 v205, s[12:15], s89 offen offset:1024 nt lds\n"
      "v_mfma_f32_16x16x32_bf16 v[140:143], a[180:183], a[164:167], v[140:143]\n"
      "s_waitcnt vmcnt(22)\n"
      "s_barrier\n"
      "s_waitcnt lgkmcnt(6)\n"
      "v_mfma_f32_16x16x32_bf16 v[144:147], a[184:187], a[144:147], v[144:147]\n"
      "ds_read_b64_tr_b16 a[168:169], v200 offset:32768\n"
      "v_cvt_pk_bf16_f32 v168, v120, v121\n"
      "s_waitcnt lgkmcnt(5)\n"
      "v_mfma_f32_16x16x32_bf16 v[144:147], a[188:191], a[156:159], v[144:147]\n"
      "ds_read_b64_tr_b16 a[170:171], v200 offset:33280\n"
      "v_cvt_pk_bf16_f32 v169, v122, v123\n"
      "v_mfma_f32_16x16x32_bf16 v[148:151], a[184:187], a[148:151], v[148:151]\n"
      "ds_read_b64_tr_b16 a[172:173], v200 offset:36864\n"
      "v_cvt_pk_bf16_f32 v172, v124, v125\n"
      "v_mfma_f32_16x16x32_bf16 v[148:151], a[188:191], a[160:163], v[148:151]\n"
      "ds_read_b64_tr_b16 a[174:175], v200 offset:37376\n"
      "v_cvt_pk_bf16_f32 v173, v126, v127\n"
      "v_mfma_f32_16x16x32_bf16 v[152:155], a[184:187], a[152:155], v[152:155]\n"
      "ds_read_b64_tr_b16 a[176:177], v201 offset:32768\n"
      "v_cvt_pk_bf16_f32 v176, v128, v129\n"
      "v_mfma_f32_16x16x32_bf16 v[152:155], a[188:191], a[164:167], v[152:155]\n"
      "ds_read_b64_tr_b16 a[178:179], v201 offset:33280\n"
      "v_cvt_pk_bf16_f32 v177, v130, v131\n"
      "s_waitcnt lgkmcnt(8)\n"
      "v_mfma_f32_16x16x32_bf16 v[156:159], a[192:195], a[144:147], v[156:159]\n"
      "ds_read_b64_tr_b16 a[180:181], v201 offset:36864\n"
      "v_cvt_pk_bf16_f32 v170, v132, v133\n"
      "s_waitcnt lgkmcnt(7)\n"
      "v_mfma_f32_16x16x32_bf16 v[156:159], a[196:199], a[156:159], v[156:159]\n"
      "ds_read_b64_tr_b16 a[182:183], v201 offset:37376\n"
      "v_cvt_pk_bf16_f32 v171, v134, v135\n"
      "v_mfma_f32_16x16x32_bf16 v[160:163], a[192:195], a[148:151], v[160:163]\n"
      "ds_read_b64_tr_b16 a[184:185], v202 offset:32768\n"
      "v_cvt_pk_bf16_f32 v174, v136, v137\n"
      "v_mfma_f32_16x16x32_bf16 v[160:163], a[196:199], a[160:163], v[160:163]\n"
      "ds_read_b64_tr_b16 a[186:187], v202 offset:33280\n"
      "v_cvt_pk_bf16_f32 v175, v138, v139\n"
      "v_mfma_f32_16x16x32_bf16 v[164:167], a[192:195], a[152:155], v[164:167]\n"
      "ds_read_b64_tr_b16 a[188:189], v202 offset:36864\n"
      "v_cvt_pk_bf16_f32 v178, v140, v141\n"
      "v_mfma_f32_16x16x32_bf16 v[164:167], a[196:199], a[164:167], v[164:167]\n"
      "ds_read_b64_tr_b16 a[190:191], v202 offset:37376\n"
      "v_cvt_pk_bf16_f32 v179, v142, v143\n"
      "s_add_u32 s89, s72, 0x8000\n"
      "s_add_u32 m0, s86, 0x4000\n"
      "s_waitcnt lgkmcnt(10)\n"
      "v_mfma_f32_16x16x32_bf16 v[120:123], a[168:171], a[0:3], 0\n"
      "ds_read_b64_tr_b16 a[192:193], v203 offset:32768\n"
      "v_cvt_pk_bf16_f32 v180, v144, v145\n"
      "buffer_load_dwordx4 v204, s[12:15], s89 offen nt lds\n"
      "s_waitcnt lgkmcnt(9)\n"
      "v_mfma_f32_16x16x32_bf16 v[120:123], a[172:175], a[12:15], v[120:123]\n"
      "ds_read_b64_tr_b16 a[194:195], v203 offset:33280\n"
      "v_cvt_pk_bf16_f32 v181, v146, v147\n"
      "v_mfma_f32_16x16x32_bf16 v[124:127], a[168:171], a[4:7], 0\n"
      "ds_read_b64_tr_b16 a[196:197], v203 offset:36864\n"
      "v_cvt_pk_bf16_f32 v184, v148, v149\n"
      "buffer_load_dwordx4 v205, s[12:15], s89 offen offset:1024 nt lds\n"
      "v_mfma_f32_16x16x32_bf16 v[124:127], a[172:175], a[16:19], v[124:127]\n"
      "ds_read_b64_tr_b16 a[198:199], v203 offset:37376\n"
      "v_cvt_pk_bf16_f32 v185, v150, v151\n"
      "s_add_u32 s88, s67, s72\n"
      "s_add_u32 m0, s85, 0x10000\n"
      "v_mfma_f32_16x16x32_bf16 v[128:131], a[168:171], a[8:11], 0\n"
      "v_cvt_pk_bf16_f32 v188, v152, v153\n"
      "buffer_load_dwordx4 v192, s[20:23], s88 offen lds\n"
      "v_mfma_f32_16x16x32_bf16 v[128:131], a[172:175], a[20:23], v[128:131]\n"
      "v_cvt_pk_bf16_f32 v189, v154, v155\n"
      "s_waitcnt lgkmcnt(10)\n"
      "v_mfma_f32_16x16x32_bf16 v[132:135], a[176:179], a[0:3], 0\n"
      "v_cvt_pk_bf16_f32 v182, v156, v157\n"
      "buffer_load_dwordx4 v193, s[20:23], s88 offen offset:1024 lds\n"
      "s_waitcnt lgkmcnt(8)\n"
      "v_mfma_f32_16x16x32_bf16 v[132:135], a[180:183], a[12:15], v[132:135]\n"
      "v_cvt_pk_bf16_f32 v183, v158, v159\n"
      "v_mfma_f32_16x16x32_bf16 v[136:139], a[176:179], a[4:7], 0\n"
      "v_cvt_pk_bf16_f32 v186, v160, v161\n"
      "buffer_load_dwordx4 v194, s[20:23], s88 offen offset:2048 lds\n"
      "v_mfma_f32_16x16x32_bf16 v[136:139], a[180:183], a[16:19], v[136:139]\n"
      "v_cvt_pk_bf16_f32 v187, v162, v163\n"
      "v_mfma_f32_16x16x32_bf16 v[140:143], a[176:179], a[8:11], 0\n"
      "v_cvt_pk_bf16_f32 v190, v164, v165\n"
      "buffer_load_dwordx4 v195, s[20:23], s88 offen offset:3072 lds\n"
      "v_mfma_f32_16x16x32_bf16 v[140:143], a[180:183], a[20:23], v[140:143]\n"
      "v_cvt_pk_bf16_f32 v191, v166, v167\n"
      "s_waitcnt vmcnt(24)\n"
      "s_barrier\n"
      "s_waitcnt lgkmcnt(6)\n"
      "v_mfma_f32_16x16x32_bf16 v[144:147], a[184:187], a[0:3], 0\n"
      "ds_read_b64_tr_b16 a[168:169], v14 offset:49152\n"
      "s_waitcnt lgkmcnt(5)\n"
      "v_mfma_f32_16x16x32_bf16 v[144:147], a[188:191], a[12:15], v[144:147]\n"
      "ds_read_b64_tr_b16 a[170:171], v14 offset:49664\n"
      "v_mfma_f32_16x16x32_bf16 v[148:151], a[184:187], a[4:7], 0\n"
      "ds_read_b64_tr_b16 a[172:173], v14 offset:53248\n"
      "v_mfma_f32_16x16x32_bf16 v[148:151], a[188:191], a[16:19], v[148:151]\n"
      "ds_read_b64_tr_b16 a[174:175], v14 offset:53760\n"
      "v_mfma_f32_16x16x32_bf16 v[152:155], a[184:187], a[8:11], 0\n"
      "ds_read_b64_tr_b16 a[176:177], v15 offset:49152\n"
      "v_mfma_f32_16x16x32_bf16 v[152:155], a[188:191], a[20:23], v[152:155]\n"
      "ds_read_b64_tr_b16 a[178:179], v15 offset:49664\n"
      "s_waitcnt lgkmcnt(8)\n"
      "v_mfma_f32_16x16x32_bf16 v[156:159], a[192:195], a[0:3], 0\n"
      "ds_read_b64_tr_b16 a[180:181], v15 offset:53248\n"
      "s_waitcnt lgkmcnt(7)\n"
      "v_mfma_f32_16x16x32_bf16 v[156:159], a[196:199], a[12:15], v[156:159]\n"
      "ds_read_b64_tr_b16 a[182:183], v15 offset:53760\n"
      "v_mfma_f32_16x16x32_bf16 v[160:163], a[192:195], a[4:7], 0\n"
      "ds_read_b64_tr_b16 a[184:185], v16 offset:49152\n"
      "v_mfma_f32_16x16x32_bf16 v[160:163], a[196:199], a[16:19], v[160:163]\n"
      "ds_read_b64_tr_b16 a[186:187], v16 offset:49664\n"
      "v_mfma_f32_16x16x32_bf16 v[164:167], a[192:195], a[8:11], 0\n"
      "ds_read_b64_tr_b16 a[188:189], v16 offset:53248\n"
      "v_mfma_f32_16x16x32_bf16 v[164:167], a[196:199], a[20:23], v[164:167]\n"
      "ds_read_b64_tr_b16 a[190:191], v16 offset:53760\n"
      "s_waitcnt lgkmcnt(10)\n"
      "v_mfma_f32_16x16x32_bf16 v[120:123], a[168:171], a[24:27], v[120:123]\n"
      "ds_read_b64_tr_b16 a[192:193], v17 offset:49152\n"
      "s_waitcnt lgkmcnt(9)\n"
      "v_mfma_f32_16x16x32_bf16 v[120:123], a[172:175], a[36:39], v[120:123]\n"
      "ds_read_b64_tr_b16 a[194:195], v17 offset:49664\n"
      "v_mfma_f32_16x16x32_bf16 v[124:127], a[168:171], a[28:31], v[124:127]\n"
      "ds_read_b64_tr_b16 a[196:197], v17 offset:53248\n"
      "v_mfma_f32_16x16x32_bf16 v[124:127], a[172:175], a[40:43], v[124:127]\n"
      "ds_read_b64_tr_b16 a[198:199], v17 offset:53760\n"
      "v_mfma_f32_16x16x32_bf16 v[128:131], a[168:171], a[32:35], v[128:131]\n"
      "v_mfma_f32_16x16x32_bf16 v[128:131], a[172:175], a[44:47], v[128:131]\n"
      "s_waitcnt lgkmcnt(10)\n"
      "v_mfma_f32_16x16x32_bf16 v[132:135], a[176:179], a[24:27], v[132:135]\n"
      "s_waitcnt lgkmcnt(8)\n"
      "v_mfma_f32_16x16x32_bf16 v[132:135], a[180:183], a[36:39], v[132:135]\n"
      "v_mfma_f32_16x16x32_bf16 v[136:139], a[176:179], a[28:31], v[136:139]\n"
      "v_mfma_f32_16x16x32_bf16 v[136:139], a[180:183], a[40:43], v[136:139]\n"
      "v_mfma_f32_16x16x32_bf16 v[140:143], a[176:179], a[32:35], v[140:143]\n"
      "v_mfma_f32_16x16x32_bf16 v[140:143], a[180:183], a[44:47], v[140:143]\n"
      "s_waitcnt vmcnt(10)\n"
      "s_barrier\n"
      "s_waitcnt lgkmcnt(6)\n"
      "v_mfma_f32_16x16x32_bf16 v[144:147], a[184:187], a[24:27], v[144:147]\n"
      "ds_read_b64_tr_b16 a[168:169], v14\n"
      "s_waitcnt lgkmcnt(5)\n"
      "v_mfma_f32_16x16x32_bf16 v[144:147], a[188:191], a[36:39], v[144:147]\n"
      "ds_read_b64_tr_b16 a[170:171], v14 offset:512\n"
      "v_mfma_f32_16x16x32_bf16 v[148:151], a[184:187], a[28:31], v[148:151]\n"
      "ds_read_b64_tr_b16 a[172:173], v14 offset:4096\n"
      "v_mfma_f32_16x16x32_bf16 v[148:151], a[188:191], a[40:43], v[148:151]\n"
      "ds_read_b64_tr_b16 a[174:175], v14 offset:4608\n"
      "v_mfma_f32_16x16x32_bf16 v[152:155], a[184:187], a[32:35], v[152:155]\n"
      "ds_read_b64_tr_b16 a[176:177], v15\n"
      "s_add_u32 s88, s68, s72\n"
      "s_add_u32 m0, s85, 0x20000\n"
      "v_mfma_f32_16x16x32_bf16 v[152:155], a[188:191], a[44:47], v[152:155]\n"
      "ds_read_b64_tr_b16 a[178:179], v15 offset:512\n"
      "buffer_load_dwordx4 v192, s[20:23], s88 offen lds\n"
      "s_waitcnt lgkmcnt(8)\n"
      "v_mfma_f32_16x16x32_bf16 v[156:159], a[192:195], a[24:27], v[156:159]\n"
      "ds_read_b64_tr_b16 a[180:181], v15 offset:4096\n"
      "s_waitcnt lgkmcnt(7)\n"
      "v_mfma_f32_16x16x32_bf16 v[156:159], a[196:199], a[36:39], v[156:159]\n"
      "ds_read_b64_tr_b16 a[182:183], v15 offset:4608\n"
      "buffer_load_dwordx4 v193, s[20:23], s88 offen offset:1024 lds\n"
      "v_mfma_f32_16x16x32_bf16 v[160:163], a[192:195], a[28:31], v[160:163]\n"
      "ds_read_b64_tr_b16 a[184:185], v16\n"
      "v_mfma_f32_16x16x32_bf16 v[160:163], a[196:199], a[40:43], v[160:163]\n"
      "ds_read_b64_tr_b16 a[186:187], v16 offset:512\n"
      "buffer_load_dwordx4 v194, s[20:23], s88 offen offset:2048 lds\n"
      "v_mfma_f32_16x16x32_bf16 v[164:167], a[192:195], a[32:35], v[164:167]\n"
      "ds_read_b64_tr_b16 a[188:189], v16 offset:4096\n"
      "v_mfma_f32_16x16x32_bf16 v[164:167], a[196:199], a[44:47], v[164:167]\n"
      "ds_read_b64_tr_b16 a[190:191], v16 offset:4608\n"
      "buffer_load_dwordx4 v195, s[20:23], s88 offen offset:3072 lds\n"
      "s_waitcnt lgkmcnt(10)\n"
      "v_mfma_f32_16x16x32_bf16 v[120:123], a[168:171], a[48:51], v[120:123]\n"
      "ds_read_b64_tr_b16 a[192:193], v17\n"
      "ds_read_b64 a[200:201], v24 offset:24576\n"
      "s_waitcnt lgkmcnt(10)\n"
      "v_mfma_f32_16x16x32_bf16 v[120:123], a[172:175], a[60:63], v[120:123]\n"
      "ds_read_b64_tr_b16 a[194:195], v17 offset:512\n"
      "ds_read_b64 a[202:203], v25 offset:24576\n"
      "v_mfma_f32_16x16x32_bf16 v[124:127], a[168:171], a[52:55], v[124:127]\n"
      "ds_read_b64_tr_b16 a[196:197], v17 offset:4096\n"
      "ds_read_b64 a[204:205], v24 offset:26624\n"
      "v_mfma_f32_16x16x32_bf16 v[124:127], a[172:175], a[64:67], v[124:127]\n"
      "ds_read_b64_tr_b16 a[198:199], v17 offset:4608\n"
      "ds_read_b64 a[206:207], v25 offset:26624\n"
      "s_add_u32 s88, s69, s72\n"
      "s_add_u32 m0, s85, 0x14000\n"
      "v_mfma_f32_16x16x32_bf16 v[128:131], a[168:171], a[56:59], v[128:131]\n"
      "ds_read_b64 a[208:209], v24 offset:32768\n"
      "buffer_load_dwordx4 v192, s[20:23], s88 offen lds\n"
      "v_mfma_f32_16x16x32_bf16 v[128:131], a[172:175], a[68:71], v[128:131]\n"
      "ds_read_b64 a[210:211], v25 offset:32768\n"
      "s_waitcnt lgkmcnt(15)\n"
      "v_mfma_f32_16x16x32_bf16 v[132:135], a[176:179], a[48:51], v[132:135]\n"
      "ds_read_b64 a[212:213], v24 offset:34816\n"
      "buffer_load_dwordx4 v193, s[20:23], s88 offen offset:1024 lds\n"
      "s_waitcnt lgkmcnt(15)\n"
      "v_mfma_f32_16x16x32_bf16 v[132:135], a[180:183], a[60:63], v[132:135]\n"
      "ds_read_b64 a[214:215], v25 offset:34816\n"
      "v_mfma_f32_16x16x32_bf16 v[136:139], a[176:179], a[52:55], v[136:139]\n"
      "ds_read_b64 a[216:217], v24 offset:40960\n"
      "buffer_load_dwordx4 v194, s[20:23], s88 offen offset:2048 lds\n"
      "v_mfma_f32_16x16x32_bf16 v[136:139], a[180:183], a[64:67], v[136:139]\n"
      "ds_read_b64 a[218:219], v25 offset:40960\n"
      "v_mfma_f32_16x16x32_bf16 v[140:143], a[176:179], a[56:59], v[140:143]\n"
      "ds_read_b64 a[220:221], v24 offset:43008\n"
      "buffer_load_dwordx4 v195, s[20:23], s88 offen offset:3072 lds\n"
      "v_mfma_f32_16x16x32_bf16 v[140:143], a[180:183], a[68:71], v[140:143]\n"
      "ds_read_b64 a[222:223], v25 offset:43008\n"
      "s_barrier\n"
      "s_waitcnt lgkmcnt(15)\n"
      "v_mfma_f32_16x16x32_bf16 v[144:147], a[184:187], a[48:51], v[144:147]\n"
      "ds_read_b64_tr_b16 a[168:169], v200\n"
      "v_mfma_f32_16x16x32_bf16 v[144:147], a[188:191], a[60:63], v[144:147]\n"
      "ds_read_b64_tr_b16 a[170:171], v200 offset:512\n"
      "v_mfma_f32_16x16x32_bf16 v[148:151], a[184:187], a[52:55], v[148:151]\n"
      "ds_read_b64_tr_b16 a[172:173], v200 offset:4096\n"
      "v_mfma_f32_16x16x32_bf16 v[148:151], a[188:191], a[64:67], v[148:151]\n"
      "ds_read_b64_tr_b16 a[174:175], v200 offset:4608\n"
      "v_mfma_f32_16x16x32_bf16 v[152:155], a[184:187], a[56:59], v[152:155]\n"
      "ds_read_b64_tr_b16 a[176:177], v201\n"
      "s_add_u32 s88, s63, s73\n"
      "s_add_u32 m0, s85, 0x24000\n"
      "v_mfma_f32_16x16x32_bf16 v[152:155], a[188:191], a[68:71], v[152:155]\n"
      "ds_read_b64_tr_b16 a[178:179], v201 offset:512\n"
      "buffer_load_dwordx4 v192, s[20:23], s88 offen lds\n"
      "s_waitcnt lgkmcnt(15)\n"
      "v_mfma_f32_16x16x32_bf16 v[156:159], a[192:195], a[48:51], v[156:159]\n"
      "ds_read_b64_tr_b16 a[180:181], v201 offset:4096\n"
      "v_mfma_f32_16x16x32_bf16 v[156:159], a[196:199], a[60:63], v[156:159]\n"
      "ds_read_b64_tr_b16 a[182:183], v201 offset:4608\n"
      "buffer_load_dwordx4 v193, s[20:23], s88 offen offset:1024 lds\n"
      "v_mfma_f32_16x16x32_bf16 v[160:163], a[192:195], a[52:55], v[160:163]\n"
      "ds_read_b64_tr_b16 a[184:185], v202\n"
      "v_mfma_f32_16x16x32_bf16 v[160:163], a[196:199], a[64:67], v[160:163]\n"
      "ds_read_b64_tr_b16 a[186:187], v202 offset:512\n"
      "buffer_load_dwordx4 v194, s[20:23], s88 offen offset:2048 lds\n"
      "v_mfma_f32_16x16x32_bf16 v[164:167], a[192:195], a[56:59], v[164:167]\n"
      "ds_read_b64_tr_b16 a[188:189], v202 offset:4096\n"
      "v_mfma_f32_16x16x32_bf16 v[164:167], a[196:199], a[68:71], v[164:167]\n"
      "ds_read_b64_tr_b16 a[190:191], v202 offset:4608\n"
      "buffer_load_dwordx4 v195, s[20:23], s88 offen offset:3072 lds\n"
      "s_waitcnt lgkmcnt(10)\n"
      "v_mfma_f32_16x16x32_bf16 v[120:123], a[168:171], a[72:75], v[120:123]\n"
      "ds_read_b64_tr_b16 a[192:193], v203\n"
      "ds_read_b64 a[224:225], v26 offset:24576\n"
      "s_waitcnt lgkmcnt(10)\n"
      "v_mfma_f32_16x16x32_bf16 v[120:123], a[172:175], a[84:87], v[120:123]\n"
      "ds_read_b64_tr_b16 a[194:195], v203 offset:512\n"
      "ds_read_b64 a[226:227], v27 offset:24576\n"
      "v_mfma_f32_16x16x32_bf16 v[124:127], a[168:171], a[76:79], v[124:127]\n"
      "ds_read_b64_tr_b16 a[196:197], v203 offset:4096\n"
      "ds_read_b64 a[228:229], v26 offset:26624\n"
      "v_mfma_f32_16x16x32_bf16 v[124:127], a[172:175], a[88:91], v[124:127]\n"
      "ds_read_b64_tr_b16 a[198:199], v203 offset:4608\n"
      "ds_read_b64 a[230:231], v27 offset:26624\n"
      "s_add_u32 s88, s64, s73\n"
      "s_add_u32 m0, s85, 0x18000\n"
      "v_mfma_f32_16x16x32_bf16 v[128:131], a[168:171], a[80:83], v[128:131]\n"
      "ds_read_b64 a[232:233], v26 offset:32768\n"
      "buffer_load_dwordx4 v192, s[20:23], s88 offen lds\n"
      "v_mfma_f32_16x16x32_bf16 v[128:131], a[172:175], a[92:95], v[128:131]\n"
      "ds_read_b64 a[234:235], v27 offset:32768\n"
      "s_waitcnt lgkmcnt(15)\n"
      "v_mfma_f32_16x16x32_bf16 v[132:135], a[176:179], a[72:75], v[132:135]\n"
      "ds_read_b64 a[236:237], v26 offset:34816\n"
      "buffer_load_dwordx4 v193, s[20:23], s88 offen offset:1024 lds\n"
      "s_waitcnt lgkmcnt(15)\n"
      "v_mfma_f32_16x16x32_bf16 v[132:135], a[180:183], a[84:87], v[132:135]\n"
      "ds_read_b64 a[238:239], v27 offset:34816\n"
      "v_mfma_f32_16x16x32_bf16 v[136:139], a[176:179], a[76:79], v[136:139]\n"
      "ds_read_b64 a[240:241], v26 offset:40960\n"
      "buffer_load_dwordx4 v194, s[20:23], s88 offen offset:2048 lds\n"
      "v_mfma_f32_16x16x32_bf16 v[136:139], a[180:183], a[88:91], v[136:139]\n"
      "ds_read_b64 a[242:243], v27 offset:40960\n"
      "v_mfma_f32_16x16x32_bf16 v[140:143], a[176:179], a[80:83], v[140:143]\n"
      "ds_read_b64 a[244:245], v26 offset:43008\n"
      "buffer_load_dwordx4 v195, s[20:23], s88 offen offset:3072 lds\n"
      "v_mfma_f32_16x16x32_bf16 v[140:143], a[180:183], a[92:95], v[140:143]\n"
      "ds_read_b64 a[246:247], v27 offset:43008\n"
      "s_waitcnt vmcnt(16)\n"
      "s_barrier\n"
      "s_waitcnt lgkmcnt(15)\n"
      "v_mfma_f32_16x16x32_bf16 v[144:147], a[184:187], a[72:75], v[144:147]\n"
      "ds_read_b64_tr_b16 a[168:169], v14 offset:16384\n"
      "v_mfma_f32_16x16x32_bf16 v[144:147], a[188:191], a[84:87], v[144:147]\n"
      "ds_read_b64_tr_b16 a[170:171], v14 offset:16896\n"
      "v_mfma_f32_16x16x32_bf16 v[148:151], a[184:187], a[76:79], v[148:151]\n"
      "ds_read_b64_tr_b16 a[172:173], v14 offset:20480\n"
      "v_mfma_f32_16x16x32_bf16 v[148:151], a[188:191], a[88:91], v[148:151]\n"
      "ds_read_b64_tr_b16 a[174:175], v14 offset:20992\n"
      "v_mfma_f32_16x16x32_bf16 v[152:155], a[184:187], a[80:83], v[152:155]\n"
      "ds_read_b64_tr_b16 a[176:177], v15 offset:16384\n"
      "v_mfma_f32_16x16x32_bf16 v[152:155], a[188:191], a[92:95], v[152:155]\n"
      "ds_read_b64_tr_b16 a[178:179], v15 offset:16896\n"
      "s_waitcnt lgkmcnt(15)\n"
      "v_mfma_f32_16x16x32_bf16 v[156:159], a[192:195], a[72:75], v[156:159]\n"
      "ds_read_b64_tr_b16 a[180:181], v15 offset:20480\n"
      "v_mfma_f32_16x16x32_bf16 v[156:159], a[196:199], a[84:87], v[156:159]\n"
      "ds_read_b64_tr_b16 a[182:183], v15 offset:20992\n"
      "v_mfma_f32_16x16x32_bf16 v[160:163], a[192:195], a[76:79], v[160:163]\n"
      "ds_read_b64_tr_b16 a[184:185], v16 offset:16384\n"
      "v_mfma_f32_16x16x32_bf16 v[160:163], a[196:199], a[88:91], v[160:163]\n"
      "ds_read_b64_tr_b16 a[186:187], v16 offset:16896\n"
      "v_mfma_f32_16x16x32_bf16 v[164:167], a[192:195], a[80:83], v[164:167]\n"
      "ds_read_b64_tr_b16 a[188:189], v16 offset:20480\n"
      "v_mfma_f32_16x16x32_bf16 v[164:167], a[196:199], a[92:95], v[164:167]\n"
      "ds_read_b64_tr_b16 a[190:191], v16 offset:20992\n"
      "s_waitcnt lgkmcnt(10)\n"
      "v_mfma_f32_16x16x32_bf16 v[120:123], a[168:171], a[96:99], v[120:123]\n"
      "ds_read_b64_tr_b16 a[192:193], v17 offset:16384\n"
      "s_waitcnt lgkmcnt(9)\n"
      "v_mfma_f32_16x16x32_bf16 v[120:123], a[172:175], a[108:111], v[120:123]\n"
      "ds_read_b64_tr_b16 a[194:195], v17 offset:16896\n"
      "v_mfma_f32_16x16x32_bf16 v[124:127], a[168:171], a[100:103], v[124:127]\n"
      "ds_read_b64_tr_b16 a[196:197], v17 offset:20480\n"
      "v_mfma_f32_16x16x32_bf16 v[124:127], a[172:175], a[112:115], v[124:127]\n"
      "ds_read_b64_tr_b16 a[198:199], v17 offset:20992\n"
      "s_add_u32 s88, s65, s73\n"
      "s_add_u32 m0, s85, 0xc000\n"
      "v_mfma_f32_16x16x32_bf16 v[128:131], a[168:171], a[104:107], v[128:131]\n"
      "buffer_load_dwordx4 v192, s[20:23], s88 offen lds\n"
      "v_mfma_f32_16x16x32_bf16 v[128:131], a[172:175], a[116:119], v[128:131]\n"
      "s_waitcnt lgkmcnt(10)\n"
      "v_mfma_f32_16x16x32_bf16 v[132:135], a[176:179], a[96:99], v[132:135]\n"
      "buffer_load_dwordx4 v193, s[20:23], s88 offen offset:1024 lds\n"
      "s_waitcnt lgkmcnt(8)\n"
      "v_mfma_f32_16x16x32_bf16 v[132:135], a[180:183], a[108:111], v[132:135]\n"
      "v_mfma_f32_16x16x32_bf16 v[136:139], a[176:179], a[100:103], v[136:139]\n"
      "buffer_load_dwordx4 v194, s[20:23], s88 offen offset:2048 lds\n"
      "v_mfma_f32_16x16x32_bf16 v[136:139], a[180:183], a[112:115], v[136:139]\n"
      "v_mfma_f32_16x16x32_bf16 v[140:143], a[176:179], a[104:107], v[140:143]\n"
      "buffer_load_dwordx4 v195, s[20:23], s88 offen offset:3072 lds\n"
      "v_mfma_f32_16x16x32_bf16 v[140:143], a[180:183], a[116:119], v[140:143]\n"
      "s_waitcnt vmcnt(16) lgkmcnt(0)\n"
      "s_barrier\n"
      "v_mfma_f32_16x16x32_bf16 v[144:147], a[184:187], a[96:99], v[144:147]\n"
      "ds_read_b64_tr_b16 a[168:169], v200 offset:16384\n"
      "v_mfma_f32_16x16x32_bf16 v[144:147], a[188:191], a[108:111], v[144:147]\n"
      "ds_read_b64_tr_b16 a[170:171], v200 offset:16896\n"
      "v_mfma_f32_16x16x32_bf16 v[148:151], a[184:187], a[100:103], v[148:151]\n"
      "ds_read_b64_tr_b16 a[172:173], v200 offset:20480\n"
      "v_mfma_f32_16x16x32_bf16 v[148:151], a[188:191], a[112:115], v[148:151]\n"
      "ds_read_b64_tr_b16 a[174:175], v200 offset:20992\n"
      "v_mfma_f32_16x16x32_bf16 v[152:155], a[184:187], a[104:107], v[152:155]\n"
      "ds_read_b64_tr_b16 a[176:177], v201 offset:16384\n"
      "v_mfma_f32_16x16x32_bf16 v[152:155], a[188:191], a[116:119], v[152:155]\n"
      "ds_read_b64_tr_b16 a[178:179], v201 offset:16896\n"
      "v_mfma_f32_16x16x32_bf16 v[156:159], a[192:195], a[96:99], v[156:159]\n"
      "ds_read_b64_tr_b16 a[180:181], v201 offset:20480\n"
      "v_mfma_f32_16x16x32_bf16 v[156:159], a[196:199], a[108:111], v[156:159]\n"
      "ds_read_b64_tr_b16 a[182:183], v201 offset:20992\n"
      "v_mfma_f32_16x16x32_bf16 v[160:163], a[192:195], a[100:103], v[160:163]\n"
      "ds_read_b64_tr_b16 a[184:185], v202 offset:16384\n"
      "v_mfma_f32_16x16x32_bf16 v[160:163], a[196:199], a[112:115], v[160:163]\n"
      "ds_read_b64_tr_b16 a[186:187], v202 offset:16896\n"
      "v_mfma_f32_16x16x32_bf16 v[164:167], a[192:195], a[104:107], v[164:167]\n"
      "ds_read_b64_tr_b16 a[188:189], v202 offset:20480\n"
      "v_mfma_f32_16x16x32_bf16 v[164:167], a[196:199], a[116:119], v[164:167]\n"
      "ds_read_b64_tr_b16 a[190:191], v202 offset:20992\n"
      "v_mfma_f32_16x16x32_bf16 v[48:51], a[200:203], v[168:171], v[48:51]\n"
      "ds_read_b64_tr_b16 a[192:193], v203 offset:16384\n"
      "v_mfma_f32_16x16x32_bf16 v[48:51], a[224:227], v[180:183], v[48:51]\n"
      "ds_read_b64_tr_b16 a[194:195], v203 offset:16896\n"
      "v_mfma_f32_16x16x32_bf16 v[52:55], a[200:203], v[172:175], v[52:55]\n"
      "ds_read_b64_tr_b16 a[196:197], v203 offset:20480\n"
      "v_mfma_f32_16x16x32_bf16 v[52:55], a[224:227], v[184:187], v[52:55]\n"
      "ds_read_b64_tr_b16 a[198:199], v203 offset:20992\n"
      "s_add_u32 s88, s66, s73\n"
      "s_add_u32 m0, s85, 0x1c000\n"
      "v_mfma_f32_16x16x32_bf16 v[56:59], a[200:203], v[176:179], v[56:59]\n"
      "buffer_load_dwordx4 v192, s[20:23], s88 offen lds\n"
      "v_mfma_f32_16x16x32_bf16 v[56:59], a[224:227], v[188:191], v[56:59]\n"
      "ds_read_b64 a[200:201], v24\n"
      "v_mfma_f32_16x16x32_bf16 v[60:63], a[204:207], v[168:171], v[60:63]\n"
      "ds_read_b64 a[202:203], v25\n"
      "v_mfma_f32_16x16x32_bf16 v[60:63], a[228:231], v[180:183], v[60:63]\n"
      "ds_read_b64 a[224:225], v26\n"
      "v_mfma_f32_16x16x32_bf16 v[64:67], a[204:207], v[172:175], v[64:67]\n"
      "ds_read_b64 a[226:227], v27\n"
      "v_mfma_f32_16x16x32_bf16 v[64:67], a[228:231], v[184:187], v[64:67]\n"
      "v_mfma_f32_16x16x32_bf16 v[68:71], a[204:207], v[176:179], v[68:71]\n"
      "buffer_load_dwordx4 v193, s[20:23], s88 offen offset:1024 lds\n"
      "v_mfma_f32_16x16x32_bf16 v[68:71], a[228:231], v[188:191], v[68:71]\n"
      "ds_read_b64 a[204:205], v24 offset:2048\n"
      "v_mfma_f32_16x16x32_bf16 v[72:75], a[208:211], v[168:171], v[72:75]\n"
      "ds_read_b64 a[206:207], v25 offset:2048\n"
      "v_mfma_f32_16x16x32_bf16 v[72:75], a[232:235], v[180:183], v[72:75]\n"
      "ds_read_b64 a[228:229], v26 offset:2048\n"
      "v_mfma_f32_16x16x32_bf16 v[76:79], a[208:211], v[172:175], v[76:79]\n"
      "ds_read_b64 a[230:231], v27 offset:2048\n"
      "v_mfma_f32_16x16x32_bf16 v[76:79], a[232:235], v[184:187], v[76:79]\n"
      "v_mfma_f32_16x16x32_bf16 v[80:83], a[208:211], v[176:179], v[80:83]\n"
      "buffer_load_dwordx4 v194, s[20:23], s88 offen offset:2048 lds\n"
      "v_mfma_f32_16x16x32_bf16 v[80:83], a[232:235], v[188:191], v[80:83]\n"
      "ds_read_b64 a[208:209], v24 offset:8192\n"
      "v_mfma_f32_16x16x32_bf16 v[84:87], a[212:215], v[168:171], v[84:87]\n"
      "ds_read_b64 a[210:211], v25 offset:8192\n"
      "v_mfma_f32_16x16x32_bf16 v[84:87], a[236:239], v[180:183], v[84:87]\n"
      "ds_read_b64 a[232:233], v26 offset:8192\n"
      "v_mfma_f32_16x16x32_bf16 v[88:91], a[212:215], v[172:175], v[88:91]\n"
      "ds_read_b64 a[234:235], v27 offset:8192\n"
      "v_mfma_f32_16x16x32_bf16 v[88:91], a[236:239], v[184:187], v[88:91]\n"
      "v_mfma_f32_16x16x32_bf16 v[92:95], a[212:215], v[176:179], v[92:95]\n"
      "buffer_load_dwordx4 v195, s[20:23], s88 offen offset:3072 lds\n"
      "v_mfma_f32_16x16x32_bf16 v[92:95], a[236:239], v[188:191], v[92:95]\n"
      "ds_read_b64 a[212:213], v24 offset:10240\n"
      "v_mfma_f32_16x16x32_bf16 v[96:99], a[216:219], v[168:171], v[96:99]\n"
      "ds_read_b64 a[214:215], v25 offset:10240\n"
      "v_mfma_f32_16x16x32_bf16 v[96:99], a[240:243], v[180:183], v[96:99]\n"
      "ds_read_b64 a[236:237], v26 offset:10240\n"
      "v_mfma_f32_16x16x32_bf16 v[100:103], a[216:219], v[172:175], v[100:103]\n"
      "ds_read_b64 a[238:239], v27 offset:10240\n"
      "v_mfma_f32_16x16x32_bf16 v[100:103], a[240:243], v[184:187], v[100:103]\n"
      "s_add_u32 s88, s67, s73\n"
      "s_add_u32 m0, s85, 0x10000\n"
      "v_mfma_f32_16x16x32_bf16 v[104:107], a[216:219], v[176:179], v[104:107]\n"
      "buffer_load_dwordx4 v192, s[20:23], s88 offen lds\n"
      "v_mfma_f32_16x16x32_bf16 v[104:107], a[240:243], v[188:191], v[104:107]\n"
      "ds_read_b64 a[216:217], v24 offset:16384\n"
      "v_mfma_f32_16x16x32_bf16 v[108:111], a[220:223], v[168:171], v[108:111]\n"
      "ds_read_b64 a[218:219], v25 offset:16384\n"
      "buffer_load_dwordx4 v193, s[20:23], s88 offen offset:1024 lds\n"
      "v_mfma_f32_16x16x32_bf16 v[108:111], a[244:247], v[180:183], v[108:111]\n"
      "ds_read_b64 a[240:241], v26 offset:16384\n"
      "v_mfma_f32_16x16x32_bf16 v[112:115], a[220:223], v[172:175], v[112:115]\n"
      "ds_read_b64 a[242:243], v27 offset:16384\n"
      "buffer_load_dwordx4 v194, s[20:23], s88 offen offset:2048 lds\n"
      "v_mfma_f32_16x16x32_bf16 v[112:115], a[244:247], v[184:187], v[112:115]\n"
      "v_mfma_f32_16x16x32_bf16 v[116:119], a[220:223], v[176:179], v[116:119]\n"
      "buffer_load_dwordx4 v195, s[20:23], s88 offen offset:3072 lds\n"
      "v_mfma_f32_16x16x32_bf16 v[116:119], a[244:247], v[188:191], v[116:119]\n"
      "ds_read_b64 a[220:221], v24 offset:18432\n"
      "s_add_u32 s89, s73, 0\n"
      "s_add_u32 m0, s86, 0x6000\n"
      "s_waitcnt lgkmcnt(15)\n"
      "v_mfma_f32_16x16x32_bf16 v[120:123], a[168:171], a[120:123], v[120:123]\n"
      "ds_read_b64 a[222:223], v25 offset:18432\n"
      "buffer_load_dwordx4 v204, s[12:15], s89 offen nt lds\n"
      "v_mfma_f32_16x16x32_bf16 v[120:123], a[172:175], a[132:135], v[120:123]\n"
      "ds_read_b64 a[244:245], v26 offset:18432\n"
      "v_mfma_f32_16x16x32_bf16 v[124:127], a[168:171], a[124:127], v[124:127]\n"
      "ds_read_b64 a[246:247], v27 offset:18432\n"
      "buffer_load_dwordx4 v205, s[12:15], s89 offen offset:1024 nt lds\n"
      "v_mfma_f32_16x16x32_bf16 v[124:127], a[172:175], a[136:139], v[124:127]\n"
      "s_add_u32 s89, s73, 0x4000\n"
      "s_add_u32 m0, s86, 0x8000\n"
      "v_mfma_f32_16x16x32_bf16 v[128:131], a[168:171], a[128:131], v[128:131]\n"
      "buffer_load_dwordx4 v204, s[12:15], s89 offen nt lds\n"
      "v_mfma_f32_16x16x32_bf16 v[128:131], a[172:175], a[140:143], v[128:131]\n"
      "v_mfma_f32_16x16x32_bf16 v[132:135], a[176:179], a[120:123], v[132:135]\n"
      "buffer_load_dwordx4 v205, s[12:15], s89 offen offset:1024 nt lds\n"
      "v_mfma_f32_16x16x32_bf16 v[132:135], a[180:183], a[132:135], v[132:135]\n"
      "s_add_u32 s89, s73, 0x8000\n"
      "s_add_u32 m0, s86, 0xa000\n"
      "v_mfma_f32_16x16x32_bf16 v[136:139], a[176:179], a[124:127], v[136:139]\n"
      "buffer_load_dwordx4 v204, s[12:15], s89 offen nt lds\n"
      "v_mfma_f32_16x16x32_bf16 v[136:139], a[180:183], a[136:139], v[136:139]\n"
      "v_mfma_f32_16x16x32_bf16 v[140:143], a[176:179], a[128:131], v[140:143]\n"
      "buffer_load_dwordx4 v205, s[12:15], s89 offen offset:1024 nt lds\n"
      "v_mfma_f32_16x16x32_bf16 v[140:143], a[180:183], a[140:143], v[140:143]\n"
      "s_waitcnt vmcnt(26) lgkmcnt(0)\n"
      "s_barrier\n"
      "v_mfma_f32_16x16x32_bf16 v[144:147], a[184:187], a[120:123], v[144:147]\n"
      "ds_read_b64_tr_b16 a[168:169], v14 offset:32768\n"
      "v_mfma_f32_16x16x32_bf16 v[144:147], a[188:191], a[132:135], v[144:147]\n"
      "ds_read_b64_tr_b16 a[170:171], v14 offset:33280\n"
      "v_mfma_f32_16x16x32_bf16 v[148:151], a[184:187], a[124:127], v[148:151]\n"
      "ds_read_b64_tr_b16 a[172:173], v14 offset:36864\n"
      "v_mfma_f32_16x16x32_bf16 v[148:151], a[188:191], a[136:139], v[148:151]\n"
      "ds_read_b64_tr_b16 a[174:175], v14 offset:37376\n"
      "v_mfma_f32_16x16x32_bf16 v[152:155], a[184:187], a[128:131], v[152:155]\n"
      "ds_read_b64_tr_b16 a[176:177], v15 offset:32768\n"
      "v_mfma_f32_16x16x32_bf16 v[152:155], a[188:191], a[140:143], v[152:155]\n"
      "ds_read_b64_tr_b16 a[178:179], v15 offset:33280\n"
      "v_mfma_f32_16x16x32_bf16 v[156:159], a[192:195], a[120:123], v[156:159]\n"
      "ds_read_b64_tr_b16 a[180:181], v15 offset:36864\n"
      "v_mfma_f32_16x16x32_bf16 v[156:159], a[196:199], a[132:135], v[156:159]\n"
      "ds_read_b64_tr_b16 a[182:183], v15 offset:37376\n"
      "v_mfma_f32_16x16x32_bf16 v[160:163], a[192:195], a[124:127], v[160:163]\n"
      "ds_read_b64_tr_b16 a[184:185], v16 offset:32768\n"
      "v_mfma_f32_16x16x32_bf16 v[160:163], a[196:199], a[136:139], v[160:163]\n"
      "ds_read_b64_tr_b16 a[186:187], v16 offset:33280\n"
      "v_mfma_f32_16x16x32_bf16 v[164:167], a[192:195], a[128:131], v[164:167]\n"
      "ds_read_b64_tr_b16 a[188:189], v16 offset:36864\n"
      "v_mfma_f32_16x16x32_bf16 v[164:167], a[196:199], a[140:143], v[164:167]\n"
      "ds_read_b64_tr_b16 a[190:191], v16 offset:37376\n"
      "s_waitcnt lgkmcnt(10)\n"
      "v_mfma_f32_16x16x32_bf16 v[120:123], a[168:171], a[144:147], v[120:123]\n"
      "ds_read_b64_tr_b16 a[192:193], v17 offset:32768\n"
      "s_waitcnt lgkmcnt(9)\n"
      "v_mfma_f32_16x16x32_bf16 v[120:123], a[172:175], a[156:159], v[120:123]\n"
      "ds_read_b64_tr_b16 a[194:195], v17 offset:33280\n"
      "v_mfma_f32_16x16x32_bf16 v[124:127], a[168:171], a[148:151], v[124:127]\n"
      "ds_read_b64_tr_b16 a[196:197], v17 offset:36864\n"
      "v_mfma_f32_16x16x32_bf16 v[124:127], a[172:175], a[160:163], v[124:127]\n"
      "ds_read_b64_tr_b16 a[198:199], v17 offset:37376\n"
      "s_add_u32 s89, s70, 0\n"
      "s_add_u32 m0, s86, 0\n"
      "v_mfma_f32_16x16x32_bf16 v[128:131], a[168:171], a[152:155], v[128:131]\n"
      "buffer_load_dwordx4 v216, s[16:19], s89 offen nt lds\n"
      "v_mfma_f32_16x16x32_bf16 v[128:131], a[172:175], a[164:167], v[128:131]\n"
      "s_waitcnt lgkmcnt(10)\n"
      "v_mfma_f32_16x16x32_bf16 v[132:135], a[176:179], a[144:147], v[132:135]\n"
      "buffer_load_dwordx4 v217, s[16:19], s89 offen offset:1024 nt lds\n"
      "s_waitcnt lgkmcnt(8)\n"
      "v_mfma_f32_16x16x32_bf16 v[132:135], a[180:183], a[156:159], v[132:135]\n"
      "s_add_u32 s89, s70, 0x8000\n"
      "s_add_u32 m0, s86, 0x4000\n"
      "v_mfma_f32_16x16x32_bf16 v[136:139], a[176:179], a[148:151], v[136:139]\n"
      "buffer_load_dwordx4 v216, s[16:19], s89 offen nt lds\n"
      "v_mfma_f32_16x16x32_bf16 v[136:139], a[180:183], a[160:163], v[136:139]\n"
      "v_mfma_f32_16x16x32_bf16 v[140:143], a[176:179], a[152:155], v[140:143]\n"
      "buffer_load_dwordx4 v217, s[16:19], s89 offen offset:1024 nt lds\n"
      "v_mfma_f32_16x16x32_bf16 v[140:143], a[180:183], a[164:167], v[140:143]\n"
      "s_waitcnt vmcnt(26)\n"
      "s_barrier\n"
      "s_waitcnt lgkmcnt(6)\n"
      "v_mfma_f32_16x16x32_bf16 v[144:147], a[184:187], a[144:147], v[144:147]\n"
      "ds_read_b64_tr_b16 a[168:169], v200 offset:32768\n"
      "v_cvt_pk_bf16_f32 v220, v120, v121\n"
      "s_waitcnt lgkmcnt(5)\n"
      "v_mfma_f32_16x16x32_bf16 v[144:147], a[188:191], a[156:159], v[144:147]\n"
      "ds_read_b64_tr_b16 a[170:171], v200 offset:33280\n"
      "v_cvt_pk_bf16_f32 v221, v122, v123\n"
      "v_mfma_f32_16x16x32_bf16 v[148:151], a[184:187], a[148:151], v[148:151]\n"
      "ds_read_b64_tr_b16 a[172:173], v200 offset:36864\n"
      "v_cvt_pk_bf16_f32 v224, v124, v125\n"
      "v_mfma_f32_16x16x32_bf16 v[148:151], a[188:191], a[160:163], v[148:151]\n"
      "ds_read_b64_tr_b16 a[174:175], v200 offset:37376\n"
      "v_cvt_pk_bf16_f32 v225, v126, v127\n"
      "v_mfma_f32_16x16x32_bf16 v[152:155], a[184:187], a[152:155], v[152:155]\n"
      "ds_read_b64_tr_b16 a[176:177], v201 offset:32768\n"
      "v_cvt_pk_bf16_f32 v228, v128, v129\n"
      "v_mfma_f32_16x16x32_bf16 v[152:155], a[188:191], a[164:167], v[152:155]\n"
      "ds_read_b64_tr_b16 a[178:179], v201 offset:33280\n"
      "v_cvt_pk_bf16_f32 v229, v130, v131\n"
      "s_waitcnt lgkmcnt(8)\n"
      "v_mfma_f32_16x16x32_bf16 v[156:159], a[192:195], a[144:147], v[156:159]\n"
      "ds_read_b64_tr_b16 a[180:181], v201 offset:36864\n"
      "v_cvt_pk_bf16_f32 v222, v132, v133\n"
      "s_waitcnt lgkmcnt(7)\n"
      "v_mfma_f32_16x16x32_bf16 v[156:159], a[196:199], a[156:159], v[156:159]\n"
      "ds_read_b64_tr_b16 a[182:183], v201 offset:37376\n"
      "v_cvt_pk_bf16_f32 v223, v134, v135\n"
      "v_mfma_f32_16x16x32_bf16 v[160:163], a[192:195], a[148:151], v[160:163]\n"
      "ds_read_b64_tr_b16 a[184:185], v202 offset:32768\n"
      "v_cvt_pk_bf16_f32 v226, v136, v137\n"
      "v_mfma_f32_16x16x32_bf16 v[160:163], a[196:199], a[160:163], v[160:163]\n"
      "ds_read_b64_tr_b16 a[186:187], v202 offset:33280\n"
      "v_cvt_pk_bf16_f32 v227, v138, v139\n"
      "v_mfma_f32_16x16x32_bf16 v[164:167], a[192:195], a[152:155], v[164:167]\n"
      "ds_read_b64_tr_b16 a[188:189], v202 offset:36864\n"
      "v_cvt_pk_bf16_f32 v230, v140, v141\n"
      "v_mfma_f32_16x16x32_bf16 v[164:167], a[196:199], a[164:167], v[164:167]\n"
      "ds_read_b64_tr_b16 a[190:191], v202 offset:37376\n"
      "v_cvt_pk_bf16_f32 v231, v142, v143\n"
      "s_waitcnt lgkmcnt(10)\n"
      "v_mfma_f32_16x16x32_bf16 v[120:123], a[168:171], a[0:3], 0\n"
      "ds_read_b64_tr_b16 a[192:193], v203 offset:32768\n"
      "v_cvt_pk_bf16_f32 v232, v144, v145\n"
      "s_waitcnt lgkmcnt(9)\n"
      "v_mfma_f32_16x16x32_bf16 v[120:123], a[172:175], a[12:15], v[120:123]\n"
      "ds_read_b64_tr_b16 a[194:195], v203 offset:33280\n"
      "v_cvt_pk_bf16_f32 v233, v146, v147\n"
      "v_mfma_f32_16x16x32_bf16 v[124:127], a[168:171], a[4:7], 0\n"
      "ds_read_b64_tr_b16 a[196:197], v203 offset:36864\n"
      "v_cvt_pk_bf16_f32 v236, v148, v149\n"
      "v_mfma_f32_16x16x32_bf16 v[124:127], a[172:175], a[16:19], v[124:127]\n"
      "ds_read_b64_tr_b16 a[198:199], v203 offset:37376\n"
      "v_cvt_pk_bf16_f32 v237, v150, v151\n"
      "v_mfma_f32_16x16x32_bf16 v[128:131], a[168:171], a[8:11], 0\n"
      "v_cvt_pk_bf16_f32 v240, v152, v153\n"
      "v_mfma_f32_16x16x32_bf16 v[128:131], a[172:175], a[20:23], v[128:131]\n"
      "v_cvt_pk_bf16_f32 v241, v154, v155\n"
      "s_waitcnt lgkmcnt(10)\n"
      "v_mfma_f32_16x16x32_bf16 v[132:135], a[176:179], a[0:3], 0\n"
      "v_cvt_pk_bf16_f32 v234, v156, v157\n"
      "s_waitcnt lgkmcnt(8)\n"
      "v_mfma_f32_16x16x32_bf16 v[132:135], a[180:183], a[12:15], v[132:135]\n"
      "v_cvt_pk_bf16_f32 v235, v158, v159\n"
      "s_add_u32 s89, s70, 0x4000\n"
      "s_add_u32 m0, s86, 0x2000\n"
      "v_mfma_f32_16x16x32_bf16 v[136:139], a[176:179], a[4:7], 0\n"
      "v_cvt_pk_bf16_f32 v238, v160, v161\n"
      "buffer_load_dwordx4 v216, s[16:19], s89 offen nt lds\n"
      "v_mfma_f32_16x16x32_bf16 v[136:139], a[180:183], a[16:19], v[136:139]\n"
      "v_cvt_pk_bf16_f32 v239, v162, v163\n"
      "v_mfma_f32_16x16x32_bf16 v[140:143], a[176:179], a[8:11], 0\n"
      "v_cvt_pk_bf16_f32 v242, v164, v165\n"
      "buffer_load_dwordx4 v217, s[16:19], s89 offen offset:1024 nt lds\n"
      "v_mfma_f32_16x16x32_bf16 v[140:143], a[180:183], a[20:23], v[140:143]\n"
      "v_cvt_pk_bf16_f32 v243, v166, v167\n"
      "s_waitcnt vmcnt(24)\n"
      "s_barrier\n"
      "s_waitcnt lgkmcnt(6)\n"
      "v_mfma_f32_16x16x32_bf16 v[144:147], a[184:187], a[0:3], 0\n"
      "ds_read_b64_tr_b16 a[168:169], v14 offset:49152\n"
      "s_waitcnt lgkmcnt(5)\n"
      "v_mfma_f32_16x16x32_bf16 v[144:147], a[188:191], a[12:15], v[144:147]\n"
      "ds_read_b64_tr_b16 a[170:171], v14 offset:49664\n"
      "v_mfma_f32_16x16x32_bf16 v[148:151], a[184:187], a[4:7], 0\n"
      "ds_read_b64_tr_b16 a[172:173], v14 offset:53248\n"
      "v_mfma_f32_16x16x32_bf16 v[148:151], a[188:191], a[16:19], v[148:151]\n"
      "ds_read_b64_tr_b16 a[174:175], v14 offset:53760\n"
      "v_mfma_f32_16x16x32_bf16 v[152:155], a[184:187], a[8:11], 0\n"
      "ds_read_b64_tr_b16 a[176:177], v15 offset:49152\n"
      "v_mfma_f32_16x16x32_bf16 v[152:155], a[188:191], a[20:23], v[152:155]\n"
      "ds_read_b64_tr_b16 a[178:179], v15 offset:49664\n"
      "s_waitcnt lgkmcnt(8)\n"
      "v_mfma_f32_16x16x32_bf16 v[156:159], a[192:195], a[0:3], 0\n"
      "ds_read_b64_tr_b16 a[180:181], v15 offset:53248\n"
      "s_waitcnt lgkmcnt(7)\n"
      "v_mfma_f32_16x16x32_bf16 v[156:159], a[196:199], a[12:15], v[156:159]\n"
      "ds_read_b64_tr_b16 a[182:183], v15 offset:53760\n"
      "v_mfma_f32_16x16x32_bf16 v[160:163], a[192:195], a[4:7], 0\n"
      "ds_read_b64_tr_b16 a[184:185], v16 offset:49152\n"
      "v_mfma_f32_16x16x32_bf16 v[160:163], a[196:199], a[16:19], v[160:163]\n"
      "ds_read_b64_tr_b16 a[186:187], v16 offset:49664\n"
      "v_mfma_f32_16x16x32_bf16 v[164:167], a[192:195], a[8:11], 0\n"
      "ds_read_b64_tr_b16 a[188:189], v16 offset:53248\n"
      "v_mfma_f32_16x16x32_bf16 v[164:167], a[196:199], a[20:23], v[164:167]\n"
      "ds_read_b64_tr_b16 a[190:191], v16 offset:53760\n"
      "s_waitcnt lgkmcnt(10)\n"
      "v_mfma_f32_16x16x32_bf16 v[120:123], a[168:171], a[24:27], v[120:123]\n"
      "ds_read_b64_tr_b16 a[192:193], v17 offset:49152\n"
      "s_waitcnt lgkmcnt(9)\n"
      "v_mfma_f32_16x16x32_bf16 v[120:123], a[172:175], a[36:39], v[120:123]\n"
      "ds_read_b64_tr_b16 a[194:195], v17 offset:49664\n"
      "v_mfma_f32_16x16x32_bf16 v[124:127], a[168:171], a[28:31], v[124:127]\n"
      "ds_read_b64_tr_b16 a[196:197], v17 offset:53248\n"
      "v_mfma_f32_16x16x32_bf16 v[124:127], a[172:175], a[40:43], v[124:127]\n"
      "ds_read_b64_tr_b16 a[198:199], v17 offset:53760\n"
      "v_mfma_f32_16x16x32_bf16 v[128:131], a[168:171], a[32:35], v[128:131]\n"
      "v_mfma_f32_16x16x32_bf16 v[128:131], a[172:175], a[44:47], v[128:131]\n"
      "s_waitcnt lgkmcnt(10)\n"
      "v_mfma_f32_16x16x32_bf16 v[132:135], a[176:179], a[24:27], v[132:135]\n"
      "s_waitcnt lgkmcnt(8)\n"
      "v_mfma_f32_16x16x32_bf16 v[132:135], a[180:183], a[36:39], v[132:135]\n"
      "v_mfma_f32_16x16x32_bf16 v[136:139], a[176:179], a[28:31], v[136:139]\n"
      "v_mfma_f32_16x16x32_bf16 v[136:139], a[180:183], a[40:43], v[136:139]\n"
      "v_mfma_f32_16x16x32_bf16 v[140:143], a[176:179], a[32:35], v[140:143]\n"
      "v_mfma_f32_16x16x32_bf16 v[140:143], a[180:183], a[44:47], v[140:143]\n"
      "s_waitcnt vmcnt(6)\n"
      "s_barrier\n"
      "s_waitcnt lgkmcnt(6)\n"
      "v_mfma_f32_16x16x32_bf16 v[144:147], a[184:187], a[24:27], v[144:147]\n"
      "ds_read_b64_tr_b16 a[168:169], v14\n"
      "s_waitcnt lgkmcnt(5)\n"
      "v_mfma_f32_16x16x32_bf16 v[144:147], a[188:191], a[36:39], v[144:147]\n"
      "ds_read_b64_tr_b16 a[170:171], v14 offset:512\n"
      "v_mfma_f32_16x16x32_bf16 v[148:151], a[184:187], a[28:31], v[148:151]\n"
      "ds_read_b64_tr_b16 a[172:173], v14 offset:4096\n"
      "v_mfma_f32_16x16x32_bf16 v[148:151], a[188:191], a[40:43], v[148:151]\n"
      "ds_read_b64_tr_b16 a[174:175], v14 offset:4608\n"
      "v_mfma_f32_16x16x32_bf16 v[152:155], a[184:187], a[32:35], v[152:155]\n"
      "ds_read_b64_tr_b16 a[176:177], v15\n"
      "v_mfma_f32_16x16x32_bf16 v[152:155], a[188:191], a[44:47], v[152:155]\n"
      "ds_read_b64_tr_b16 a[178:179], v15 offset:512\n"
      "s_waitcnt lgkmcnt(8)\n"
      "v_mfma_f32_16x16x32_bf16 v[156:159], a[192:195], a[24:27], v[156:159]\n"
      "ds_read_b64_tr_b16 a[180:181], v15 offset:4096\n"
      "s_waitcnt lgkmcnt(7)\n"
      "v_mfma_f32_16x16x32_bf16 v[156:159], a[196:199], a[36:39], v[156:159]\n"
      "ds_read_b64_tr_b16 a[182:183], v15 offset:4608\n"
      "v_mfma_f32_16x16x32_bf16 v[160:163], a[192:195], a[28:31], v[160:163]\n"
      "ds_read_b64_tr_b16 a[184:185], v16\n"
      "v_mfma_f32_16x16x32_bf16 v[160:163], a[196:199], a[40:43], v[160:163]\n"
      "ds_read_b64_tr_b16 a[186:187], v16 offset:512\n"
      "v_mfma_f32_16x16x32_bf16 v[164:167], a[192:195], a[32:35], v[164:167]\n"
      "ds_read_b64_tr_b16 a[188:189], v16 offset:4096\n"
      "v_mfma_f32_16x16x32_bf16 v[164:167], a[196:199], a[44:47], v[164:167]\n"
      "ds_read_b64_tr_b16 a[190:191], v16 offset:4608\n"
      "v_mfma_f32_16x16x32_bf16 v[48:51], a[200:203], v[220:223], v[48:51]\n"
      "ds_read_b64_tr_b16 a[192:193], v17\n"
      "v_mfma_f32_16x16x32_bf16 v[48:51], a[224:227], v[232:235], v[48:51]\n"
      "ds_read_b64_tr_b16 a[194:195], v17 offset:512\n"
      "v_mfma_f32_16x16x32_bf16 v[52:55], a[200:203], v[224:227], v[52:55]\n"
      "ds_read_b64_tr_b16 a[196:197], v17 offset:4096\n"
      "v_mfma_f32_16x16x32_bf16 v[52:55], a[224:227], v[236:239], v[52:55]\n"
      "ds_read_b64_tr_b16 a[198:199], v17 offset:4608\n"
      "v_mfma_f32_16x16x32_bf16 v[56:59], a[200:203], v[228:231], v[56:59]\n"
      "v_mfma_f32_16x16x32_bf16 v[56:59], a[224:227], v[240:243], v[56:59]\n"
      "v_mfma_f32_16x16x32_bf16 v[60:63], a[204:207], v[220:223], v[60:63]\n"
      "v_mfma_f32_16x16x32_bf16 v[60:63], a[228:231], v[232:235], v[60:63]\n"
      "v_mfma_f32_16x16x32_bf16 v[64:67], a[204:207], v[224:227], v[64:67]\n"
      "v_mfma_f32_16x16x32_bf16 v[64:67], a[228:231], v[236:239], v[64:67]\n"
      "v_mfma_f32_16x16x32_bf16 v[68:71], a[204:207], v[228:231], v[68:71]\n"
      "v_mfma_f32_16x16x32_bf16 v[68:71], a[228:231], v[240:243], v[68:71]\n"
      "v_mfma_f32_16x16x32_bf16 v[72:75], a[208:211], v[220:223], v[72:75]\n"
      "v_mfma_f32_16x16x32_bf16 v[72:75], a[232:235], v[232:235], v[72:75]\n"
      "v_mfma_f32_16x16x32_bf16 v[76:79], a[208:211], v[224:227], v[76:79]\n"
      "v_mfma_f32_16x16x32_bf16 v[76:79], a[232:235], v[236:239], v[76:79]\n"
      "v_mfma_f32_16x16x32_bf16 v[80:83], a[208:211], v[228:231], v[80:83]\n"
      "v_mfma_f32_16x16x32_bf16 v[80:83], a[232:235], v[240:243], v[80:83]\n"
      "v_mfma_f32_16x16x32_bf16 v[84:87], a[212:215], v[220:223], v[84:87]\n"
      "v_mfma_f32_16x16x32_bf16 v[84:87], a[236:239], v[232:235], v[84:87]\n"
      "v_mfma_f32_16x16x32_bf16 v[88:91], a[212:215], v[224:227], v[88:91]\n"
      "v_mfma_f32_16x16x32_bf16 v[88:91], a[236:239], v[236:239], v[88:91]\n"
      "v_mfma_f32_16x16x32_bf16 v[92:95], a[212:215], v[228:231], v[92:95]\n"
      "v_mfma_f32_16x16x32_bf16 v[92:95], a[236:239], v[240:243], v[92:95]\n"
      "v_mfma_f32_16x16x32_bf16 v[96:99], a[216:219], v[220:223], v[96:99]\n"
      "v_mfma_f32_16x16x32_bf16 v[96:99], a[240:243], v[232:235], v[96:99]\n"
      "v_mfma_f32_16x16x32_bf16 v[100:103], a[216:219], v[224:227], v[100:103]\n"
      "v_mfma_f32_16x16x32_bf16 v[100:103], a[240:243], v[236:239], v[100:103]\n"
      "v_mfma_f32_16x16x32_bf16 v[104:107], a[216:219], v[228:231], v[104:107]\n"
      "v_mfma_f32_16x16x32_bf16 v[104:107], a[240:243], v[240:243], v[104:107]\n"
      "v_mfma_f32_16x16x32_bf16 v[108:111], a[220:223], v[220:223], v[108:111]\n"
      "v_mfma_f32_16x16x32_bf16 v[108:111], a[244:247], v[232:235], v[108:111]\n"
      "v_mfma_f32_16x16x32_bf16 v[112:115], a[220:223], v[224:227], v[112:115]\n"
      "s_add_u32 s88, s68, s73\n"
      "s_add_u32 m0, s85, 0x20000\n"
      "v_mfma_f32_16x16x32_bf16 v[112:115], a[244:247], v[236:239], v[112:115]\n"
      "buffer_load_dwordx4 v192, s[20:23], s88 offen lds\n"
      "v_mfma_f32_16x16x32_bf16 v[116:119], a[220:223], v[228:231], v[116:119]\n"
      "v_mfma_f32_16x16x32_bf16 v[116:119], a[244:247], v[240:243], v[116:119]\n"
      "buffer_load_dwordx4 v193, s[20:23], s88 offen offset:1024 lds\n"
      "s_waitcnt lgkmcnt(14)\n"
      "v_mfma_f32_16x16x32_bf16 v[120:123], a[168:171], a[48:51], v[120:123]\n"
      "ds_read_b64 a[200:201], v24 offset:24576\n"
      "ds_read_b64 a[202:203], v25 offset:24576\n"
      "s_waitcnt lgkmcnt(14)\n"
      "v_mfma_f32_16x16x32_bf16 v[120:123], a[172:175], a[60:63], v[120:123]\n"
      "ds_read_b64 a[204:205], v24 offset:26624\n"
      "v_mfma_f32_16x16x32_bf16 v[124:127], a[168:171], a[52:55], v[124:127]\n"
      "ds_read_b64 a[206:207], v25 offset:26624\n"
      "ds_read_b64 a[208:209], v24 offset:32768\n"
      "v_mfma_f32_16x16x32_bf16 v[124:127], a[172:175], a[64:67], v[124:127]\n"
      "ds_read_b64 a[210:211], v25 offset:32768\n"
      "v_mfma_f32_16x16x32_bf16 v[128:131], a[168:171], a[56:59], v[128:131]\n"
      "ds_read_b64 a[212:213], v24 offset:34816\n"
      "ds_read_b64 a[214:215], v25 offset:34816\n"
      "v_mfma_f32_16x16x32_bf16 v[128:131], a[172:175], a[68:71], v[128:131]\n"
      "ds_read_b64 a[216:217], v24 offset:40960\n"
      "s_waitcnt lgkmcnt(15)\n"
      "v_mfma_f32_16x16x32_bf16 v[132:135], a[176:179], a[48:51], v[132:135]\n"
      "ds_read_b64 a[218:219], v25 offset:40960\n"
      "ds_read_b64 a[220:221], v24 offset:43008\n"
      "v_mfma_f32_16x16x32_bf16 v[132:135], a[180:183], a[60:63], v[132:135]\n"
      "ds_read_b64 a[222:223], v25 offset:43008\n"
      "v_mfma_f32_16x16x32_bf16 v[136:139], a[176:179], a[52:55], v[136:139]\n"
      "buffer_load_dwordx4 v194, s[20:23], s88 offen offset:2048 lds\n"
      "v_mfma_f32_16x16x32_bf16 v[136:139], a[180:183], a[64:67], v[136:139]\n"
      "v_mfma_f32_16x16x32_bf16 v[140:143], a[176:179], a[56:59], v[140:143]\n"
      "buffer_load_dwordx4 v195, s[20:23], s88 offen offset:3072 lds\n"
      "v_mfma_f32_16x16x32_bf16 v[140:143], a[180:183], a[68:71], v[140:143]\n"
      "s_barrier\n"
      "v_mfma_f32_16x16x32_bf16 v[144:147], a[184:187], a[48:51], v[144:147]\n"
      "ds_read_b64_tr_b16 a[168:169], v200\n"
      "s_waitcnt lgkmcnt(15)\n"
      "v_mfma_f32_16x16x32_bf16 v[144:147], a[188:191], a[60:63], v[144:147]\n"
      "ds_read_b64_tr_b16 a[170:171], v200 offset:512\n"
      "v_mfma_f32_16x16x32_bf16 v[148:151], a[184:187], a[52:55], v[148:151]\n"
      "ds_read_b64_tr_b16 a[172:173], v200 offset:4096\n"
      "v_mfma_f32_16x16x32_bf16 v[148:151], a[188:191], a[64:67], v[148:151]\n"
      "ds_read_b64_tr_b16 a[174:175], v200 offset:4608\n"
      "v_mfma_f32_16x16x32_bf16 v[152:155], a[184:187], a[56:59], v[152:155]\n"
      "ds_read_b64_tr_b16 a[176:177], v201\n"
      "v_mfma_f32_16x16x32_bf16 v[152:155], a[188:191], a[68:71], v[152:155]\n"
      "ds_read_b64_tr_b16 a[178:179], v201 offset:512\n"
      "v_mfma_f32_16x16x32_bf16 v[156:159], a[192:195], a[48:51], v[156:159]\n"
      "ds_read_b64_tr_b16 a[180:181], v201 offset:4096\n"
      "s_waitcnt lgkmcnt(15)\n"
      "v_mfma_f32_16x16x32_bf16 v[156:159], a[196:199], a[60:63], v[156:159]\n"
      "ds_read_b64_tr_b16 a[182:183], v201 offset:4608\n"
      "v_mfma_f32_16x16x32_bf16 v[160:163], a[192:195], a[52:55], v[160:163]\n"
      "ds_read_b64_tr_b16 a[184:185], v202\n"
      "s_add_u32 s88, s69, s73\n"
      "s_add_u32 m0, s85, 0x14000\n"
      "v_mfma_f32_16x16x32_bf16 v[160:163], a[196:199], a[64:67], v[160:163]\n"
      "ds_read_b64_tr_b16 a[186:187], v202 offset:512\n"
      "buffer_load_dwordx4 v192, s[20:23], s88 offen lds\n"
      "v_mfma_f32_16x16x32_bf16 v[164:167], a[192:195], a[56:59], v[164:167]\n"
      "ds_read_b64_tr_b16 a[188:189], v202 offset:4096\n"
      "v_mfma_f32_16x16x32_bf16 v[164:167], a[196:199], a[68:71], v[164:167]\n"
      "ds_read_b64_tr_b16 a[190:191], v202 offset:4608\n"
      "buffer_load_dwordx4 v193, s[20:23], s88 offen offset:1024 lds\n"
      "s_waitcnt lgkmcnt(10)\n"
      "v_mfma_f32_16x16x32_bf16 v[120:123], a[168:171], a[72:75], v[120:123]\n"
      "ds_read_b64_tr_b16 a[192:193], v203\n"
      "ds_read_b64 a[224:225], v26 offset:24576\n"
      "s_waitcnt lgkmcnt(10)\n"
      "v_mfma_f32_16x16x32_bf16 v[120:123], a[172:175], a[84:87], v[120:123]\n"
      "ds_read_b64_tr_b16 a[194:195], v203 offset:512\n"
      "ds_read_b64 a[226:227], v27 offset:24576\n"
      "v_mfma_f32_16x16x32_bf16 v[124:127], a[168:171], a[76:79], v[124:127]\n"
      "ds_read_b64_tr_b16 a[196:197], v203 offset:4096\n"
      "ds_read_b64 a[228:229], v26 offset:26624\n"
      "v_mfma_f32_16x16x32_bf16 v[124:127], a[172:175], a[88:91], v[124:127]\n"
      "ds_read_b64_tr_b16 a[198:199], v203 offset:4608\n"
      "ds_read_b64 a[230:231], v27 offset:26624\n"
      "v_mfma_f32_16x16x32_bf16 v[128:131], a[168:171], a[80:83], v[128:131]\n"
      "ds_read_b64 a[232:233], v26 offset:32768\n"
      "buffer_load_dwordx4 v194, s[20:23], s88 offen offset:2048 lds\n"
      "v_mfma_f32_16x16x32_bf16 v[128:131], a[172:175], a[92:95], v[128:131]\n"
      "ds_read_b64 a[234:235], v27 offset:32768\n"
      "s_waitcnt lgkmcnt(15)\n"
      "v_mfma_f32_16x16x32_bf16 v[132:135], a[176:179], a[72:75], v[132:135]\n"
      "ds_read_b64 a[236:237], v26 offset:34816\n"
      "buffer_load_dwordx4 v195, s[20:23], s88 offen offset:3072 lds\n"
      "s_waitcnt lgkmcnt(15)\n"
      "v_mfma_f32_16x16x32_bf16 v[132:135], a[180:183], a[84:87], v[132:135]\n"
      "ds_read_b64 a[238:239], v27 offset:34816\n"
      "s_add_u32 s89, s71, 0\n"
      "s_add_u32 m0, s86, 0x24000\n"
      "v_mfma_f32_16x16x32_bf16 v[136:139], a[176:179], a[76:79], v[136:139]\n"
      "ds_read_b64 a[240:241], v26 offset:40960\n"
      "buffer_load_dwordx4 v216, s[16:19], s89 offen nt lds\n"
      "v_mfma_f32_16x16x32_bf16 v[136:139], a[180:183], a[88:91], v[136:139]\n"
      "ds_read_b64 a[242:243], v27 offset:40960\n"
      "v_mfma_f32_16x16x32_bf16 v[140:143], a[176:179], a[80:83], v[140:143]\n"
      "ds_read_b64 a[244:245], v26 offset:43008\n"
      "buffer_load_dwordx4 v217, s[16:19], s89 offen offset:1024 nt lds\n"
      "v_mfma_f32_16x16x32_bf16 v[140:143], a[180:183], a[92:95], v[140:143]\n"
      "ds_read_b64 a[246:247], v27 offset:43008\n"
      "s_barrier\n"
      "s_waitcnt lgkmcnt(15)\n"
      "v_mfma_f32_16x16x32_bf16 v[144:147], a[184:187], a[72:75], v[144:147]\n"
      "ds_read_b64_tr_b16 a[168:169], v14 offset:16384\n"
      "v_mfma_f32_16x16x32_bf16 v[144:147], a[188:191], a[84:87], v[144:147]\n"
      "ds_read_b64_tr_b16 a[170:171], v14 offset:16896\n"
      "v_mfma_f32_16x16x32_bf16 v[148:151], a[184:187], a[76:79], v[148:151]\n"
      "ds_read_b64_tr_b16 a[172:173], v14 offset:20480\n"
      "v_mfma_f32_16x16x32_bf16 v[148:151], a[188:191], a[88:91], v[148:151]\n"
      "ds_read_b64_tr_b16 a[174:175], v14 offset:20992\n"
      "v_mfma_f32_16x16x32_bf16 v[152:155], a[184:187], a[80:83], v[152:155]\n"
      "ds_read_b64_tr_b16 a[176:177], v15 offset:16384\n"
      "v_mfma_f32_16x16x32_bf16 v[152:155], a[188:191], a[92:95], v[152:155]\n"
      "ds_read_b64_tr_b16 a[178:179], v15 offset:16896\n"
      "s_waitcnt lgkmcnt(15)\n"
      "v_mfma_f32_16x16x32_bf16 v[156:159], a[192:195], a[72:75], v[156:159]\n"
      "ds_read_b64_tr_b16 a[180:181], v15 offset:20480\n"
      "v_mfma_f32_16x16x32_bf16 v[156:159], a[196:199], a[84:87], v[156:159]\n"
      "ds_read_b64_tr_b16 a[182:183], v15 offset:20992\n"
      "v_mfma_f32_16x16x32_bf16 v[160:163], a[192:195], a[76:79], v[160:163]\n"
      "ds_read_b64_tr_b16 a[184:185], v16 offset:16384\n"
      "v_mfma_f32_16x16x32_bf16 v[160:163], a[196:199], a[88:91], v[160:163]\n"
      "ds_read_b64_tr_b16 a[186:187], v16 offset:16896\n"
      "v_mfma_f32_16x16x32_bf16 v[164:167], a[192:195], a[80:83], v[164:167]\n"
      "ds_read_b64_tr_b16 a[188:189], v16 offset:20480\n"
      "v_mfma_f32_16x16x32_bf16 v[164:167], a[196:199], a[92:95], v[164:167]\n"
      "ds_read_b64_tr_b16 a[190:191], v16 offset:20992\n"
      "s_waitcnt lgkmcnt(10)\n"
      "v_mfma_f32_16x16x32_bf16 v[120:123], a[168:171], a[96:99], v[120:123]\n"
      "ds_read_b64_tr_b16 a[192:193], v17 offset:16384\n"
      "s_waitcnt lgkmcnt(9)\n"
      "v_mfma_f32_16x16x32_bf16 v[120:123], a[172:175], a[108:111], v[120:123]\n"
      "ds_read_b64_tr_b16 a[194:195], v17 offset:16896\n"
      "v_mfma_f32_16x16x32_bf16 v[124:127], a[168:171], a[100:103], v[124:127]\n"
      "ds_read_b64_tr_b16 a[196:197], v17 offset:20480\n"
      "v_mfma_f32_16x16x32_bf16 v[124:127], a[172:175], a[112:115], v[124:127]\n"
      "ds_read_b64_tr_b16 a[198:199], v17 offset:20992\n"
      "s_add_u32 s89, s71, 0x4000\n"
      "s_add_u32 m0, s86, 0x26000\n"
      "v_mfma_f32_16x16x32_bf16 v[128:131], a[168:171], a[104:107], v[128:131]\n"
      "buffer_load_dwordx4 v216, s[16:19], s89 offen nt lds\n"
      "v_mfma_f32_16x16x32_bf16 v[128:131], a[172:175], a[116:119], v[128:131]\n"
      "s_waitcnt lgkmcnt(10)\n"
      "v_mfma_f32_16x16x32_bf16 v[132:135], a[176:179], a[96:99], v[132:135]\n"
      "buffer_load_dwordx4 v217, s[16:19], s89 offen offset:1024 nt lds\n"
      "s_waitcnt lgkmcnt(8)\n"
      "v_mfma_f32_16x16x32_bf16 v[132:135], a[180:183], a[108:111], v[132:135]\n"
      "s_add_u32 s89, s71, 0x8000\n"
      "s_add_u32 m0, s86, 0x18000\n"
      "v_mfma_f32_16x16x32_bf16 v[136:139], a[176:179], a[100:103], v[136:139]\n"
      "buffer_load_dwordx4 v216, s[16:19], s89 offen nt lds\n"
      "v_mfma_f32_16x16x32_bf16 v[136:139], a[180:183], a[112:115], v[136:139]\n"
      "v_mfma_f32_16x16x32_bf16 v[140:143], a[176:179], a[104:107], v[140:143]\n"
      "buffer_load_dwordx4 v217, s[16:19], s89 offen offset:1024 nt lds\n"
      "v_mfma_f32_16x16x32_bf16 v[140:143], a[180:183], a[116:119], v[140:143]\n"
      "s_waitcnt vmcnt(10)\n"
      "s_barrier\n"
      "s_waitcnt lgkmcnt(6)\n"
      "v_mfma_f32_16x16x32_bf16 v[144:147], a[184:187], a[96:99], v[144:147]\n"
      "ds_read_b64_tr_b16 a[168:169], v200 offset:16384\n"
      "s_waitcnt lgkmcnt(5)\n"
      "v_mfma_f32_16x16x32_bf16 v[144:147], a[188:191], a[108:111], v[144:147]\n"
      "ds_read_b64_tr_b16 a[170:171], v200 offset:16896\n"
      "v_mfma_f32_16x16x32_bf16 v[148:151], a[184:187], a[100:103], v[148:151]\n"
      "ds_read_b64_tr_b16 a[172:173], v200 offset:20480\n"
      "v_mfma_f32_16x16x32_bf16 v[148:151], a[188:191], a[112:115], v[148:151]\n"
      "ds_read_b64_tr_b16 a[174:175], v200 offset:20992\n"
      "v_mfma_f32_16x16x32_bf16 v[152:155], a[184:187], a[104:107], v[152:155]\n"
      "ds_read_b64_tr_b16 a[176:177], v201 offset:16384\n"
      "v_mfma_f32_16x16x32_bf16 v[152:155], a[188:191], a[116:119], v[152:155]\n"
      "ds_read_b64_tr_b16 a[178:179], v201 offset:16896\n"
      "s_waitcnt lgkmcnt(8)\n"
      "v_mfma_f32_16x16x32_bf16 v[156:159], a[192:195], a[96:99], v[156:159]\n"
      "ds_read_b64_tr_b16 a[180:181], v201 offset:20480\n"
      "s_waitcnt lgkmcnt(7)\n"
      "v_mfma_f32_16x16x32_bf16 v[156:159], a[196:199], a[108:111], v[156:159]\n"
      "ds_read_b64_tr_b16 a[182:183], v201 offset:20992\n"
      "v_mfma_f32_16x16x32_bf16 v[160:163], a[192:195], a[100:103], v[160:163]\n"
      "ds_read_b64_tr_b16 a[184:185], v202 offset:16384\n"
      "v_mfma_f32_16x16x32_bf16 v[160:163], a[196:199], a[112:115], v[160:163]\n"
      "ds_read_b64_tr_b16 a[186:187], v202 offset:16896\n"
      "v_mfma_f32_16x16x32_bf16 v[164:167], a[192:195], a[104:107], v[164:167]\n"
      "ds_read_b64_tr_b16 a[188:189], v202 offset:20480\n"
      "v_mfma_f32_16x16x32_bf16 v[164:167], a[196:199], a[116:119], v[164:167]\n"
      "ds_read_b64_tr_b16 a[190:191], v202 offset:20992\n"
      "s_waitcnt lgkmcnt(10)\n"
      "v_mfma_f32_16x16x32_bf16 v[120:123], a[168:171], a[120:123], v[120:123]\n"
      "ds_read_b64_tr_b16 a[192:193], v203 offset:16384\n"
      "s_waitcnt lgkmcnt(9)\n"
      "v_mfma_f32_16x16x32_bf16 v[120:123], a[172:175], a[132:135], v[120:123]\n"
      "ds_read_b64_tr_b16 a[194:195], v203 offset:16896\n"
      "v_mfma_f32_16x16x32_bf16 v[124:127], a[168:171], a[124:127], v[124:127]\n"
      "ds_read_b64_tr_b16 a[196:197], v203 offset:20480\n"
      "v_mfma_f32_16x16x32_bf16 v[124:127], a[172:175], a[136:139], v[124:127]\n"
      "ds_read_b64_tr_b16 a[198:199], v203 offset:20992\n"
      "s_add_u32 s89, s72, 0\n"
      "s_add_u32 m0, s86, 0x1a000\n"
      "v_mfma_f32_16x16x32_bf16 v[128:131], a[168:171], a[128:131], v[128:131]\n"
      "buffer_load_dwordx4 v216, s[16:19], s89 offen nt lds\n"
      "v_mfma_f32_16x16x32_bf16 v[128:131], a[172:175], a[140:143], v[128:131]\n"
      "s_waitcnt lgkmcnt(10)\n"
      "v_mfma_f32_16x16x32_bf16 v[132:135], a[176:179], a[120:123], v[132:135]\n"
      "buffer_load_dwordx4 v217, s[16:19], s89 offen offset:1024 nt lds\n"
      "s_waitcnt lgkmcnt(8)\n"
      "v_mfma_f32_16x16x32_bf16 v[132:135], a[180:183], a[132:135], v[132:135]\n"
      "s_add_u32 s89, s72, 0x4000\n"
      "s_add_u32 m0, s86, 0x6000\n"
      "v_mfma_f32_16x16x32_bf16 v[136:139], a[176:179], a[124:127], v[136:139]\n"
      "buffer_load_dwordx4 v216, s[16:19], s89 offen nt lds\n"
      "v_mfma_f32_16x16x32_bf16 v[136:139], a[180:183], a[136:139], v[136:139]\n"
      "v_mfma_f32_16x16x32_bf16 v[140:143], a[176:179], a[128:131], v[140:143]\n"
      "buffer_load_dwordx4 v217, s[16:19], s89 offen offset:1024 nt lds\n"
      "v_mfma_f32_16x16x32_bf16 v[140:143], a[180:183], a[140:143], v[140:143]\n"
      "s_waitcnt vmcnt(10)\n"
      "s_barrier\n"
      "s_waitcnt lgkmcnt(6)\n"
      "v_mfma_f32_16x16x32_bf16 v[144:147], a[184:187], a[120:123], v[144:147]\n"
      "ds_read_b64_tr_b16 a[168:169], v14 offset:32768\n"
      "s_waitcnt lgkmcnt(5)\n"
      "v_mfma_f32_16x16x32_bf16 v[144:147], a[188:191], a[132:135], v[144:147]\n"
      "ds_read_b64_tr_b16 a[170:171], v14 offset:33280\n"
      "v_mfma_f32_16x16x32_bf16 v[148:151], a[184:187], a[124:127], v[148:151]\n"
      "ds_read_b64_tr_b16 a[172:173], v14 offset:36864\n"
      "v_mfma_f32_16x16x32_bf16 v[148:151], a[188:191], a[136:139], v[148:151]\n"
      "ds_read_b64_tr_b16 a[174:175], v14 offset:37376\n"
      "v_mfma_f32_16x16x32_bf16 v[152:155], a[184:187], a[128:131], v[152:155]\n"
      "ds_read_b64_tr_b16 a[176:177], v15 offset:32768\n"
      "v_mfma_f32_16x16x32_bf16 v[152:155], a[188:191], a[140:143], v[152:155]\n"
      "ds_read_b64_tr_b16 a[178:179], v15 offset:33280\n"
      "s_waitcnt lgkmcnt(8)\n"
      "v_mfma_f32_16x16x32_bf16 v[156:159], a[192:195], a[120:123], v[156:159]\n"
      "ds_read_b64_tr_b16 a[180:181], v15 offset:36864\n"
      "s_waitcnt lgkmcnt(7)\n"
      "v_mfma_f32_16x16x32_bf16 v[156:159], a[196:199], a[132:135], v[156:159]\n"
      "ds_read_b64_tr_b16 a[182:183], v15 offset:37376\n"
      "v_mfma_f32_16x16x32_bf16 v[160:163], a[192:195], a[124:127], v[160:163]\n"
      "ds_read_b64_tr_b16 a[184:185], v16 offset:32768\n"
      "v_mfma_f32_16x16x32_bf16 v[160:163], a[196:199], a[136:139], v[160:163]\n"
      "ds_read_b64_tr_b16 a[186:187], v16 offset:33280\n"
      "v_mfma_f32_16x16x32_bf16 v[164:167], a[192:195], a[128:131], v[164:167]\n"
      "ds_read_b64_tr_b16 a[188:189], v16 offset:36864\n"
      "v_mfma_f32_16x16x32_bf16 v[164:167], a[196:199], a[140:143], v[164:167]\n"
      "ds_read_b64_tr_b16 a[190:191], v16 offset:37376\n"
      "s_waitcnt lgkmcnt(10)\n"
      "v_mfma_f32_16x16x32_bf16 v[120:123], a[168:171], a[144:147], v[120:123]\n"
      "ds_read_b64_tr_b16 a[192:193], v17 offset:32768\n"
      "s_waitcnt lgkmcnt(9)\n"
      "v_mfma_f32_16x16x32_bf16 v[120:123], a[172:175], a[156:159], v[120:123]\n"
      "ds_read_b64_tr_b16 a[194:195], v17 offset:33280\n"
      "v_mfma_f32_16x16x32_bf16 v[124:127], a[168:171], a[148:151], v[124:127]\n"
      "ds_read_b64_tr_b16 a[196:197], v17 offset:36864\n"
      "v_mfma_f32_16x16x32_bf16 v[124:127], a[172:175], a[160:163], v[124:127]\n"
      "ds_read_b64_tr_b16 a[198:199], v17 offset:37376\n"
      "v_mfma_f32_16x16x32_bf16 v[128:131], a[168:171], a[152:155], v[128:131]\n"
      "v_mfma_f32_16x16x32_bf16 v[128:131], a[172:175], a[164:167], v[128:131]\n"
      "s_waitcnt lgkmcnt(10)\n"
      "v_mfma_f32_16x16x32_bf16 v[132:135], a[176:179], a[144:147], v[132:135]\n"
      "s_waitcnt lgkmcnt(8)\n"
      "v_mfma_f32_16x16x32_bf16 v[132:135], a[180:183], a[156:159], v[132:135]\n"
      "s_add_u32 s89, s72, 0x8000\n"
      "s_add_u32 m0, s86, 0x8000\n"
      "v_mfma_f32_16x16x32_bf16 v[136:139], a[176:179], a[148:151], v[136:139]\n"
      "buffer_load_dwordx4 v216, s[16:19], s89 offen nt lds\n"
      "v_mfma_f32_16x16x32_bf16 v[136:139], a[180:183], a[160:163], v[136:139]\n"
      "v_mfma_f32_16x16x32_bf16 v[140:143], a[176:179], a[152:155], v[140:143]\n"
      "buffer_load_dwordx4 v217, s[16:19], s89 offen offset:1024 nt lds\n"
      "v_mfma_f32_16x16x32_bf16 v[140:143], a[180:183], a[164:167], v[140:143]\n"
      "4:\n"
      "s_barrier\n"
      "s_waitcnt lgkmcnt(6)\n"
      "v_mfma_f32_16x16x32_bf16 v[144:147], a[184:187], a[144:147], v[144:147]\n"
      "v_cvt_pk_bf16_f32 v168, v120, v121\n"
      "v_cvt_pk_bf16_f32 v169, v122, v123\n"
      "s_waitcnt lgkmcnt(4)\n"
      "v_mfma_f32_16x16x32_bf16 v[144:147], a[188:191], a[156:159], v[144:147]\n"
      "v_cvt_pk_bf16_f32 v172, v124, v125\n"
      "v_mfma_f32_16x16x32_bf16 v[148:151], a[184:187], a[148:151], v[148:151]\n"
      "v_cvt_pk_bf16_f32 v173, v126, v127\n"
      "v_mfma_f32_16x16x32_bf16 v[148:151], a[188:191], a[160:163], v[148:151]\n"
      "v_cvt_pk_bf16_f32 v176, v128, v129\n"
      "v_mfma_f32_16x16x32_bf16 v[152:155], a[184:187], a[152:155], v[152:155]\n"
      "v_cvt_pk_bf16_f32 v177, v130, v131\n"
      "v_mfma_f32_16x16x32_bf16 v[152:155], a[188:191], a[164:167], v[152:155]\n"
      "v_cvt_pk_bf16_f32 v170, v132, v133\n"
      "v_cvt_pk_bf16_f32 v171, v134, v135\n"
      "s_waitcnt lgkmcnt(2)\n"
      "v_mfma_f32_16x16x32_bf16 v[156:159], a[192:195], a[144:147], v[156:159]\n"
      "v_cvt_pk_bf16_f32 v174, v136, v137\n"
      "s_waitcnt lgkmcnt(0)\n"
      "v_mfma_f32_16x16x32_bf16 v[156:159], a[196:199], a[156:159], v[156:159]\n"
      "v_cvt_pk_bf16_f32 v175, v138, v139\n"
      "v_mfma_f32_16x16x32_bf16 v[160:163], a[192:195], a[148:151], v[160:163]\n"
      "v_cvt_pk_bf16_f32 v178, v140, v141\n"
      "v_mfma_f32_16x16x32_bf16 v[160:163], a[196:199], a[160:163], v[160:163]\n"
      "v_cvt_pk_bf16_f32 v179, v142, v143\n"
      "v_mfma_f32_16x16x32_bf16 v[164:167], a[192:195], a[152:155], v[164:167]\n"
      "v_mfma_f32_16x16x32_bf16 v[164:167], a[196:199], a[164:167], v[164:167]\n"
      "v_mfma_f32_16x16x32_bf16 v[48:51], a[200:203], v[168:171], v[48:51]\n"
      "v_cvt_pk_bf16_f32 v180, v144, v145\n"
      "v_mfma_f32_16x16x32_bf16 v[52:55], a[200:203], v[172:175], v[52:55]\n"
      "v_cvt_pk_bf16_f32 v181, v146, v147\n"
      "v_mfma_f32_16x16x32_bf16 v[56:59], a[200:203], v[176:179], v[56:59]\n"
      "v_cvt_pk_bf16_f32 v184, v148, v149\n"
      "v_mfma_f32_16x16x32_bf16 v[60:63], a[204:207], v[168:171], v[60:63]\n"
      "v_cvt_pk_bf16_f32 v185, v150, v151\n"
      "v_mfma_f32_16x16x32_bf16 v[64:67], a[204:207], v[172:175], v[64:67]\n"
      "v_cvt_pk_bf16_f32 v188, v152, v153\n"
      "v_mfma_f32_16x16x32_bf16 v[68:71], a[204:207], v[176:179], v[68:71]\n"
      "v_cvt_pk_bf16_f32 v189, v154, v155\n"
      "v_mfma_f32_16x16x32_bf16 v[72:75], a[208:211], v[168:171], v[72:75]\n"
      "v_cvt_pk_bf16_f32 v182, v156, v157\n"
      "v_mfma_f32_16x16x32_bf16 v[76:79], a[208:211], v[172:175], v[76:79]\n"
      "v_cvt_pk_bf16_f32 v183, v158, v159\n"
      "v_mfma_f32_16x16x32_bf16 v[80:83], a[208:211], v[176:179], v[80:83]\n"
      "v_cvt_pk_bf16_f32 v186, v160, v161\n"
      "v_mfma_f32_16x16x32_bf16 v[84:87], a[212:215], v[168:171], v[84:87]\n"
      "v_cvt_pk_bf16_f32 v187, v162, v163\n"
      "v_mfma_f32_16x16x32_bf16 v[88:91], a[212:215], v[172:175], v[88:91]\n"
      "v_cvt_pk_bf16_f32 v190, v164, v165\n"
      "v_mfma_f32_16x16x32_bf16 v[92:95], a[212:215], v[176:179], v[92:95]\n"
      "v_cvt_pk_bf16_f32 v191, v166, v167\n"
      "v_mfma_f32_16x16x32_bf16 v[96:99], a[216:219], v[168:171], v[96:99]\n"
      "v_mfma_f32_16x16x32_bf16 v[100:103], a[216:219], v[172:175], v[100:103]\n"
      "v_xor_b32 v24, 0x2000, v18\n"
      "v_xor_b32 v25, 0x2000, v19\n"
      "v_mfma_f32_16x16x32_bf16 v[104:107], a[216:219], v[176:179], v[104:107]\n"
      "v_add_u32 v14, 0x10000, v18\n"
      "v_add_u32 v15, 0x10000, v19\n"
      "v_mfma_f32_16x16x32_bf16 v[108:111], a[220:223], v[168:171], v[108:111]\n"
      "v_add_u32 v16, 0x20000, v18\n"
      "v_add_u32 v17, 0x20000, v19\n"
      "v_mfma_f32_16x16x32_bf16 v[112:115], a[220:223], v[172:175], v[112:115]\n"
      "v_add_u32 v26, 0x10000, v24\n"
      "v_add_u32 v27, 0x10000, v25\n"
      "v_mfma_f32_16x16x32_bf16 v[116:119], a[220:223], v[176:179], v[116:119]\n"
      "v_add_u32 v28, 0x20000, v24\n"
      "v_add_u32 v29, 0x20000, v25\n"
      "v_mfma_f32_16x16x32_bf16 v[48:51], a[224:227], v[180:183], v[48:51]\n"
      "v_mfma_f32_16x16x32_bf16 v[52:55], a[224:227], v[184:187], v[52:55]\n"
      "v_mfma_f32_16x16x32_bf16 v[56:59], a[224:227], v[188:191], v[56:59]\n"
      "v_mfma_f32_16x16x32_bf16 v[60:63], a[228:231], v[180:183], v[60:63]\n"
      "v_mfma_f32_16x16x32_bf16 v[64:67], a[228:231], v[184:187], v[64:67]\n"
      "v_mfma_f32_16x16x32_bf16 v[68:71], a[228:231], v[188:191], v[68:71]\n"
      "v_mfma_f32_16x16x32_bf16 v[72:75], a[232:235], v[180:183], v[72:75]\n"
      "v_mfma_f32_16x16x32_bf16 v[76:79], a[232:235], v[184:187], v[76:79]\n"
      "v_mfma_f32_16x16x32_bf16 v[80:83], a[232:235], v[188:191], v[80:83]\n"
      "buffer_load_dwordx2 v[34:35], v34, s[36:39], 0 offen sc1\n"
      "v_mfma_f32_16x16x32_bf16 v[84:87], a[236:239], v[180:183], v[84:87]\n"
      "s_add_u32 s89, s73, 0\n"
      "s_add_u32 m0, s86, 0xa000\n"
      "v_mfma_f32_16x16x32_bf16 v[88:91], a[236:239], v[184:187], v[88:91]\n"
      "buffer_load_dwordx4 v216, s[16:19], s89 offen nt lds\n"
      "v_mfma_f32_16x16x32_bf16 v[92:95], a[236:239], v[188:191], v[92:95]\n"
      "v_mfma_f32_16x16x32_bf16 v[96:99], a[240:243], v[180:183], v[96:99]\n"
      "buffer_load_dwordx4 v217, s[16:19], s89 offen offset:1024 nt lds\n"
      "v_mfma_f32_16x16x32_bf16 v[100:103], a[240:243], v[184:187], v[100:103]\n"
      "s_add_u32 s89, s73, 0x8000\n"
      "s_add_u32 m0, s86, 0xe000\n"
      "v_mfma_f32_16x16x32_bf16 v[104:107], a[240:243], v[188:191], v[104:107]\n"
      "buffer_load_dwordx4 v216, s[16:19], s89 offen nt lds\n"
      "v_mfma_f32_16x16x32_bf16 v[108:111], a[244:247], v[180:183], v[108:111]\n"
      "v_mfma_f32_16x16x32_bf16 v[112:115], a[244:247], v[184:187], v[112:115]\n"
      "buffer_load_dwordx4 v217, s[16:19], s89 offen offset:1024 nt lds\n"
      "v_mfma_f32_16x16x32_bf16 v[116:119], a[244:247], v[188:191], v[116:119]\n"
      "v_max3_f32 v240, v48, v49, v50\n"
      "v_max3_f32 v241, v52, v53, v54\n"
      "v_max3_f32 v242, v56, v57, v58\n"
      "v_max3_f32 v240, v240, v51, v60\n"
      "v_max3_f32 v241, v241, v55, v64\n"
      "v_max3_f32 v242, v242, v59, v68\n"
      "v_max3_f32 v240, v240, v61, v62\n"
      "v_max3_f32 v241, v241, v65, v66\n"
      "v_max3_f32 v242, v242, v69, v70\n"
      "v_max3_f32 v240, v240, v63, v72\n"
      "v_max3_f32 v241, v241, v67, v76\n"
      "v_max3_f32 v242, v242, v71, v80\n"
      "v_max3_f32 v240, v240, v73, v74\n"
      "v_max3_f32 v241, v241, v77, v78\n"
      "v_max3_f32 v242, v242, v81, v82\n"
      "v_max3_f32 v240, v240, v75, v84\n"
      "v_max3_f32 v241, v241, v79, v88\n"
      "v_max3_f32 v242, v242, v83, v92\n"
      "v_max3_f32 v240, v240, v85, v86\n"
      "v_max3_f32 v241, v241, v89, v90\n"
      "v_max3_f32 v242, v242, v93, v94\n"
      "v_max3_f32 v240, v240, v87, v96\n"
      "v_max3_f32 v241, v241, v91, v100\n"
      "v_max3_f32 v242, v242, v95, v104\n"
      "v_max3_f32 v240, v240, v97, v98\n"
      "v_max3_f32 v241, v241, v101, v102\n"
      "v_max3_f32 v242, v242, v105, v106\n"
      "v_max3_f32 v240, v240, v99, v108\n"
      "v_max3_f32 v241, v241, v103, v112\n"
      "v_max3_f32 v242, v242, v107, v116\n"
      "v_max3_f32 v240, v240, v109, v110\n"
      "v_max3_f32 v241, v241, v113, v114\n"
      "v_max3_f32 v242, v242, v117, v118\n"
      "v_max_f32 v240, v240, v111\n"
      "v_max_f32 v241, v241, v115\n"
      "v_max_f32 v242, v242, v119\n"
      "v_mov_b32 v243, v240\n"
      "v_mov_b32 v244, v241\n"
      "v_mov_b32 v245, v242\n"
      "v_permlane32_swap_b32 v240, v243\n"
      "v_permlane32_swap_b32 v241, v244\n"
      "v_permlane32_swap_b32 v242, v245\n"
      "v_max_f32 v240, v240, v243\n"
      "v_max_f32 v241, v241, v244\n"
      "v_max_f32 v242, v242, v245\n"
      "v_mov_b32 v243, v240\n"
      "v_mov_b32 v244, v241\n"
      "v_mov_b32 v245, v242\n"
      "v_permlane16_swap_b32 v240, v243\n"
      "v_permlane16_swap_b32 v241, v244\n"
      "v_permlane16_swap_b32 v242, v245\n"
      "v_max_f32 v240, v240, v243\n"
      "v_max_f32 v241, v241, v244\n"
      "v_max_f32 v242, v242, v245\n"
      "v_mul_f32 v246, s96, v240\n"
      "v_mul_f32 v248, s96, v241\n"
      "v_mul_f32 v250, s96, v242\n"
      "v_pk_fma_f32 v[48:49], v[48:49], s[96:97], v[246:247] op_sel_hi:[1,1,0] neg_lo:[0,0,1] neg_hi:[0,0,1]\n"
      "v_pk_fma_f32 v[52:53], v[52:53], s[96:97], v[248:249] op_sel_hi:[1,1,0] neg_lo:[0,0,1] neg_hi:[0,0,1]\n"
      "v_pk_fma_f32 v[56:57], v[56:57], s[96:97], v[250:251] op_sel_hi:[1,1,0] neg_lo:[0,0,1] neg_hi:[0,0,1]\n"
      "v_pk_fma_f32 v[50:51], v[50:51], s[96:97], v[246:247] op_sel_hi:[1,1,0] neg_lo:[0,0,1] neg_hi:[0,0,1]\n"
      "v_pk_fma_f32 v[54:55], v[54:55], s[96:97], v[248:249] op_sel_hi:[1,1,0] neg_lo:[0,0,1] neg_hi:[0,0,1]\n"
      "v_pk_fma_f32 v[58:59], v[58:59], s[96:97], v[250:251] op_sel_hi:[1,1,0] neg_lo:[0,0,1] neg_hi:[0,0,1]\n"
      "v_pk_fma_f32 v[60:61], v[60:61], s[96:97], v[246:247] op_sel_hi:[1,1,0] neg_lo:[0,0,1] neg_hi:[0,0,1]\n"
      "v_pk_fma_f32 v[64:65], v[64:65], s[96:97], v[248:249] op_sel_hi:[1,1,0] neg_lo:[0,0,1] neg_hi:[0,0,1]\n"
      "v_pk_fma_f32 v[68:69], v[68:69], s[96:97], v[250:251] op_sel_hi:[1,1,0] neg_lo:[0,0,1] neg_hi:[0,0,1]\n"
      "v_pk_fma_f32 v[62:63], v[62:63], s[96:97], v[246:247] op_sel_hi:[1,1,0] neg_lo:[0,0,1] neg_hi:[0,0,1]\n"
      "v_pk_fma_f32 v[66:67], v[66:67], s[96:97], v[248:249] op_sel_hi:[1,1,0] neg_lo:[0,0,1] neg_hi:[0,0,1]\n"
      "v_pk_fma_f32 v[70:71], v[70:71], s[96:97], v[250:251] op_sel_hi:[1,1,0] neg_lo:[0,0,1] neg_hi:[0,0,1]\n"
      "v_pk_fma_f32 v[72:73], v[72:73], s[96:97], v[246:247] op_sel_hi:[1,1,0] neg_lo:[0,0,1] neg_hi:[0,0,1]\n"
      "v_pk_fma_f32 v[76:77], v[76:77], s[96:97], v[248:249] op_sel_hi:[1,1,0] neg_lo:[0,0,1] neg_hi:[0,0,1]\n"
      "v_pk_fma_f32 v[80:81], v[80:81], s[96:97], v[250:251] op_sel_hi:[1,1,0] neg_lo:[0,0,1] neg_hi:[0,0,1]\n"
      "v_pk_fma_f32 v[74:75], v[74:75], s[96:97], v[246:247] op_sel_hi:[1,1,0] neg_lo:[0,0,1] neg_hi:[0,0,1]\n"
      "v_pk_fma_f32 v[78:79], v[78:79], s[96:97], v[248:249] op_sel_hi:[1,1,0] neg_lo:[0,0,1] neg_hi:[0,0,1]\n"
      "v_pk_fma_f32 v[82:83], v[82:83], s[96:97], v[250:251] op_sel_hi:[1,1,0] neg_lo:[0,0,1] neg_hi:[0,0,1]\n"
      "v_pk_fma_f32 v[84:85], v[84:85], s[96:97], v[246:247] op_sel_hi:[1,1,0] neg_lo:[0,0,1] neg_hi:[0,0,1]\n"
      "v_pk_fma_f32 v[88:89], v[88:89], s[96:97], v[248:249] op_sel_hi:[1,1,0] neg_lo:[0,0,1] neg_hi:[0,0,1]\n"
      "v_pk_fma_f32 v[92:93], v[92:93], s[96:97], v[250:251] op_sel_hi:[1,1,0] neg_lo:[0,0,1] neg_hi:[0,0,1]\n"
      "v_pk_fma_f32 v[86:87], v[86:87], s[96:97], v[246:247] op_sel_hi:[1,1,0] neg_lo:[0,0,1] neg_hi:[0,0,1]\n"
      "v_pk_fma_f32 v[90:91], v[90:91], s[96:97], v[248:249] op_sel_hi:[1,1,0] neg_lo:[0,0,1] neg_hi:[0,0,1]\n"
      "v_pk_fma_f32 v[94:95], v[94:95], s[96:97], v[250:251] op_sel_hi:[1,1,0] neg_lo:[0,0,1] neg_hi:[0,0,1]\n"
      "v_pk_fma_f32 v[96:97], v[96:97], s[96:97], v[246:247] op_sel_hi:[1,1,0] neg_lo:[0,0,1] neg_hi:[0,0,1]\n"
      "v_pk_fma_f32 v[100:101], v[100:101], s[96:97], v[248:249] op_sel_hi:[1,1,0] neg_lo:[0,0,1] neg_hi:[0,0,1]\n"
      "v_pk_fma_f32 v[104:105], v[104:105], s[96:97], v[250:251] op_sel_hi:[1,1,0] neg_lo:[0,0,1] neg_hi:[0,0,1]\n"
      "v_pk_fma_f32 v[98:99], v[98:99], s[96:97], v[246:247] op_sel_hi:[1,1,0] neg_lo:[0,0,1] neg_hi:[0,0,1]\n"
      "v_pk_fma_f32 v[102:103], v[102:103], s[96:97], v[248:249] op_sel_hi:[1,1,0] neg_lo:[0,0,1] neg_hi:[0,0,1]\n"
      "v_pk_fma_f32 v[106:107], v[106:107], s[96:97], v[250:251] op_sel_hi:[1,1,0] neg_lo:[0,0,1] neg_hi:[0,0,1]\n"
      "v_pk_fma_f32 v[108:109], v[108:109], s[96:97], v[246:247] op_sel_hi:[1,1,0] neg_lo:[0,0,1] neg_hi:[0,0,1]\n"
      "v_pk_fma_f32 v[112:113], v[112:113], s[96:97], v[248:249] op_sel_hi:[1,1,0] neg_lo:[0,0,1] neg_hi:[0,0,1]\n"
      "v_pk_fma_f32 v[116:117], v[116:117], s[96:97], v[250:251] op_sel_hi:[1,1,0] neg_lo:[0,0,1] neg_hi:[0,0,1]\n"
      "v_pk_fma_f32 v[110:111], v[110:111], s[96:97], v[246:247] op_sel_hi:[1,1,0] neg_lo:[0,0,1] neg_hi:[0,0,1]\n"
      "v_pk_fma_f32 v[114:115], v[114:115], s[96:97], v[248:249] op_sel_hi:[1,1,0] neg_lo:[0,0,1] neg_hi:[0,0,1]\n"
      "v_pk_fma_f32 v[118:119], v[118:119], s[96:97], v[250:251] op_sel_hi:[1,1,0] neg_lo:[0,0,1] neg_hi:[0,0,1]\n"
      "v_exp_f32 v48, v48\n"
      "v_exp_f32 v52, v52\n"
      "v_exp_f32 v56, v56\n"
      "v_exp_f32 v49, v49\n"
      "v_exp_f32 v53, v53\n"
      "v_exp_f32 v57, v57\n"
      "v_exp_f32 v50, v50\n"
      "v_exp_f32 v54, v54\n"
      "v_exp_f32 v58, v58\n"
      "v_exp_f32 v51, v51\n"
      "v_exp_f32 v55, v55\n"
      "v_exp_f32 v59, v59\n"
      "v_exp_f32 v60, v60\n"
      "v_exp_f32 v64, v64\n"
      "v_exp_f32 v68, v68\n"
      "v_exp_f32 v61, v61\n"
      "v_exp_f32 v65, v65\n"
      "v_exp_f32 v69, v69\n"
      "v_exp_f32 v62, v62\n"
      "v_exp_f32 v66, v66\n"
      "v_exp_f32 v70, v70\n"
      "v_exp_f32 v63, v63\n"
      "v_exp_f32 v67, v67\n"
      "v_exp_f32 v71, v71\n"
      "v_exp_f32 v72, v72\n"
      "v_exp_f32 v76, v76\n"
      "v_exp_f32 v80, v80\n"
      "v_exp_f32 v73, v73\n"
      "v_exp_f32 v77, v77\n"
      "v_exp_f32 v81, v81\n"
      "v_exp_f32 v74, v74\n"
      "v_exp_f32 v78, v78\n"
      "v_exp_f32 v82, v82\n"
      "v_exp_f32 v75, v75\n"
      "v_exp_f32 v79, v79\n"
      "v_exp_f32 v83, v83\n"
      "v_exp_f32 v84, v84\n"
      "v_exp_f32 v88, v88\n"
      "v_exp_f32 v92, v92\n"
      "v_exp_f32 v85, v85\n"
      "v_exp_f32 v89, v89\n"
      "v_exp_f32 v93, v93\n"
      "v_exp_f32 v86, v86\n"
      "v_exp_f32 v90, v90\n"
      "v_exp_f32 v94, v94\n"
      "v_exp_f32 v87, v87\n"
      "v_exp_f32 v91, v91\n"
      "v_exp_f32 v95, v95\n"
      "v_exp_f32 v96, v96\n"
      "v_exp_f32 v100, v100\n"
      "v_exp_f32 v104, v104\n"
      "v_exp_f32 v97, v97\n"
      "v_exp_f32 v101, v101\n"
      "v_exp_f32 v105, v105\n"
      "v_exp_f32 v98, v98\n"
      "v_exp_f32 v102, v102\n"
      "v_exp_f32 v106, v106\n"
      "s_add_u32 s88, s74, s82\n"
      "s_add_u32 m0, s85, 0x1c000\n"
      "v_exp_f32 v99, v99\n"
      "buffer_load_dwordx4 v10, s[24:27], s88 offen lds\n"
      "v_exp_f32 v103, v103\n"
      "v_exp_f32 v107, v107\n"
      "v_exp_f32 v108, v108\n"
      "v_exp_f32 v112, v112\n"
      "v_exp_f32 v116, v116\n"
      "v_exp_f32 v109, v109\n"
      "v_exp_f32 v113, v113\n"
      "v_exp_f32 v117, v117\n"
      "v_exp_f32 v110, v110\n"
      "v_exp_f32 v114, v114\n"
      "v_exp_f32 v118, v118\n"
      "v_exp_f32 v111, v111\n"
      "buffer_load_dwordx4 v11, s[24:27], s88 offen offset:1024 lds\n"
      "v_exp_f32 v115, v115\n"
      "v_exp_f32 v119, v119\n"
      "v_pk_add_f32 v[246:247], v[48:49], v[50:51]\n"
      "v_pk_add_f32 v[248:249], v[52:53], v[54:55]\n"
      "v_pk_add_f32 v[250:251], v[56:57], v[58:59]\n"
      "v_pk_add_f32 v[246:247], v[246:247], v[60:61]\n"
      "v_pk_add_f32 v[248:249], v[248:249], v[64:65]\n"
      "v_pk_add_f32 v[250:251], v[250:251], v[68:69]\n"
      "v_pk_add_f32 v[246:247], v[246:247], v[62:63]\n"
      "v_pk_add_f32 v[248:249], v[248:249], v[66:67]\n"
      "v_pk_add_f32 v[250:251], v[250:251], v[70:71]\n"
      "v_pk_add_f32 v[246:247], v[246:247], v[72:73]\n"
      "buffer_load_dwordx4 v12, s[24:27], s88 offen offset:2048 lds\n"
      "v_pk_add_f32 v[248:249], v[248:249], v[76:77]\n"
      "v_pk_add_f32 v[250:251], v[250:251], v[80:81]\n"
      "v_pk_add_f32 v[246:247], v[246:247], v[74:75]\n"
      "v_pk_add_f32 v[248:249], v[248:249], v[78:79]\n"
      "v_pk_add_f32 v[250:251], v[250:251], v[82:83]\n"
      "v_pk_add_f32 v[246:247], v[246:247], v[84:85]\n"
      "v_pk_add_f32 v[248:249], v[248:249], v[88:89]\n"
      "v_pk_add_f32 v[250:251], v[250:251], v[92:93]\n"
      "v_pk_add_f32 v[246:247], v[246:247], v[86:87]\n"
      "v_pk_add_f32 v[248:249], v[248:249], v[90:91]\n"
      "v_pk_add_f32 v[250:251], v[250:251], v[94:95]\n"
      "v_pk_add_f32 v[246:247], v[246:247], v[96:97]\n"
      "buffer_load_dwordx4 v13, s[24:27], s88 offen offset:3072 lds\n"
      "v_pk_add_f32 v[248:249], v[248:249], v[100:101]\n"
      "v_pk_add_f32 v[250:251], v[250:251], v[104:105]\n"
      "v_pk_add_f32 v[246:247], v[246:247], v[98:99]\n"
      "v_pk_add_f32 v[248:249], v[248:249], v[102:103]\n"
      "v_pk_add_f32 v[250:251], v[250:251], v[106:107]\n"
      "v_pk_add_f32 v[246:247], v[246:247], v[108:109]\n"
      "v_pk_add_f32 v[248:249], v[248:249], v[112:113]\n"
      "v_pk_add_f32 v[250:251], v[250:251], v[116:117]\n"
      "v_pk_add_f32 v[246:247], v[246:247], v[110:111]\n"
      "v_pk_add_f32 v[248:249], v[248:249], v[114:115]\n"
      "v_pk_add_f32 v[250:251], v[250:251], v[118:119]\n"
      "s_add_u32 s88, s75, s82\n"
      "s_add_u32 m0, s85, 0x10000\n"
      "v_add_f32 v240, v246, v247\n"
      "buffer_load_dwordx4 v10, s[24:27], s88 offen lds\n"
      "v_add_f32 v241, v248, v249\n"
      "v_add_f32 v242, v250, v251\n"
      "v_mov_b32 v243, v240\n"
      "v_mov_b32 v244, v241\n"
      "v_mov_b32 v245, v242\n"
      "v_permlane32_swap_b32 v240, v243\n"
      "v_permlane32_swap_b32 v241, v244\n"
      "v_permlane32_swap_b32 v242, v245\n"
      "v_add_f32 v240, v240, v243\n"
      "v_add_f32 v241, v241, v244\n"
      "v_add_f32 v242, v242, v245\n"
      "v_mov_b32 v243, v240\n"
      "buffer_load_dwordx4 v11, s[24:27], s88 offen offset:1024 lds\n"
      "v_mov_b32 v244, v241\n"
      "v_mov_b32 v245, v242\n"
      "v_permlane16_swap_b32 v240, v243\n"
      "v_permlane16_swap_b32 v241, v244\n"
      "v_permlane16_swap_b32 v242, v245\n"
      "v_add_f32 v240, v240, v243\n"
      "v_add_f32 v241, v241, v244\n"
      "v_add_f32 v242, v242, v245\n"
      "v_rcp_f32 v246, v240\n"
      "v_rcp_f32 v248, v241\n"
      "v_rcp_f32 v250, v242\n"
      "v_pk_mul_f32 v[48:49], v[48:49], v[246:247] op_sel_hi:[1,0]\n"
      "buffer_load_dwordx4 v12, s[24:27], s88 offen offset:2048 lds\n"
      "v_pk_mul_f32 v[52:53], v[52:53], v[248:249] op_sel_hi:[1,0]\n"
      "v_pk_mul_f32 v[56:57], v[56:57], v[250:251] op_sel_hi:[1,0]\n"
      "v_pk_mul_f32 v[50:51], v[50:51], v[246:247] op_sel_hi:[1,0]\n"
      "v_pk_mul_f32 v[54:55], v[54:55], v[248:249] op_sel_hi:[1,0]\n"
      "v_pk_mul_f32 v[58:59], v[58:59], v[250:251] op_sel_hi:[1,0]\n"
      "v_pk_mul_f32 v[60:61], v[60:61], v[246:247] op_sel_hi:[1,0]\n"
      "v_pk_mul_f32 v[64:65], v[64:65], v[248:249] op_sel_hi:[1,0]\n"
      "v_pk_mul_f32 v[68:69], v[68:69], v[250:251] op_sel_hi:[1,0]\n"
      "v_pk_mul_f32 v[62:63], v[62:63], v[246:247] op_sel_hi:[1,0]\n"
      "v_pk_mul_f32 v[66:67], v[66:67], v[248:249] op_sel_hi:[1,0]\n"
      "v_pk_mul_f32 v[70:71], v[70:71], v[250:251] op_sel_hi:[1,0]\n"
      "v_pk_mul_f32 v[72:73], v[72:73], v[246:247] op_sel_hi:[1,0]\n"
      "buffer_load_dwordx4 v13, s[24:27], s88 offen offset:3072 lds\n"
      "v_pk_mul_f32 v[76:77], v[76:77], v[248:249] op_sel_hi:[1,0]\n"
      "v_pk_mul_f32 v[80:81], v[80:81], v[250:251] op_sel_hi:[1,0]\n"
      "v_pk_mul_f32 v[74:75], v[74:75], v[246:247] op_sel_hi:[1,0]\n"
      "v_pk_mul_f32 v[78:79], v[78:79], v[248:249] op_sel_hi:[1,0]\n"
      "v_pk_mul_f32 v[82:83], v[82:83], v[250:251] op_sel_hi:[1,0]\n"
      "v_pk_mul_f32 v[84:85], v[84:85], v[246:247] op_sel_hi:[1,0]\n"
      "v_pk_mul_f32 v[88:89], v[88:89], v[248:249] op_sel_hi:[1,0]\n"
      "v_pk_mul_f32 v[92:93], v[92:93], v[250:251] op_sel_hi:[1,0]\n"
      "v_pk_mul_f32 v[86:87], v[86:87], v[246:247] op_sel_hi:[1,0]\n"
      "v_pk_mul_f32 v[90:91], v[90:91], v[248:249] op_sel_hi:[1,0]\n"
      "s_barrier\n"
      "v_pk_mul_f32 v[94:95], v[94:95], v[250:251] op_sel_hi:[1,0]\n"
      "ds_read_b64_tr_b16 a[208:209], v20\n"
      "v_pk_mul_f32 v[96:97], v[96:97], v[246:247] op_sel_hi:[1,0]\n"
      "v_pk_mul_f32 v[100:101], v[100:101], v[248:249] op_sel_hi:[1,0]\n"
      "ds_read_b64_tr_b16 a[210:211], v20 offset:2048\n"
      "v_pk_mul_f32 v[104:105], v[104:105], v[250:251] op_sel_hi:[1,0]\n"
      "v_pk_mul_f32 v[98:99], v[98:99], v[246:247] op_sel_hi:[1,0]\n"
      "ds_read_b64_tr_b16 a[212:213], v20 offset:8192\n"
      "v_pk_mul_f32 v[102:103], v[102:103], v[248:249] op_sel_hi:[1,0]\n"
      "v_pk_mul_f32 v[106:107], v[106:107], v[250:251] op_sel_hi:[1,0]\n"
      "v_pk_mul_f32 v[108:109], v[108:109], v[246:247] op_sel_hi:[1,0]\n"
      "ds_read_b64_tr_b16 a[214:215], v20 offset:10240\n"
      "v_pk_mul_f32 v[112:113], v[112:113], v[248:249] op_sel_hi:[1,0]\n"
      "v_pk_mul_f32 v[116:117], v[116:117], v[250:251] op_sel_hi:[1,0]\n"
      "ds_read_b64_tr_b16 a[216:217], v20 offset:16384\n"
      "v_pk_mul_f32 v[110:111], v[110:111], v[246:247] op_sel_hi:[1,0]\n"
      "v_pk_mul_f32 v[114:115], v[114:115], v[248:249] op_sel_hi:[1,0]\n"
      "ds_read_b64_tr_b16 a[218:219], v20 offset:18432\n"
      "v_pk_mul_f32 v[118:119], v[118:119], v[250:251] op_sel_hi:[1,0]\n"
      "v_cvt_pk_bf16_f32 v48, v48, v49\n"
      "v_cvt_pk_bf16_f32 v49, v50, v51\n"
      "ds_read_b64_tr_b16 a[220:221], v21\n"
      "v_cvt_pk_bf16_f32 v50, v60, v61\n"
      "v_cvt_pk_bf16_f32 v51, v62, v63\n"
      "ds_read_b64_tr_b16 a[222:223], v21 offset:2048\n"
      "v_cvt_pk_bf16_f32 v52, v52, v53\n"
      "v_cvt_pk_bf16_f32 v53, v54, v55\n"
      "ds_read_b64_tr_b16 a[224:225], v21 offset:8192\n"
      "v_cvt_pk_bf16_f32 v54, v64, v65\n"
      "v_cvt_pk_bf16_f32 v55, v66, v67\n"
      "v_cvt_pk_bf16_f32 v56, v56, v57\n"
      "ds_read_b64_tr_b16 a[226:227], v21 offset:10240\n"
      "v_cvt_pk_bf16_f32 v57, v58, v59\n"
      "v_cvt_pk_bf16_f32 v58, v68, v69\n"
      "ds_read_b64_tr_b16 a[228:229], v21 offset:16384\n"
      "v_cvt_pk_bf16_f32 v59, v70, v71\n"
      "v_cvt_pk_bf16_f32 v72, v72, v73\n"
      "ds_read_b64_tr_b16 a[230:231], v21 offset:18432\n"
      "v_cvt_pk_bf16_f32 v73, v74, v75\n"
      "v_cvt_pk_bf16_f32 v74, v84, v85\n"
      "v_cvt_pk_bf16_f32 v75, v86, v87\n"
      "ds_read_b64_tr_b16 a[232:233], v22\n"
      "v_cvt_pk_bf16_f32 v76, v76, v77\n"
      "v_cvt_pk_bf16_f32 v77, v78, v79\n"
      "ds_read_b64_tr_b16 a[234:235], v22 offset:2048\n"
      "v_cvt_pk_bf16_f32 v78, v88, v89\n"
      "v_cvt_pk_bf16_f32 v79, v90, v91\n"
      "ds_read_b64_tr_b16 a[236:237], v22 offset:8192\n"
      "v_cvt_pk_bf16_f32 v80, v80, v81\n"
      "v_cvt_pk_bf16_f32 v81, v82, v83\n"
      "v_cvt_pk_bf16_f32 v82, v92, v93\n"
      "ds_read_b64_tr_b16 a[238:239], v22 offset:10240\n"
      "v_cvt_pk_bf16_f32 v83, v94, v95\n"
      "v_cvt_pk_bf16_f32 v96, v96, v97\n"
      "ds_read_b64_tr_b16 a[240:241], v22 offset:16384\n"
      "v_cvt_pk_bf16_f32 v97, v98, v99\n"
      "v_cvt_pk_bf16_f32 v98, v108, v109\n"
      "ds_read_b64_tr_b16 a[242:243], v22 offset:18432\n"
      "v_cvt_pk_bf16_f32 v99, v110, v111\n"
      "v_cvt_pk_bf16_f32 v100, v100, v101\n"
      "v_cvt_pk_bf16_f32 v101, v102, v103\n"
      "v_cvt_pk_bf16_f32 v102, v112, v113\n"
      "v_cvt_pk_bf16_f32 v103, v114, v115\n"
      "v_cvt_pk_bf16_f32 v104, v104, v105\n"
      "v_cvt_pk_bf16_f32 v105, v106, v107\n"
      "v_cvt_pk_bf16_f32 v106, v116, v117\n"
      "v_cvt_pk_bf16_f32 v107, v118, v119\n"
      "s_waitcnt lgkmcnt(15)\n"
      "v_mfma_f32_16x16x32_bf16 v[120:123], a[208:211], v[48:51], 0\n"
      "ds_read_b64_tr_b16 a[244:245], v23\n"
      "s_waitcnt lgkmcnt(15)\n"
      "v_mfma_f32_16x16x32_bf16 v[120:123], a[212:215], v[72:75], v[120:123]\n"
      "ds_read_b64_tr_b16 a[246:247], v23 offset:2048\n"
      "s_waitcnt lgkmcnt(14)\n"
      "v_mfma_f32_16x16x32_bf16 v[120:123], a[216:219], v[96:99], v[120:123]\n"
      "ds_read_b64_tr_b16 a[248:249], v23 offset:8192\n"
      "v_mfma_f32_16x16x32_bf16 v[124:127], a[208:211], v[52:55], 0\n"
      "ds_read_b64_tr_b16 a[250:251], v23 offset:10240\n"
      "v_mfma_f32_16x16x32_bf16 v[124:127], a[212:215], v[76:79], v[124:127]\n"
      "ds_read_b64_tr_b16 a[252:253], v23 offset:16384\n"
      "v_mfma_f32_16x16x32_bf16 v[124:127], a[216:219], v[100:103], v[124:127]\n"
      "ds_read_b64_tr_b16 a[254:255], v23 offset:18432\n"
      "v_mfma_f32_16x16x32_bf16 v[128:131], a[208:211], v[56:59], 0\n"
      "v_mfma_f32_16x16x32_bf16 v[128:131], a[212:215], v[80:83], v[128:131]\n"
      "v_mfma_f32_16x16x32_bf16 v[128:131], a[216:219], v[104:107], v[128:131]\n"
      "s_waitcnt lgkmcnt(15)\n"
      "v_mfma_f32_16x16x32_bf16 v[132:135], a[220:223], v[48:51], 0\n"
      "s_waitcnt lgkmcnt(14)\n"
      "v_mfma_f32_16x16x32_bf16 v[132:135], a[224:227], v[72:75], v[132:135]\n"
      "s_waitcnt lgkmcnt(12)\n"
      "v_mfma_f32_16x16x32_bf16 v[132:135], a[228:231], v[96:99], v[132:135]\n"
      "v_mfma_f32_16x16x32_bf16 v[136:139], a[220:223], v[52:55], 0\n"
      "v_cvt_pk_bf16_f32 v60, v120, v121\n"
      "v_mfma_f32_16x16x32_bf16 v[136:139], a[224:227], v[76:79], v[136:139]\n"
      "v_cvt_pk_bf16_f32 v61, v122, v123\n"
      "v_mfma_f32_16x16x32_bf16 v[136:139], a[228:231], v[100:103], v[136:139]\n"
      "v_cvt_pk_bf16_f32 v64, v124, v125\n"
      "v_mfma_f32_16x16x32_bf16 v[140:143], a[220:223], v[56:59], 0\n"
      "v_cvt_pk_bf16_f32 v65, v126, v127\n"
      "v_mfma_f32_16x16x32_bf16 v[140:143], a[224:227], v[80:83], v[140:143]\n"
      "v_cvt_pk_bf16_f32 v68, v128, v129\n"
      "v_mfma_f32_16x16x32_bf16 v[140:143], a[228:231], v[104:107], v[140:143]\n"
      "v_cvt_pk_bf16_f32 v69, v130, v131\n"
      "s_waitcnt lgkmcnt(10)\n"
      "v_mfma_f32_16x16x32_bf16 v[144:147], a[232:235], v[48:51], 0\n"
      "s_waitcnt lgkmcnt(8)\n"
      "v_mfma_f32_16x16x32_bf16 v[144:147], a[236:239], v[72:75], v[144:147]\n"
      "s_waitcnt lgkmcnt(6)\n"
      "v_mfma_f32_16x16x32_bf16 v[144:147], a[240:243], v[96:99], v[144:147]\n"
      "v_cvt_pk_bf16_f32 v62, v132, v133\n"
      "s_add_u32 s89, s73, 0x4000\n"
      "s_add_u32 m0, s86, 0xc000\n"
      "v_mfma_f32_16x16x32_bf16 v[148:151], a[232:235], v[52:55], 0\n"
      "v_cvt_pk_bf16_f32 v63, v134, v135\n"
      "buffer_load_dwordx4 v216, s[16:19], s89 offen nt lds\n"
      "v_mfma_f32_16x16x32_bf16 v[148:151], a[236:239], v[76:79], v[148:151]\n"
      "v_cvt_pk_bf16_f32 v66, v136, v137\n"
      "v_mfma_f32_16x16x32_bf16 v[148:151], a[240:243], v[100:103], v[148:151]\n"
      "s_waitcnt vmcnt(20) lgkmcnt(0)\n"
      "s_barrier\n"
      "v_cvt_pk_bf16_f32 v67, v138, v139\n"
      "buffer_load_dwordx4 v217, s[16:19], s89 offen offset:1024 nt lds\n"
      "v_mfma_f32_16x16x32_bf16 v[152:155], a[232:235], v[56:59], 0\n"
      "ds_read_b64_tr_b16 a[208:209], v40 offset:49152\n"
      "v_cvt_pk_bf16_f32 v70, v140, v141\n"
      "v_mfma_f32_16x16x32_bf16 v[152:155], a[236:239], v[80:83], v[152:155]\n"
      "ds_read_b64_tr_b16 a[210:211], v40 offset:51200\n"
      "v_cvt_pk_bf16_f32 v71, v142, v143\n"
      "v_mfma_f32_16x16x32_bf16 v[152:155], a[240:243], v[104:107], v[152:155]\n"
      "ds_read_b64_tr_b16 a[212:213], v40 offset:57344\n"
      "v_mfma_f32_16x16x32_bf16 v[156:159], a[244:247], v[48:51], 0\n"
      "ds_read_b64_tr_b16 a[214:215], v40 offset:59392\n"
      "v_mfma_f32_16x16x32_bf16 v[156:159], a[248:251], v[72:75], v[156:159]\n"
      "ds_read_b64_tr_b16 a[216:217], v40\n"
      "v_mfma_f32_16x16x32_bf16 v[156:159], a[252:255], v[96:99], v[156:159]\n"
      "ds_read_b64_tr_b16 a[218:219], v40 offset:2048\n"
      "v_cvt_pk_bf16_f32 v84, v144, v145\n"
      "v_mfma_f32_16x16x32_bf16 v[160:163], a[244:247], v[52:55], 0\n"
      "ds_read_b64_tr_b16 a[220:221], v41 offset:49152\n"
      "v_cvt_pk_bf16_f32 v85, v146, v147\n"
      "v_mfma_f32_16x16x32_bf16 v[160:163], a[248:251], v[76:79], v[160:163]\n"
      "ds_read_b64_tr_b16 a[222:223], v41 offset:51200\n"
      "v_cvt_pk_bf16_f32 v88, v148, v149\n"
      "v_mfma_f32_16x16x32_bf16 v[160:163], a[252:255], v[100:103], v[160:163]\n"
      "ds_read_b64_tr_b16 a[224:225], v41 offset:57344\n"
      "v_cvt_pk_bf16_f32 v89, v150, v151\n"
      "v_mfma_f32_16x16x32_bf16 v[164:167], a[244:247], v[56:59], 0\n"
      "ds_read_b64_tr_b16 a[226:227], v41 offset:59392\n"
      "v_cvt_pk_bf16_f32 v92, v152, v153\n"
      "v_mfma_f32_16x16x32_bf16 v[164:167], a[248:251], v[80:83], v[164:167]\n"
      "ds_read_b64_tr_b16 a[228:229], v41\n"
      "v_cvt_pk_bf16_f32 v93, v154, v155\n"
      "v_mfma_f32_16x16x32_bf16 v[164:167], a[252:255], v[104:107], v[164:167]\n"
      "ds_read_b64_tr_b16 a[230:231], v41 offset:2048\n"
      "s_waitcnt lgkmcnt(10)\n"
      "v_mfma_f32_16x16x32_bf16 v[120:123], a[208:211], v[48:51], 0\n"
      "ds_read_b64_tr_b16 a[232:233], v42 offset:49152\n"
      "s_waitcnt lgkmcnt(9)\n"
      "v_mfma_f32_16x16x32_bf16 v[120:123], a[212:215], v[72:75], v[120:123]\n"
      "ds_read_b64_tr_b16 a[234:235], v42 offset:51200\n"
      "v_cvt_pk_bf16_f32 v86, v156, v157\n"
      "s_waitcnt lgkmcnt(8)\n"
      "v_mfma_f32_16x16x32_bf16 v[120:123], a[216:219], v[96:99], v[120:123]\n"
      "ds_read_b64_tr_b16 a[236:237], v42 offset:57344\n"
      "v_cvt_pk_bf16_f32 v87, v158, v159\n"
      "v_mfma_f32_16x16x32_bf16 v[124:127], a[208:211], v[52:55], 0\n"
      "ds_read_b64_tr_b16 a[238:239], v42 offset:59392\n"
      "v_cvt_pk_bf16_f32 v90, v160, v161\n"
      "v_mfma_f32_16x16x32_bf16 v[124:127], a[212:215], v[76:79], v[124:127]\n"
      "ds_read_b64_tr_b16 a[240:241], v42\n"
      "v_cvt_pk_bf16_f32 v91, v162, v163\n"
      "v_mfma_f32_16x16x32_bf16 v[124:127], a[216:219], v[100:103], v[124:127]\n"
      "ds_read_b64_tr_b16 a[242:243], v42 offset:2048\n"
      "v_cvt_pk_bf16_f32 v94, v164, v165\n"
      "v_mfma_f32_16x16x32_bf16 v[128:131], a[208:211], v[56:59], 0\n"
      "ds_read_b64_tr_b16 a[244:245], v43 offset:49152\n"
      "v_cvt_pk_bf16_f32 v95, v166, v167\n"
      "v_mfma_f32_16x16x32_bf16 v[128:131], a[212:215], v[80:83], v[128:131]\n"
      "ds_read_b64_tr_b16 a[246:247], v43 offset:51200\n"
      "v_mfma_f32_16x16x32_bf16 v[128:131], a[216:219], v[104:107], v[128:131]\n"
      "ds_read_b64_tr_b16 a[248:249], v43 offset:57344\n"
      "s_waitcnt lgkmcnt(13)\n"
      "v_mfma_f32_16x16x32_bf16 v[132:135], a[220:223], v[48:51], 0\n"
      "ds_read_b64_tr_b16 a[250:251], v43 offset:59392\n"
      "ds_write_b128 v30, v[60:63]\n"
      "s_waitcnt lgkmcnt(13)\n"
      "v_mfma_f32_16x16x32_bf16 v[132:135], a[224:227], v[72:75], v[132:135]\n"
      "ds_read_b64_tr_b16 a[252:253], v43\n"
      "s_waitcnt lgkmcnt(12)\n"
      "v_mfma_f32_16x16x32_bf16 v[132:135], a[228:231], v[96:99], v[132:135]\n"
      "ds_read_b64_tr_b16 a[254:255], v43 offset:2048\n"
      "ds_write_b128 v30, v[64:67] offset:8192\n"
      "v_mfma_f32_16x16x32_bf16 v[136:139], a[220:223], v[52:55], 0\n"
      "v_cvt_pk_bf16_f32 v108, v120, v121\n"
      "v_mfma_f32_16x16x32_bf16 v[136:139], a[224:227], v[76:79], v[136:139]\n"
      "ds_write_b128 v30, v[68:71] offset:16384\n"
      "v_cvt_pk_bf16_f32 v109, v122, v123\n"
      "v_mfma_f32_16x16x32_bf16 v[136:139], a[228:231], v[100:103], v[136:139]\n"
      "v_cvt_pk_bf16_f32 v112, v124, v125\n"
      "v_mfma_f32_16x16x32_bf16 v[140:143], a[220:223], v[56:59], 0\n"
      "ds_write_b128 v30, v[84:87] offset:1024\n"
      "v_cvt_pk_bf16_f32 v113, v126, v127\n"
      "s_add_u32 s88, s76, s82\n"
      "s_add_u32 m0, s85, 0x20000\n"
      "v_mfma_f32_16x16x32_bf16 v[140:143], a[224:227], v[80:83], v[140:143]\n"
      "v_cvt_pk_bf16_f32 v116, v128, v129\n"
      "buffer_load_dwordx4 v10, s[24:27], s88 offen lds\n"
      "v_mfma_f32_16x16x32_bf16 v[140:143], a[228:231], v[104:107], v[140:143]\n"
      "ds_write_b128 v30, v[88:91] offset:9216\n"
      "v_cvt_pk_bf16_f32 v117, v130, v131\n"
      "s_waitcnt lgkmcnt(15)\n"
      "v_mfma_f32_16x16x32_bf16 v[144:147], a[232:235], v[48:51], 0\n"
      "buffer_load_dwordx4 v11, s[24:27], s88 offen offset:1024 lds\n"
      "s_waitcnt lgkmcnt(13)\n"
      "v_mfma_f32_16x16x32_bf16 v[144:147], a[236:239], v[72:75], v[144:147]\n"
      "ds_write_b128 v30, v[92:95] offset:17408\n"
      "s_waitcnt lgkmcnt(12)\n"
      "v_mfma_f32_16x16x32_bf16 v[144:147], a[240:243], v[96:99], v[144:147]\n"
      "v_cvt_pk_bf16_f32 v110, v132, v133\n"
      "v_mfma_f32_16x16x32_bf16 v[148:151], a[232:235], v[52:55], 0\n"
      "v_cvt_pk_bf16_f32 v111, v134, v135\n"
      "buffer_load_dwordx4 v12, s[24:27], s88 offen offset:2048 lds\n"
      "v_mfma_f32_16x16x32_bf16 v[148:151], a[236:239], v[76:79], v[148:151]\n"
      "v_cvt_pk_bf16_f32 v114, v136, v137\n"
      "v_mfma_f32_16x16x32_bf16 v[148:151], a[240:243], v[100:103], v[148:151]\n"
      "s_waitcnt vmcnt(18) lgkmcnt(0)\n"
      "s_barrier\n"
      "v_cvt_pk_bf16_f32 v115, v138, v139\n"
      "buffer_load_dwordx4 v13, s[24:27], s88 offen offset:3072 lds\n"
      "v_mfma_f32_16x16x32_bf16 v[152:155], a[232:235], v[56:59], 0\n"
      "ds_read_b64_tr_b16 a[208:209], v40 offset:8192\n"
      "v_cvt_pk_bf16_f32 v118, v140, v141\n"
      "v_mfma_f32_16x16x32_bf16 v[152:155], a[236:239], v[80:83], v[152:155]\n"
      "ds_read_b64_tr_b16 a[210:211], v40 offset:10240\n"
      "v_cvt_pk_bf16_f32 v119, v142, v143\n"
      "v_mfma_f32_16x16x32_bf16 v[152:155], a[240:243], v[104:107], v[152:155]\n"
      "ds_read_b64_tr_b16 a[212:213], v20 offset:24576\n"
      "v_mfma_f32_16x16x32_bf16 v[156:159], a[244:247], v[48:51], 0\n"
      "ds_read_b64_tr_b16 a[214:215], v20 offset:26624\n"
      "v_mfma_f32_16x16x32_bf16 v[156:159], a[248:251], v[72:75], v[156:159]\n"
      "ds_read_b64_tr_b16 a[216:217], v20 offset:32768\n"
      "v_mfma_f32_16x16x32_bf16 v[156:159], a[252:255], v[96:99], v[156:159]\n"
      "ds_read_b64_tr_b16 a[218:219], v20 offset:34816\n"
      "v_cvt_pk_bf16_f32 v168, v144, v145\n"
      "v_mfma_f32_16x16x32_bf16 v[160:163], a[244:247], v[52:55], 0\n"
      "ds_read_b64_tr_b16 a[220:221], v41 offset:8192\n"
      "v_cvt_pk_bf16_f32 v169, v146, v147\n"
      "v_mfma_f32_16x16x32_bf16 v[160:163], a[248:251], v[76:79], v[160:163]\n"
      "ds_read_b64_tr_b16 a[222:223], v41 offset:10240\n"
      "v_cvt_pk_bf16_f32 v172, v148, v149\n"
      "v_mfma_f32_16x16x32_bf16 v[160:163], a[252:255], v[100:103], v[160:163]\n"
      "ds_read_b64_tr_b16 a[224:225], v21 offset:24576\n"
      "v_cvt_pk_bf16_f32 v173, v150, v151\n"
      "v_mfma_f32_16x16x32_bf16 v[164:167], a[244:247], v[56:59], 0\n"
      "ds_read_b64_tr_b16 a[226:227], v21 offset:26624\n"
      "v_cvt_pk_bf16_f32 v176, v152, v153\n"
      "v_mfma_f32_16x16x32_bf16 v[164:167], a[248:251], v[80:83], v[164:167]\n"
      "ds_read_b64_tr_b16 a[228:229], v21 offset:32768\n"
      "v_cvt_pk_bf16_f32 v177, v154, v155\n"
      "v_mfma_f32_16x16x32_bf16 v[164:167], a[252:255], v[104:107], v[164:167]\n"
      "ds_read_b64_tr_b16 a[230:231], v21 offset:34816\n"
      "s_waitcnt lgkmcnt(10)\n"
      "v_mfma_f32_16x16x32_bf16 v[120:123], a[208:211], v[48:51], 0\n"
      "ds_read_b64_tr_b16 a[232:233], v42 offset:8192\n"
      "s_waitcnt lgkmcnt(9)\n"
      "v_mfma_f32_16x16x32_bf16 v[120:123], a[212:215], v[72:75], v[120:123]\n"
      "ds_read_b64_tr_b16 a[234:235], v42 offset:10240\n"
      "v_cvt_pk_bf16_f32 v170, v156, v157\n"
      "s_waitcnt lgkmcnt(8)\n"
      "v_mfma_f32_16x16x32_bf16 v[120:123], a[216:219], v[96:99], v[120:123]\n"
      "ds_read_b64_tr_b16 a[236:237], v22 offset:24576\n"
      "v_cvt_pk_bf16_f32 v171, v158, v159\n"
      "v_mfma_f32_16x16x32_bf16 v[124:127], a[208:211], v[52:55], 0\n"
      "ds_read_b64_tr_b16 a[238:239], v22 offset:26624\n"
      "v_cvt_pk_bf16_f32 v174, v160, v161\n"
      "v_mfma_f32_16x16x32_bf16 v[124:127], a[212:215], v[76:79], v[124:127]\n"
      "ds_read_b64_tr_b16 a[240:241], v22 offset:32768\n"
      "v_cvt_pk_bf16_f32 v175, v162, v163\n"
      "v_mfma_f32_16x16x32_bf16 v[124:127], a[216:219], v[100:103], v[124:127]\n"
      "ds_read_b64_tr_b16 a[242:243], v22 offset:34816\n"
      "v_cvt_pk_bf16_f32 v178, v164, v165\n"
      "v_mfma_f32_16x16x32_bf16 v[128:131], a[208:211], v[56:59], 0\n"
      "ds_read_b64_tr_b16 a[244:245], v43 offset:8192\n"
      "v_cvt_pk_bf16_f32 v179, v166, v167\n"
      "v_mfma_f32_16x16x32_bf16 v[128:131], a[212:215], v[80:83], v[128:131]\n"
      "ds_read_b64_tr_b16 a[246:247], v43 offset:10240\n"
      "v_mfma_f32_16x16x32_bf16 v[128:131], a[216:219], v[104:107], v[128:131]\n"
      "ds_read_b64_tr_b16 a[248:249], v23 offset:24576\n"
      "s_waitcnt lgkmcnt(13)\n"
      "v_mfma_f32_16x16x32_bf16 v[132:135], a[220:223], v[48:51], 0\n"
      "ds_read_b64_tr_b16 a[250:251], v23 offset:26624\n"
      "ds_write_b128 v44, v[108:111] offset:49152\n"
      "s_waitcnt lgkmcnt(13)\n"
      "v_mfma_f32_16x16x32_bf16 v[132:135], a[224:227], v[72:75], v[132:135]\n"
      "ds_read_b64_tr_b16 a[252:253], v23 offset:32768\n"
      "s_waitcnt lgkmcnt(12)\n"
      "v_mfma_f32_16x16x32_bf16 v[132:135], a[228:231], v[96:99], v[132:135]\n"
      "ds_read_b64_tr_b16 a[254:255], v23 offset:34816\n"
      "ds_write_b128 v44, v[112:115] offset:57344\n"
      "v_mfma_f32_16x16x32_bf16 v[136:139], a[220:223], v[52:55], 0\n"
      "ds_read_b128 a[0:3], v31\n"
      "v_cvt_pk_bf16_f32 v180, v120, v121\n"
      "v_mfma_f32_16x16x32_bf16 v[136:139], a[224:227], v[76:79], v[136:139]\n"
      "ds_read_b128 a[12:15], v31 offset:1024\n"
      "ds_write_b128 v44, v[116:119]\n"
      "v_cvt_pk_bf16_f32 v181, v122, v123\n"
      "v_mfma_f32_16x16x32_bf16 v[136:139], a[228:231], v[100:103], v[136:139]\n"
      "ds_read_b128 a[4:7], v31 offset:8192\n"
      "v_cvt_pk_bf16_f32 v184, v124, v125\n"
      "v_mfma_f32_16x16x32_bf16 v[140:143], a[220:223], v[56:59], 0\n"
      "ds_read_b128 a[16:19], v31 offset:9216\n"
      "ds_write_b128 v44, v[168:171] offset:50176\n"
      "v_cvt_pk_bf16_f32 v185, v126, v127\n"
      "v_mfma_f32_16x16x32_bf16 v[140:143], a[224:227], v[80:83], v[140:143]\n"
      "ds_read_b128 a[8:11], v31 offset:16384\n"
      "v_cvt_pk_bf16_f32 v188, v128, v129\n"
      "v_mfma_f32_16x16x32_bf16 v[140:143], a[228:231], v[104:107], v[140:143]\n"
      "ds_read_b128 a[20:23], v31 offset:17408\n"
      "ds_write_b128 v44, v[172:175] offset:58368\n"
      "v_cvt_pk_bf16_f32 v189, v130, v131\n"
      "s_add_u32 s88, s77, s82\n"
      "s_add_u32 m0, s85, 0x14000\n"
      "s_waitcnt lgkmcnt(15)\n"
      "v_mfma_f32_16x16x32_bf16 v[144:147], a[232:235], v[48:51], 0\n"
      "buffer_load_dwordx4 v10, s[24:27], s88 offen lds\n"
      "v_mfma_f32_16x16x32_bf16 v[144:147], a[236:239], v[72:75], v[144:147]\n"
      "ds_write_b128 v44, v[176:179] offset:1024\n"
      "v_mfma_f32_16x16x32_bf16 v[144:147], a[240:243], v[96:99], v[144:147]\n"
      "v_cvt_pk_bf16_f32 v182, v132, v133\n"
      "v_mfma_f32_16x16x32_bf16 v[148:151], a[232:235], v[52:55], 0\n"
      "v_cvt_pk_bf16_f32 v183, v134, v135\n"
      "buffer_load_dwordx4 v11, s[24:27], s88 offen offset:1024 lds\n"
      "v_mfma_f32_16x16x32_bf16 v[148:151], a[236:239], v[76:79], v[148:151]\n"
      "v_cvt_pk_bf16_f32 v186, v136, v137\n"
      "v_mfma_f32_16x16x32_bf16 v[148:151], a[240:243], v[100:103], v[148:151]\n"
      "s_waitcnt vmcnt(6) lgkmcnt(1)\n"
      "s_barrier\n"
      "v_cvt_pk_bf16_f32 v187, v138, v139\n"
      "buffer_load_dwordx4 v12, s[24:27], s88 offen offset:2048 lds\n"
      "v_mfma_f32_16x16x32_bf16 v[152:155], a[232:235], v[56:59], 0\n"
      "ds_read_b64_tr_b16 a[208:209], v20 offset:40960\n"
      "v_cvt_pk_bf16_f32 v190, v140, v141\n"
      "v_mfma_f32_16x16x32_bf16 v[152:155], a[236:239], v[80:83], v[152:155]\n"
      "ds_read_b64_tr_b16 a[210:211], v20 offset:43008\n"
      "v_cvt_pk_bf16_f32 v191, v142, v143\n"
      "v_mfma_f32_16x16x32_bf16 v[152:155], a[240:243], v[104:107], v[152:155]\n"
      "ds_read_b64_tr_b16 a[212:213], v20 offset:49152\n"
      "v_mfma_f32_16x16x32_bf16 v[156:159], a[244:247], v[48:51], 0\n"
      "ds_read_b64_tr_b16 a[214:215], v20 offset:51200\n"
      "v_mfma_f32_16x16x32_bf16 v[156:159], a[248:251], v[72:75], v[156:159]\n"
      "ds_read_b64_tr_b16 a[216:217], v20 offset:57344\n"
      "v_mfma_f32_16x16x32_bf16 v[156:159], a[252:255], v[96:99], v[156:159]\n"
      "ds_read_b64_tr_b16 a[218:219], v20 offset:59392\n"
      "v_cvt_pk_bf16_f32 v240, v144, v145\n"
      "v_mfma_f32_16x16x32_bf16 v[160:163], a[244:247], v[52:55], 0\n"
      "ds_read_b64_tr_b16 a[220:221], v21 offset:40960\n"
      "v_cvt_pk_bf16_f32 v241, v146, v147\n"
      "buffer_load_dwordx4 v13, s[24:27], s88 offen offset:3072 lds\n"
      "v_mfma_f32_16x16x32_bf16 v[160:163], a[248:251], v[76:79], v[160:163]\n"
      "ds_read_b64_tr_b16 a[222:223], v21 offset:43008\n"
      "v_cvt_pk_bf16_f32 v244, v148, v149\n"
      "v_mfma_f32_16x16x32_bf16 v[160:163], a[252:255], v[100:103], v[160:163]\n"
      "ds_read_b64_tr_b16 a[224:225], v21 offset:49152\n"
      "v_cvt_pk_bf16_f32 v245, v150, v151\n"
      "v_mfma_f32_16x16x32_bf16 v[164:167], a[244:247], v[56:59], 0\n"
      "ds_read_b64_tr_b16 a[226:227], v21 offset:51200\n"
      "v_cvt_pk_bf16_f32 v248, v152, v153\n"
      "v_mfma_f32_16x16x32_bf16 v[164:167], a[248:251], v[80:83], v[164:167]\n"
      "ds_read_b64_tr_b16 a[228:229], v21 offset:57344\n"
      "v_cvt_pk_bf16_f32 v249, v154, v155\n"
      "v_mfma_f32_16x16x32_bf16 v[164:167], a[252:255], v[104:107], v[164:167]\n"
      "ds_read_b64_tr_b16 a[230:231], v21 offset:59392\n"
      "s_waitcnt lgkmcnt(10)\n"
      "v_mfma_f32_16x16x32_bf16 v[120:123], a[208:211], v[48:51], 0\n"
      "ds_read_b64_tr_b16 a[232:233], v22 offset:40960\n"
      "s_waitcnt lgkmcnt(9)\n"
      "v_mfma_f32_16x16x32_bf16 v[120:123], a[212:215], v[72:75], v[120:123]\n"
      "ds_read_b64_tr_b16 a[234:235], v22 offset:43008\n"
      "v_cvt_pk_bf16_f32 v242, v156, v157\n"
      "s_waitcnt lgkmcnt(8)\n"
      "v_mfma_f32_16x16x32_bf16 v[120:123], a[216:219], v[96:99], v[120:123]\n"
      "ds_read_b64_tr_b16 a[236:237], v22 offset:49152\n"
      "v_cvt_pk_bf16_f32 v243, v158, v159\n"
      "v_mfma_f32_16x16x32_bf16 v[124:127], a[208:211], v[52:55], 0\n"
      "ds_read_b64_tr_b16 a[238:239], v22 offset:51200\n"
      "v_cvt_pk_bf16_f32 v246, v160, v161\n"
      "v_mfma_f32_16x16x32_bf16 v[124:127], a[212:215], v[76:79], v[124:127]\n"
      "ds_read_b64_tr_b16 a[240:241], v22 offset:57344\n"
      "v_cvt_pk_bf16_f32 v247, v162, v163\n"
      "v_mfma_f32_16x16x32_bf16 v[124:127], a[216:219], v[100:103], v[124:127]\n"
      "ds_read_b64_tr_b16 a[242:243], v22 offset:59392\n"
      "v_cvt_pk_bf16_f32 v250, v164, v165\n"
      "v_mfma_f32_16x16x32_bf16 v[128:131], a[208:211], v[56:59], 0\n"
      "ds_read_b64_tr_b16 a[244:245], v23 offset:40960\n"
      "v_cvt_pk_bf16_f32 v251, v166, v167\n"
      "v_mfma_f32_16x16x32_bf16 v[128:131], a[212:215], v[80:83], v[128:131]\n"
      "ds_read_b64_tr_b16 a[246:247], v23 offset:43008\n"
      "v_mfma_f32_16x16x32_bf16 v[128:131], a[216:219], v[104:107], v[128:131]\n"
      "ds_read_b64_tr_b16 a[248:249], v23 offset:49152\n"
      "s_waitcnt lgkmcnt(13)\n"
      "v_mfma_f32_16x16x32_bf16 v[132:135], a[220:223], v[48:51], 0\n"
      "ds_read_b64_tr_b16 a[250:251], v23 offset:51200\n"
      "ds_write_b128 v44, v[180:183] offset:8192\n"
      "s_waitcnt lgkmcnt(13)\n"
      "v_mfma_f32_16x16x32_bf16 v[132:135], a[224:227], v[72:75], v[132:135]\n"
      "ds_read_b64_tr_b16 a[252:253], v23 offset:57344\n"
      "s_waitcnt lgkmcnt(12)\n"
      "v_mfma_f32_16x16x32_bf16 v[132:135], a[228:231], v[96:99], v[132:135]\n"
      "ds_read_b64_tr_b16 a[254:255], v23 offset:59392\n"
      "ds_write_b128 v30, v[184:187] offset:24576\n"
      "v_mfma_f32_16x16x32_bf16 v[136:139], a[220:223], v[52:55], 0\n"
      "ds_read_b128 a[24:27], v45 offset:49152\n"
      "v_cvt_pk_bf16_f32 v252, v120, v121\n"
      "v_mfma_f32_16x16x32_bf16 v[136:139], a[224:227], v[76:79], v[136:139]\n"
      "ds_read_b128 a[36:39], v45 offset:50176\n"
      "ds_write_b128 v30, v[188:191] offset:32768\n"
      "v_cvt_pk_bf16_f32 v253, v122, v123\n"
      "v_mfma_f32_16x16x32_bf16 v[136:139], a[228:231], v[100:103], v[136:139]\n"
      "ds_read_b128 a[28:31], v45 offset:57344\n"
      "v_cvt_pk_bf16_f32 v216, v124, v125\n"
      "v_mfma_f32_16x16x32_bf16 v[140:143], a[220:223], v[56:59], 0\n"
      "ds_read_b128 a[40:43], v45 offset:58368\n"
      "ds_write_b128 v44, v[240:243] offset:9216\n"
      "v_cvt_pk_bf16_f32 v217, v126, v127\n"
      "v_mfma_f32_16x16x32_bf16 v[140:143], a[224:227], v[80:83], v[140:143]\n"
      "s_barrier\n"
      "ds_read_b128 a[32:35], v45\n"
      "v_cvt_pk_bf16_f32 v220, v128, v129\n"
      "v_mfma_f32_16x16x32_bf16 v[140:143], a[228:231], v[104:107], v[140:143]\n"
      "ds_read_b64_tr_b16 a[192:193], v14 offset:49152\n"
      "ds_write_b128 v30, v[244:247] offset:25600\n"
      "v_cvt_pk_bf16_f32 v221, v130, v131\n"
      "s_waitcnt lgkmcnt(15)\n"
      "v_mfma_f32_16x16x32_bf16 v[144:147], a[232:235], v[48:51], 0\n"
      "ds_read_b64_tr_b16 a[194:195], v14 offset:51200\n"
      "v_mfma_f32_16x16x32_bf16 v[144:147], a[236:239], v[72:75], v[144:147]\n"
      "ds_read_b64_tr_b16 a[196:197], v14 offset:53248\n"
      "ds_write_b128 v30, v[248:251] offset:33792\n"
      "v_mfma_f32_16x16x32_bf16 v[144:147], a[240:243], v[96:99], v[144:147]\n"
      "ds_read_b64_tr_b16 a[198:199], v14 offset:55296\n"
      "v_cvt_pk_bf16_f32 v254, v132, v133\n"
      "v_mfma_f32_16x16x32_bf16 v[148:151], a[232:235], v[52:55], 0\n"
      "ds_read_b64_tr_b16 a[200:201], v26 offset:49152\n"
      "v_cvt_pk_bf16_f32 v255, v134, v135\n"
      "v_mfma_f32_16x16x32_bf16 v[148:151], a[236:239], v[76:79], v[148:151]\n"
      "ds_read_b64_tr_b16 a[202:203], v26 offset:51200\n"
      "v_cvt_pk_bf16_f32 v218, v136, v137\n"
      "v_mfma_f32_16x16x32_bf16 v[148:151], a[240:243], v[100:103], v[148:151]\n"
      "ds_read_b64_tr_b16 a[204:205], v26 offset:53248\n"
      "v_cvt_pk_bf16_f32 v219, v138, v139\n"
      "v_mfma_f32_16x16x32_bf16 v[152:155], a[232:235], v[56:59], 0\n"
      "ds_read_b64_tr_b16 a[206:207], v26 offset:55296\n"
      "v_cvt_pk_bf16_f32 v222, v140, v141\n"
      "v_mfma_f32_16x16x32_bf16 v[152:155], a[236:239], v[80:83], v[152:155]\n"
      "ds_read_b64_tr_b16 a[208:209], v15 offset:49152\n"
      "v_cvt_pk_bf16_f32 v223, v142, v143\n"
      "v_mfma_f32_16x16x32_bf16 v[152:155], a[240:243], v[104:107], v[152:155]\n"
      "ds_read_b64_tr_b16 a[210:211], v15 offset:51200\n"
      "v_mfma_f32_16x16x32_bf16 v[156:159], a[244:247], v[48:51], 0\n"
      "ds_read_b64_tr_b16 a[212:213], v15 offset:53248\n"
      "s_waitcnt lgkmcnt(15)\n"
      "v_mfma_f32_16x16x32_bf16 v[156:159], a[248:251], v[72:75], v[156:159]\n"
      "ds_read_b64_tr_b16 a[214:215], v15 offset:55296\n"
      "v_mfma_f32_16x16x32_bf16 v[156:159], a[252:255], v[96:99], v[156:159]\n"
      "ds_read_b64_tr_b16 a[216:217], v27 offset:49152\n"
      "v_cvt_pk_bf16_f32 v224, v144, v145\n"
      "v_mfma_f32_16x16x32_bf16 v[160:163], a[244:247], v[52:55], 0\n"
      "ds_read_b64_tr_b16 a[218:219], v27 offset:51200\n"
      "v_cvt_pk_bf16_f32 v225, v146, v147\n"
      "v_mfma_f32_16x16x32_bf16 v[160:163], a[248:251], v[76:79], v[160:163]\n"
      "ds_read_b64_tr_b16 a[220:221], v27 offset:53248\n"
      "v_cvt_pk_bf16_f32 v228, v148, v149\n"
      "v_mfma_f32_16x16x32_bf16 v[160:163], a[252:255], v[100:103], v[160:163]\n"
      "ds_read_b64_tr_b16 a[222:223], v27 offset:55296\n"
      "v_cvt_pk_bf16_f32 v229, v150, v151\n"
      "v_mfma_f32_16x16x32_bf16 v[164:167], a[244:247], v[56:59], 0\n"
      "ds_read_b128 a[44:47], v45 offset:1024\n"
      "v_cvt_pk_bf16_f32 v232, v152, v153\n"
      "v_mfma_f32_16x16x32_bf16 v[164:167], a[248:251], v[80:83], v[164:167]\n"
      "v_cvt_pk_bf16_f32 v233, v154, v155\n"
      "v_mfma_f32_16x16x32_bf16 v[164:167], a[252:255], v[104:107], v[164:167]\n"
      "s_waitcnt lgkmcnt(15)\n"
      "v_mfma_f32_16x16x32_bf16 v[192:195], a[192:195], v[60:63], 0\n"
      "s_waitcnt lgkmcnt(13)\n"
      "v_mfma_f32_16x16x32_bf16 v[192:195], a[196:199], v[84:87], v[192:195]\n"
      "v_cvt_pk_bf16_f32 v226, v156, v157\n"
      "s_waitcnt lgkmcnt(11)\n"
      "v_mfma_f32_16x16x32_bf16 v[192:195], a[200:203], a[0:3], v[192:195]\n"
      "v_cvt_pk_bf16_f32 v227, v158, v159\n"
      "s_waitcnt lgkmcnt(9)\n"
      "v_mfma_f32_16x16x32_bf16 v[192:195], a[204:207], a[12:15], v[192:195]\n"
      "v_cvt_pk_bf16_f32 v230, v160, v161\n"
      "v_mfma_f32_16x16x32_bf16 v[196:199], a[204:207], a[16:19], 0\n"
      "v_cvt_pk_bf16_f32 v231, v162, v163\n"
      "v_mfma_f32_16x16x32_bf16 v[196:199], a[200:203], a[4:7], v[196:199]\n"
      "v_cvt_pk_bf16_f32 v234, v164, v165\n"
      "v_mfma_f32_16x16x32_bf16 v[196:199], a[196:199], v[88:91], v[196:199]\n"
      "v_cvt_pk_bf16_f32 v235, v166, v167\n"
      "v_mfma_f32_16x16x32_bf16 v[196:199], a[192:195], v[64:67], v[196:199]\n"
      "v_mfma_f32_16x16x32_bf16 v[200:203], a[192:195], v[68:71], 0\n"
      "v_mfma_f32_16x16x32_bf16 v[200:203], a[196:199], v[92:95], v[200:203]\n"
      "ds_write_b128 v30, v[252:255]\n"
      "v_mfma_f32_16x16x32_bf16 v[200:203], a[200:203], a[8:11], v[200:203]\n"
      "v_mfma_f32_16x16x32_bf16 v[200:203], a[204:207], a[20:23], v[200:203]\n"
      "ds_write_b128 v30, v[216:219] offset:8192\n"
      "ds_write_b128 v30, v[220:223] offset:16384\n"
      "ds_write_b128 v30, v[224:227] offset:1024\n"
      "ds_write_b128 v30, v[228:231] offset:9216\n"
      "ds_write_b128 v30, v[232:235] offset:17408\n"
      "s_cmp_eq_u32 s50, 7\n"
      "s_cbranch_scc1 7f\n"
      "18:\n"
      "s_barrier\n"
      "s_waitcnt lgkmcnt(13)\n"
      "v_mfma_f32_16x16x32_bf16 v[204:207], a[208:211], v[60:63], 0\n"
      "ds_read_b64_tr_b16 a[192:193], v14\n"
      "s_waitcnt lgkmcnt(12)\n"
      "v_mfma_f32_16x16x32_bf16 v[204:207], a[212:215], v[84:87], v[204:207]\n"
      "ds_read_b64_tr_b16 a[194:195], v14 offset:2048\n"
      "s_waitcnt lgkmcnt(11)\n"
      "v_mfma_f32_16x16x32_bf16 v[204:207], a[216:219], a[0:3], v[204:207]\n"
      "ds_read_b64_tr_b16 a[196:197], v14 offset:4096\n"
      "s_waitcnt lgkmcnt(10)\n"
      "v_mfma_f32_16x16x32_bf16 v[204:207], a[220:223], a[12:15], v[204:207]\n"
      "ds_read_b64_tr_b16 a[198:199], v14 offset:6144\n"
      "v_mfma_f32_16x16x32_bf16 v[208:211], a[220:223], a[16:19], 0\n"
      "ds_read_b64_tr_b16 a[200:201], v26\n"
      "v_mfma_f32_16x16x32_bf16 v[208:211], a[216:219], a[4:7], v[208:211]\n"
      "ds_read_b64_tr_b16 a[202:203], v26 offset:2048\n"
      "v_mfma_f32_16x16x32_bf16 v[208:211], a[212:215], v[88:91], v[208:211]\n"
      "ds_read_b64_tr_b16 a[204:205], v26 offset:4096\n"
      "v_mfma_f32_16x16x32_bf16 v[208:211], a[208:211], v[64:67], v[208:211]\n"
      "ds_read_b64_tr_b16 a[206:207], v26 offset:6144\n"
      "s_add_u32 s82, s82, 0x80\n"
      "s_cmp_eq_u32 s82, 0x380\n"
      "s_cselect_b32 s82, 0, s82\n"
      "s_add_u32 s88, s74, s82\n"
      "s_add_u32 m0, s85, 0xc000\n"
      "v_mfma_f32_16x16x32_bf16 v[212:215], a[208:211], v[68:71], 0\n"
      "ds_read_b64_tr_b16 a[208:209], v15\n"
      "buffer_load_dwordx4 v10, s[24:27], s88 offen lds\n"
      "v_mfma_f32_16x16x32_bf16 v[212:215], a[212:215], v[92:95], v[212:215]\n"
      "ds_read_b64_tr_b16 a[210:211], v15 offset:2048\n"
      "v_mfma_f32_16x16x32_bf16 v[212:215], a[216:219], a[8:11], v[212:215]\n"
      "ds_read_b64_tr_b16 a[212:213], v15 offset:4096\n"
      "buffer_load_dwordx4 v11, s[24:27], s88 offen offset:1024 lds\n"
      "v_mfma_f32_16x16x32_bf16 v[212:215], a[220:223], a[20:23], v[212:215]\n"
      "ds_read_b64_tr_b16 a[214:215], v15 offset:6144\n"
      "s_waitcnt lgkmcnt(10)\n"
      "v_mfma_f32_16x16x32_bf16 v[192:195], a[192:195], v[108:111], v[192:195]\n"
      "ds_read_b64_tr_b16 a[216:217], v27\n"
      "buffer_load_dwordx4 v12, s[24:27], s88 offen offset:2048 lds\n"
      "s_waitcnt lgkmcnt(9)\n"
      "v_mfma_f32_16x16x32_bf16 v[192:195], a[196:199], v[168:171], v[192:195]\n"
      "ds_read_b64_tr_b16 a[218:219], v27 offset:2048\n"
      "s_waitcnt lgkmcnt(8)\n"
      "v_mfma_f32_16x16x32_bf16 v[192:195], a[200:203], a[24:27], v[192:195]\n"
      "ds_read_b64_tr_b16 a[220:221], v27 offset:4096\n"
      "buffer_load_dwordx4 v13, s[24:27], s88 offen offset:3072 lds\n"
      "s_waitcnt lgkmcnt(7)\n"
      "v_mfma_f32_16x16x32_bf16 v[192:195], a[204:207], a[36:39], v[192:195]\n"
      "ds_read_b64_tr_b16 a[222:223], v27 offset:6144\n"
      "s_add_u32 s88, s75, s82\n"
      "s_add_u32 m0, s85, 0x24000\n"
      "v_mfma_f32_16x16x32_bf16 v[196:199], a[204:207], a[40:43], v[196:199]\n"
      "ds_read_b128 a[48:51], v45 offset:8192\n"
      "buffer_load_dwordx4 v10, s[24:27], s88 offen lds\n"
      "v_mfma_f32_16x16x32_bf16 v[196:199], a[200:203], a[28:31], v[196:199]\n"
      "ds_read_b128 a[60:63], v45 offset:9216\n"
      "v_mfma_f32_16x16x32_bf16 v[196:199], a[196:199], v[172:175], v[196:199]\n"
      "ds_read_b128 a[52:55], v31 offset:24576\n"
      "buffer_load_dwordx4 v11, s[24:27], s88 offen offset:1024 lds\n"
      "v_mfma_f32_16x16x32_bf16 v[196:199], a[192:195], v[112:115], v[196:199]\n"
      "ds_read_b128 a[64:67], v31 offset:25600\n"
      "v_mfma_f32_16x16x32_bf16 v[200:203], a[192:195], v[116:119], v[200:203]\n"
      "ds_read_b128 a[56:59], v31 offset:32768\n"
      "buffer_load_dwordx4 v12, s[24:27], s88 offen offset:2048 lds\n"
      "v_mfma_f32_16x16x32_bf16 v[200:203], a[196:199], v[176:179], v[200:203]\n"
      "ds_read_b128 a[68:71], v31 offset:33792\n"
      "v_mfma_f32_16x16x32_bf16 v[200:203], a[200:203], a[32:35], v[200:203]\n"
      "buffer_load_dwordx4 v13, s[24:27], s88 offen offset:3072 lds\n"
      "v_mfma_f32_16x16x32_bf16 v[200:203], a[204:207], a[44:47], v[200:203]\n"
      "s_waitcnt vmcnt(12) lgkmcnt(0)\n"
      "s_barrier\n"
      "v_mfma_f32_16x16x32_bf16 v[204:207], a[208:211], v[108:111], v[204:207]\n"
      "ds_read_b64_tr_b16 a[192:193], v16\n"
      "v_mfma_f32_16x16x32_bf16 v[204:207], a[212:215], v[168:171], v[204:207]\n"
      "ds_read_b64_tr_b16 a[194:195], v16 offset:2048\n"
      "v_mfma_f32_16x16x32_bf16 v[204:207], a[216:219], a[24:27], v[204:207]\n"
      "ds_read_b64_tr_b16 a[196:197], v16 offset:4096\n"
      "v_mfma_f32_16x16x32_bf16 v[204:207], a[220:223], a[36:39], v[204:207]\n"
      "ds_read_b64_tr_b16 a[198:199], v16 offset:6144\n"
      "v_mfma_f32_16x16x32_bf16 v[208:211], a[220:223], a[40:43], v[208:211]\n"
      "ds_read_b64_tr_b16 a[200:201], v28\n"
      "v_mfma_f32_16x16x32_bf16 v[208:211], a[216:219], a[28:31], v[208:211]\n"
      "ds_read_b64_tr_b16 a[202:203], v28 offset:2048\n"
      "v_mfma_f32_16x16x32_bf16 v[208:211], a[212:215], v[172:175], v[208:211]\n"
      "ds_read_b64_tr_b16 a[204:205], v28 offset:4096\n"
      "v_mfma_f32_16x16x32_bf16 v[208:211], a[208:211], v[112:115], v[208:211]\n"
      "ds_read_b64_tr_b16 a[206:207], v28 offset:6144\n"
      "s_add_u32 s88, s76, s82\n"
      "s_add_u32 m0, s85, 0x18000\n"
      "v_mfma_f32_16x16x32_bf16 v[212:215], a[208:211], v[116:119], v[212:215]\n"
      "ds_read_b64_tr_b16 a[208:209], v17\n"
      "buffer_load_dwordx4 v10, s[24:27], s88 offen lds\n"
      "v_mfma_f32_16x16x32_bf16 v[212:215], a[212:215], v[176:179], v[212:215]\n"
      "ds_read_b64_tr_b16 a[210:211], v17 offset:2048\n"
      "v_mfma_f32_16x16x32_bf16 v[212:215], a[216:219], a[32:35], v[212:215]\n"
      "ds_read_b64_tr_b16 a[212:213], v17 offset:4096\n"
      "buffer_load_dwordx4 v11, s[24:27], s88 offen offset:1024 lds\n"
      "v_mfma_f32_16x16x32_bf16 v[212:215], a[220:223], a[44:47], v[212:215]\n"
      "ds_read_b64_tr_b16 a[214:215], v17 offset:6144\n"
      "s_waitcnt lgkmcnt(10)\n"
      "v_mfma_f32_16x16x32_bf16 v[192:195], a[192:195], v[180:183], v[192:195]\n"
      "ds_read_b64_tr_b16 a[216:217], v29\n"
      "buffer_load_dwordx4 v12, s[24:27], s88 offen offset:2048 lds\n"
      "s_waitcnt lgkmcnt(9)\n"
      "v_mfma_f32_16x16x32_bf16 v[192:195], a[196:199], v[240:243], v[192:195]\n"
      "ds_read_b64_tr_b16 a[218:219], v29 offset:2048\n"
      "s_waitcnt lgkmcnt(8)\n"
      "v_mfma_f32_16x16x32_bf16 v[192:195], a[200:203], a[48:51], v[192:195]\n"
      "ds_read_b64_tr_b16 a[220:221], v29 offset:4096\n"
      "buffer_load_dwordx4 v13, s[24:27], s88 offen offset:3072 lds\n"
      "s_waitcnt lgkmcnt(7)\n"
      "v_mfma_f32_16x16x32_bf16 v[192:195], a[204:207], a[60:63], v[192:195]\n"
      "ds_read_b64_tr_b16 a[222:223], v29 offset:6144\n"
      "s_add_u32 s88, s77, s82\n"
      "s_add_u32 m0, s85, 0x8000\n"
      "v_mfma_f32_16x16x32_bf16 v[196:199], a[204:207], a[64:67], v[196:199]\n"
      "ds_read_b128 a[72:75], v31\n"
      "buffer_load_dwordx4 v10, s[24:27], s88 offen lds\n"
      "v_mfma_f32_16x16x32_bf16 v[196:199], a[200:203], a[52:55], v[196:199]\n"
      "ds_read_b128 a[84:87], v31 offset:1024\n"
      "v_mfma_f32_16x16x32_bf16 v[196:199], a[196:199], v[244:247], v[196:199]\n"
      "ds_read_b128 a[76:79], v31 offset:8192\n"
      "buffer_load_dwordx4 v11, s[24:27], s88 offen offset:1024 lds\n"
      "v_mfma_f32_16x16x32_bf16 v[196:199], a[192:195], v[184:187], v[196:199]\n"
      "ds_read_b128 a[88:91], v31 offset:9216\n"
      "v_mfma_f32_16x16x32_bf16 v[200:203], a[192:195], v[188:191], v[200:203]\n"
      "ds_read_b128 a[80:83], v31 offset:16384\n"
      "buffer_load_dwordx4 v12, s[24:27], s88 offen offset:2048 lds\n"
      "v_mfma_f32_16x16x32_bf16 v[200:203], a[196:199], v[248:251], v[200:203]\n"
      "ds_read_b128 a[92:95], v31 offset:17408\n"
      "v_mfma_f32_16x16x32_bf16 v[200:203], a[200:203], a[56:59], v[200:203]\n"
      "buffer_load_dwordx4 v13, s[24:27], s88 offen offset:3072 lds\n"
      "v_mfma_f32_16x16x32_bf16 v[200:203], a[204:207], a[68:71], v[200:203]\n"
      "s_waitcnt vmcnt(16) lgkmcnt(0)\n"
      "s_barrier\n"
      "v_mfma_f32_16x16x32_bf16 v[204:207], a[208:211], v[180:183], v[204:207]\n"
      "ds_read_b64_tr_b16 a[192:193], v14 offset:16384\n"
      "v_mfma_f32_16x16x32_bf16 v[204:207], a[212:215], v[240:243], v[204:207]\n"
      "ds_read_b64_tr_b16 a[194:195], v14 offset:18432\n"
      "v_mfma_f32_16x16x32_bf16 v[204:207], a[216:219], a[48:51], v[204:207]\n"
      "ds_read_b64_tr_b16 a[196:197], v14 offset:20480\n"
      "v_mfma_f32_16x16x32_bf16 v[204:207], a[220:223], a[60:63], v[204:207]\n"
      "ds_read_b64_tr_b16 a[198:199], v14 offset:22528\n"
      "v_mfma_f32_16x16x32_bf16 v[208:211], a[220:223], a[64:67], v[208:211]\n"
      "ds_read_b64_tr_b16 a[200:201], v26 offset:16384\n"
      "v_add_co_u32 v34, vcc, 0x7f4a7c15, v34\n"
      "v_mfma_f32_16x16x32_bf16 v[208:211], a[216:219], a[52:55], v[208:211]\n"
      "ds_read_b64_tr_b16 a[202:203], v26 offset:18432\n"
      "v_mov_b32 v4, 0x9e3779b9\n"
      "v_mfma_f32_16x16x32_bf16 v[208:211], a[212:215], v[244:247], v[208:211]\n"
      "ds_read_b64_tr_b16 a[204:205], v26 offset:20480\n"
      "v_addc_co_u32 v35, vcc, v35, v4, vcc\n"
      "v_mfma_f32_16x16x32_bf16 v[208:211], a[208:211], v[184:187], v[208:211]\n"
      "ds_read_b64_tr_b16 a[206:207], v26 offset:22528\n"
      "v_mov_b32 v48, 0\n"
      "s_add_u32 s82, s82, 0x80\n"
      "s_cmp_eq_u32 s82, 0x380\n"
      "s_cselect_b32 s82, 0, s82\n"
      "s_add_u32 s88, s74, s82\n"
      "s_add_u32 m0, s85, 0\n"
      "v_mfma_f32_16x16x32_bf16 v[212:215], a[208:211], v[188:191], v[212:215]\n"
      "ds_read_b64_tr_b16 a[208:209], v15 offset:16384\n"
      "v_mov_b32 v49, 0\n"
      "buffer_load_dwordx4 v10, s[24:27], s88 offen lds\n"
      "v_mfma_f32_16x16x32_bf16 v[212:215], a[212:215], v[248:251], v[212:215]\n"
      "ds_read_b64_tr_b16 a[210:211], v15 offset:18432\n"
      "v_mov_b32 v52, 0\n"
      "v_mfma_f32_16x16x32_bf16 v[212:215], a[216:219], a[56:59], v[212:215]\n"
      "ds_read_b64_tr_b16 a[212:213], v15 offset:20480\n"
      "v_mov_b32 v53, 0\n"
      "buffer_load_dwordx4 v11, s[24:27], s88 offen offset:1024 lds\n"
      "v_mfma_f32_16x16x32_bf16 v[212:215], a[220:223], a[68:71], v[212:215]\n"
      "ds_read_b64_tr_b16 a[214:215], v15 offset:22528\n"
      "v_and_b32 v4, 1, v1\n"
      "s_waitcnt lgkmcnt(10)\n"
      "v_mfma_f32_16x16x32_bf16 v[192:195], a[192:195], v[252:255], v[192:195]\n"
      "ds_read_b64_tr_b16 a[216:217], v27 offset:16384\n"
      "v_lshlrev_b32 v4, 4, v4\n"
      "buffer_load_dwordx4 v12, s[24:27], s88 offen offset:2048 lds\n"
      "s_waitcnt lgkmcnt(9)\n"
      "v_mfma_f32_16x16x32_bf16 v[192:195], a[196:199], v[224:227], v[192:195]\n"
      "ds_read_b64_tr_b16 a[218:219], v27 offset:18432\n"
      "s_waitcnt lgkmcnt(8)\n"
      "v_mfma_f32_16x16x32_bf16 v[192:195], a[200:203], a[72:75], v[192:195]\n"
      "ds_read_b64_tr_b16 a[220:221], v27 offset:20480\n"
      "v_mov_b32 v5, 0x3f80\n"
      "buffer_load_dwordx4 v13, s[24:27], s88 offen offset:3072 lds\n"
      "s_waitcnt lgkmcnt(7)\n"
      "v_mfma_f32_16x16x32_bf16 v[192:195], a[204:207], a[84:87], v[192:195]\n"
      "ds_read_b64_tr_b16 a[222:223], v27 offset:22528\n"
      "v_lshlrev_b32 v5, v4, v5\n"
      "s_add_u32 s88, s75, s82\n"
      "s_add_u32 m0, s85, 0x4000\n"
      "v_mfma_f32_16x16x32_bf16 v[196:199], a[204:207], a[88:91], v[196:199]\n"
      "v_lshrrev_b32 v6, 1, v3\n"
      "buffer_load_dwordx4 v10, s[24:27], s88 offen lds\n"
      "v_mfma_f32_16x16x32_bf16 v[196:199], a[200:203], a[76:79], v[196:199]\n"
      "v_lshlrev_b32 v7, 1, v2\n"
      "v_mfma_f32_16x16x32_bf16 v[196:199], a[196:199], v[228:231], v[196:199]\n"
      "v_cmp_eq_u32 vcc, v6, v7\n"
      "buffer_load_dwordx4 v11, s[24:27], s88 offen offset:1024 lds\n"
      "v_mfma_f32_16x16x32_bf16 v[196:199], a[192:195], v[216:219], v[196:199]\n"
      "v_cndmask_b32 v50, 0, v5, vcc\n"
      "v_mfma_f32_16x16x32_bf16 v[200:203], a[192:195], v[220:223], v[200:203]\n"
      "v_add_u32 v7, 1, v7\n"
      "buffer_load_dwordx4 v12, s[24:27], s88 offen offset:2048 lds\n"
      "v_mfma_f32_16x16x32_bf16 v[200:203], a[196:199], v[232:235], v[200:203]\n"
      "v_cmp_eq_u32 vcc, v6, v7\n"
      "v_mfma_f32_16x16x32_bf16 v[200:203], a[200:203], a[80:83], v[200:203]\n"
      "s_nop 0\n"
      "v_cndmask_b32 v51, 0, v5, vcc\n"
      "buffer_load_dwordx4 v13, s[24:27], s88 offen offset:3072 lds\n"
      "v_mfma_f32_16x16x32_bf16 v[200:203], a[204:207], a[92:95], v[200:203]\n"
      "s_waitcnt vmcnt(20)\n"
      "s_barrier\n"
      "s_waitcnt lgkmcnt(6)\n"
      "v_mfma_f32_16x16x32_bf16 v[204:207], a[208:211], v[252:255], v[204:207]\n"
      "ds_read_b64_tr_b16 a[192:193], v18 offset:49152\n"
      "s_waitcnt lgkmcnt(5)\n"
      "v_mfma_f32_16x16x32_bf16 v[204:207], a[212:215], v[224:227], v[204:207]\n"
      "ds_read_b64_tr_b16 a[194:195], v18 offset:51200\n"
      "s_waitcnt lgkmcnt(4)\n"
      "v_mfma_f32_16x16x32_bf16 v[204:207], a[216:219], a[72:75], v[204:207]\n"
      "ds_read_b64_tr_b16 a[196:197], v18 offset:53248\n"
      "v_cvt_pk_bf16_f32 v144, v192, v193\n"
      "s_mul_i32 s90, s83, 0x300\n"
      "s_add_u32 s83, s83, 0x80\n"
      "s_cmp_eq_u32 s83, 0x380\n"
      "s_cselect_b32 s83, 0, s83\n"
      "s_waitcnt lgkmcnt(3)\n"
      "v_mfma_f32_16x16x32_bf16 v[204:207], a[220:223], a[84:87], v[204:207]\n"
      "ds_read_b64_tr_b16 a[198:199], v18 offset:55296\n"
      "v_cvt_pk_bf16_f32 v145, v194, v195\n"
      "v_mfma_f32_16x16x32_bf16 v[208:211], a[220:223], a[88:91], v[208:211]\n"
      "ds_read_b64_tr_b16 a[200:201], v24 offset:49152\n"
      "v_cvt_pk_bf16_f32 v148, v196, v197\n"
      "v_mfma_f32_16x16x32_bf16 v[208:211], a[216:219], a[76:79], v[208:211]\n"
      "ds_read_b64_tr_b16 a[202:203], v24 offset:51200\n"
      "v_mfma_f32_16x16x32_bf16 v[208:211], a[212:215], v[228:231], v[208:211]\n"
      "ds_read_b64_tr_b16 a[204:205], v24 offset:53248\n"
      "v_cvt_pk_bf16_f32 v149, v198, v199\n"
      "v_mfma_f32_16x16x32_bf16 v[208:211], a[208:211], v[216:219], v[208:211]\n"
      "ds_read_b64_tr_b16 a[206:207], v24 offset:55296\n"
      "v_cvt_pk_bf16_f32 v152, v200, v201\n"
      "v_mfma_f32_16x16x32_bf16 v[212:215], a[208:211], v[220:223], v[212:215]\n"
      "ds_read_b64_tr_b16 a[208:209], v19 offset:49152\n"
      "v_cvt_pk_bf16_f32 v153, v202, v203\n"
      "v_mfma_f32_16x16x32_bf16 v[212:215], a[212:215], v[232:235], v[212:215]\n"
      "ds_read_b64_tr_b16 a[210:211], v19 offset:51200\n"
      "s_add_u32 s88, s76, s82\n"
      "s_add_u32 m0, s85, 0x1c000\n"
      "v_mfma_f32_16x16x32_bf16 v[212:215], a[216:219], a[80:83], v[212:215]\n"
      "ds_read_b64_tr_b16 a[212:213], v19 offset:53248\n"
      "v_cvt_pk_bf16_f32 v146, v204, v205\n"
      "v_cvt_pk_bf16_f32 v147, v206, v207\n"
      "buffer_load_dwordx4 v10, s[24:27], s88 offen lds\n"
      "v_mfma_f32_16x16x32_bf16 v[212:215], a[220:223], a[92:95], v[212:215]\n"
      "ds_read_b64_tr_b16 a[214:215], v19 offset:55296\n"
      "s_waitcnt lgkmcnt(10)\n"
      "v_mfma_f32_16x16x32_bf16 v[192:195], a[192:195], v[60:63], 0\n"
      "ds_read_b64_tr_b16 a[216:217], v25 offset:49152\n"
      "v_cvt_pk_bf16_f32 v150, v208, v209\n"
      "v_cvt_pk_bf16_f32 v151, v210, v211\n"
      "buffer_load_dwordx4 v11, s[24:27], s88 offen offset:1024 lds\n"
      "s_waitcnt lgkmcnt(9)\n"
      "v_mfma_f32_16x16x32_bf16 v[192:195], a[196:199], v[84:87], v[192:195]\n"
      "ds_read_b64_tr_b16 a[218:219], v25 offset:51200\n"
      "s_waitcnt lgkmcnt(8)\n"
      "v_mfma_f32_16x16x32_bf16 v[192:195], a[200:203], a[0:3], v[192:195]\n"
      "ds_read_b64_tr_b16 a[220:221], v25 offset:53248\n"
      "v_cvt_pk_bf16_f32 v154, v212, v213\n"
      "v_cvt_pk_bf16_f32 v155, v214, v215\n"
      "buffer_load_dwordx4 v12, s[24:27], s88 offen offset:2048 lds\n"
      "s_waitcnt lgkmcnt(7)\n"
      "v_mfma_f32_16x16x32_bf16 v[192:195], a[204:207], a[12:15], v[192:195]\n"
      "ds_read_b64_tr_b16 a[222:223], v25 offset:55296\n"
      "v_mfma_f32_16x16x32_bf16 v[196:199], a[204:207], a[16:19], 0\n"
      "buffer_load_dwordx4 v13, s[24:27], s88 offen offset:3072 lds\n"
      "v_mfma_f32_16x16x32_bf16 v[196:199], a[200:203], a[4:7], v[196:199]\n"
      "v_mfma_f32_16x16x32_bf16 v[196:199], a[196:199], v[88:91], v[196:199]\n"
      "buffer_store_dwordx4 v[144:147], v32, s[36:39], s90 offen sc1\n"
      "v_mfma_f32_16x16x32_bf16 v[196:199], a[192:195], v[64:67], v[196:199]\n"
      "v_mfma_f32_16x16x32_bf16 v[200:203], a[192:195], v[68:71], 0\n"
      "buffer_store_dwordx4 v[148:151], v32, s[36:39], s90 offen offset:1024 sc1\n"
      "v_mfma_f32_16x16x32_bf16 v[200:203], a[196:199], v[92:95], v[200:203]\n"
      "v_mfma_f32_16x16x32_bf16 v[200:203], a[200:203], a[8:11], v[200:203]\n"
      "buffer_store_dwordx4 v[152:155], v32, s[36:39], s90 offen offset:2048 sc1\n"
      "v_mfma_f32_16x16x32_bf16 v[200:203], a[204:207], a[20:23], v[200:203]\n"
      "s_waitcnt vmcnt(11) lgkmcnt(0)\n"
      "s_barrier\n"
      "v_mfma_f32_16x16x32_bf16 v[204:207], a[208:211], v[60:63], 0\n"
      "ds_read_b64_tr_b16 a[192:193], v16 offset:16384\n"
      "v_mfma_f32_16x16x32_bf16 v[204:207], a[212:215], v[84:87], v[204:207]\n"
      "ds_read_b64_tr_b16 a[194:195], v16 offset:18432\n"
      "v_mfma_f32_16x16x32_bf16 v[204:207], a[216:219], a[0:3], v[204:207]\n"
      "ds_read_b64_tr_b16 a[196:197], v16 offset:20480\n"
      "v_mfma_f32_16x16x32_bf16 v[204:207], a[220:223], a[12:15], v[204:207]\n"
      "ds_read_b64_tr_b16 a[198:199], v16 offset:22528\n"
      "v_mfma_f32_16x16x32_bf16 v[208:211], a[220:223], a[16:19], 0\n"
      "ds_read_b64_tr_b16 a[200:201], v28 offset:16384\n"
      "v_mfma_f32_16x16x32_bf16 v[208:211], a[216:219], a[4:7], v[208:211]\n"
      "ds_read_b64_tr_b16 a[202:203], v28 offset:18432\n"
      "v_mfma_f32_16x16x32_bf16 v[208:211], a[212:215], v[88:91], v[208:211]\n"
      "ds_read_b64_tr_b16 a[204:205], v28 offset:20480\n"
      "v_mfma_f32_16x16x32_bf16 v[208:211], a[208:211], v[64:67], v[208:211]\n"
      "ds_read_b64_tr_b16 a[206:207], v28 offset:22528\n"
      "v_mfma_f32_16x16x32_bf16 v[212:215], a[208:211], v[68:71], 0\n"
      "ds_read_b64_tr_b16 a[208:209], v17 offset:16384\n"
      "v_mfma_f32_16x16x32_bf16 v[212:215], a[212:215], v[92:95], v[212:215]\n"
      "ds_read_b64_tr_b16 a[210:211], v17 offset:18432\n"
      "v_mfma_f32_16x16x32_bf16 v[212:215], a[216:219], a[8:11], v[212:215]\n"
      "ds_read_b64_tr_b16 a[212:213], v17 offset:20480\n"
      "v_mfma_f32_16x16x32_bf16 v[212:215], a[220:223], a[20:23], v[212:215]\n"
      "ds_read_b64_tr_b16 a[214:215], v17 offset:22528\n"
      "s_waitcnt lgkmcnt(10)\n"
      "v_mfma_f32_16x16x32_bf16 v[192:195], a[192:195], v[108:111], v[192:195]\n"
      "ds_read_b64_tr_b16 a[216:217], v29 offset:16384\n"
      "s_waitcnt lgkmcnt(9)\n"
      "v_mfma_f32_16x16x32_bf16 v[192:195], a[196:199], v[168:171], v[192:195]\n"
      "ds_read_b64_tr_b16 a[218:219], v29 offset:18432\n"
      "s_waitcnt lgkmcnt(8)\n"
      "v_mfma_f32_16x16x32_bf16 v[192:195], a[200:203], a[24:27], v[192:195]\n"
      "ds_read_b64_tr_b16 a[220:221], v29 offset:20480\n"
      "s_waitcnt lgkmcnt(7)\n"
      "v_mfma_f32_16x16x32_bf16 v[192:195], a[204:207], a[36:39], v[192:195]\n"
      "ds_read_b64_tr_b16 a[222:223], v29 offset:22528\n"
      "s_add_u32 s88, s77, s82\n"
      "s_add_u32 m0, s85, 0x10000\n"
      "v_mfma_f32_16x16x32_bf16 v[196:199], a[204:207], a[40:43], v[196:199]\n"
      "buffer_load_dwordx4 v10, s[24:27], s88 offen lds\n"
      "v_mfma_f32_16x16x32_bf16 v[196:199], a[200:203], a[28:31], v[196:199]\n"
      "v_mfma_f32_16x16x32_bf16 v[196:199], a[196:199], v[172:175], v[196:199]\n"
      "buffer_load_dwordx4 v11, s[24:27], s88 offen offset:1024 lds\n"
      "v_mfma_f32_16x16x32_bf16 v[196:199], a[192:195], v[112:115], v[196:199]\n"
      "v_mfma_f32_16x16x32_bf16 v[200:203], a[192:195], v[116:119], v[200:203]\n"
      "buffer_load_dwordx4 v12, s[24:27], s88 offen offset:2048 lds\n"
      "v_mfma_f32_16x16x32_bf16 v[200:203], a[196:199], v[176:179], v[200:203]\n"
      "v_mfma_f32_16x16x32_bf16 v[200:203], a[200:203], a[32:35], v[200:203]\n"
      "buffer_load_dwordx4 v13, s[24:27], s88 offen offset:3072 lds\n"
      "v_mfma_f32_16x16x32_bf16 v[200:203], a[204:207], a[44:47], v[200:203]\n"
      "s_waitcnt lgkmcnt(6)\n"
      "v_mfma_f32_16x16x32_bf16 v[204:207], a[208:211], v[108:111], v[204:207]\n"
      "ds_read_b64_tr_b16 a[192:193], v14 offset:32768\n"
      "s_waitcnt lgkmcnt(5)\n"
      "v_mfma_f32_16x16x32_bf16 v[204:207], a[212:215], v[168:171], v[204:207]\n"
      "ds_read_b64_tr_b16 a[194:195], v14 offset:34816\n"
      "s_waitcnt lgkmcnt(4)\n"
      "v_mfma_f32_16x16x32_bf16 v[204:207], a[216:219], a[24:27], v[204:207]\n"
      "ds_read_b64_tr_b16 a[196:197], v14 offset:36864\n"
      "s_waitcnt lgkmcnt(3)\n"
      "v_mfma_f32_16x16x32_bf16 v[204:207], a[220:223], a[36:39], v[204:207]\n"
      "ds_read_b64_tr_b16 a[198:199], v14 offset:38912\n"
      "v_mfma_f32_16x16x32_bf16 v[208:211], a[220:223], a[40:43], v[208:211]\n"
      "ds_read_b64_tr_b16 a[200:201], v26 offset:32768\n"
      "v_mfma_f32_16x16x32_bf16 v[208:211], a[216:219], a[28:31], v[208:211]\n"
      "ds_read_b64_tr_b16 a[202:203], v26 offset:34816\n"
      "v_mfma_f32_16x16x32_bf16 v[208:211], a[212:215], v[172:175], v[208:211]\n"
      "ds_read_b64_tr_b16 a[204:205], v26 offset:36864\n"
      "v_mfma_f32_16x16x32_bf16 v[208:211], a[208:211], v[112:115], v[208:211]\n"
      "ds_read_b64_tr_b16 a[206:207], v26 offset:38912\n"
      "v_mfma_f32_16x16x32_bf16 v[212:215], a[208:211], v[116:119], v[212:215]\n"
      "ds_read_b64_tr_b16 a[208:209], v15 offset:32768\n"
      "v_mfma_f32_16x16x32_bf16 v[212:215], a[212:215], v[176:179], v[212:215]\n"
      "ds_read_b64_tr_b16 a[210:211], v15 offset:34816\n"
      "v_mfma_f32_16x16x32_bf16 v[212:215], a[216:219], a[32:35], v[212:215]\n"
      "ds_read_b64_tr_b16 a[212:213], v15 offset:36864\n"
      "v_mfma_f32_16x16x32_bf16 v[212:215], a[220:223], a[44:47], v[212:215]\n"
      "ds_read_b64_tr_b16 a[214:215], v15 offset:38912\n"
      "s_waitcnt lgkmcnt(10)\n"
      "v_mfma_f32_16x16x32_bf16 v[192:195], a[192:195], v[180:183], v[192:195]\n"
      "ds_read_b64_tr_b16 a[216:217], v27 offset:32768\n"
      "s_waitcnt lgkmcnt(9)\n"
      "v_mfma_f32_16x16x32_bf16 v[192:195], a[196:199], v[240:243], v[192:195]\n"
      "ds_read_b64_tr_b16 a[218:219], v27 offset:34816\n"
      "s_waitcnt lgkmcnt(8)\n"
      "v_mfma_f32_16x16x32_bf16 v[192:195], a[200:203], a[48:51], v[192:195]\n"
      "ds_read_b64_tr_b16 a[220:221], v27 offset:36864\n"
      "s_waitcnt lgkmcnt(7)\n"
      "v_mfma_f32_16x16x32_bf16 v[192:195], a[204:207], a[60:63], v[192:195]\n"
      "ds_read_b64_tr_b16 a[222:223], v27 offset:38912\n"
      "s_add_u32 s82, s82, 0x80\n"
      "s_cmp_eq_u32 s82, 0x380\n"
      "s_cselect_b32 s82, 0, s82\n"
      "s_add_u32 s88, s74, s82\n"
      "s_add_u32 m0, s85, 0x20000\n"
      "v_mfma_f32_16x16x32_bf16 v[196:199], a[204:207], a[64:67], v[196:199]\n"
      "buffer_load_dwordx4 v10, s[24:27], s88 offen lds\n"
      "v_mfma_f32_16x16x32_bf16 v[196:199], a[200:203], a[52:55], v[196:199]\n"
      "v_mfma_f32_16x16x32_bf16 v[196:199], a[196:199], v[244:247], v[196:199]\n"
      "buffer_load_dwordx4 v11, s[24:27], s88 offen offset:1024 lds\n"
      "v_mfma_f32_16x16x32_bf16 v[196:199], a[192:195], v[184:187], v[196:199]\n"
      "v_mfma_f32_16x16x32_bf16 v[200:203], a[192:195], v[188:191], v[200:203]\n"
      "buffer_load_dwordx4 v12, s[24:27], s88 offen offset:2048 lds\n"
      "v_mfma_f32_16x16x32_bf16 v[200:203], a[196:199], v[248:251], v[200:203]\n"
      "v_mfma_f32_16x16x32_bf16 v[200:203], a[200:203], a[56:59], v[200:203]\n"
      "buffer_load_dwordx4 v13, s[24:27], s88 offen offset:3072 lds\n"
      "v_mfma_f32_16x16x32_bf16 v[200:203], a[204:207], a[68:71], v[200:203]\n"
      "s_waitcnt lgkmcnt(6)\n"
      "v_mfma_f32_16x16x32_bf16 v[204:207], a[208:211], v[180:183], v[204:207]\n"
      "ds_read_b64_tr_b16 a[192:193], v18 offset:32768\n"
      "s_waitcnt lgkmcnt(5)\n"
      "v_mfma_f32_16x16x32_bf16 v[204:207], a[212:215], v[240:243], v[204:207]\n"
      "ds_read_b64_tr_b16 a[194:195], v18 offset:34816\n"
      "s_waitcnt lgkmcnt(4)\n"
      "v_mfma_f32_16x16x32_bf16 v[204:207], a[216:219], a[48:51], v[204:207]\n"
      "ds_read_b64_tr_b16 a[196:197], v18 offset:36864\n"
      "s_waitcnt lgkmcnt(3)\n"
      "v_mfma_f32_16x16x32_bf16 v[204:207], a[220:223], a[60:63], v[204:207]\n"
      "ds_read_b64_tr_b16 a[198:199], v18 offset:38912\n"
      "v_mfma_f32_16x16x32_bf16 v[208:211], a[220:223], a[64:67], v[208:211]\n"
      "ds_read_b64_tr_b16 a[200:201], v24 offset:32768\n"
      "v_mfma_f32_16x16x32_bf16 v[208:211], a[216:219], a[52:55], v[208:211]\n"
      "ds_read_b64_tr_b16 a[202:203], v24 offset:34816\n"
      "v_mfma_f32_16x16x32_bf16 v[208:211], a[212:215], v[244:247], v[208:211]\n"
      "ds_read_b64_tr_b16 a[204:205], v24 offset:36864\n"
      "v_mfma_f32_16x16x32_bf16 v[208:211], a[208:211], v[184:187], v[208:211]\n"
      "ds_read_b64_tr_b16 a[206:207], v24 offset:38912\n"
      "v_mfma_f32_16x16x32_bf16 v[212:215], a[208:211], v[188:191], v[212:215]\n"
      "ds_read_b64_tr_b16 a[208:209], v19 offset:32768\n"
      "v_mfma_f32_16x16x32_bf16 v[212:215], a[212:215], v[248:251], v[212:215]\n"
      "ds_read_b64_tr_b16 a[210:211], v19 offset:34816\n"
      "v_mfma_f32_16x16x32_bf16 v[212:215], a[216:219], a[56:59], v[212:215]\n"
      "ds_read_b64_tr_b16 a[212:213], v19 offset:36864\n"
      "v_mfma_f32_16x16x32_bf16 v[212:215], a[220:223], a[68:71], v[212:215]\n"
      "ds_read_b64_tr_b16 a[214:215], v19 offset:38912\n"
      "s_waitcnt lgkmcnt(10)\n"
      "v_mfma_f32_16x16x32_bf16 v[192:195], a[192:195], v[252:255], v[192:195]\n"
      "ds_read_b64_tr_b16 a[216:217], v25 offset:32768\n"
      "s_waitcnt lgkmcnt(9)\n"
      "v_mfma_f32_16x16x32_bf16 v[192:195], a[196:199], v[224:227], v[192:195]\n"
      "ds_read_b64_tr_b16 a[218:219], v25 offset:34816\n"
      "s_add_u32 s88, s75, s82\n"
      "s_add_u32 m0, s85, 0x14000\n"
      "s_waitcnt lgkmcnt(8)\n"
      "v_mfma_f32_16x16x32_bf16 v[192:195], a[200:203], a[72:75], v[192:195]\n"
      "ds_read_b64_tr_b16 a[220:221], v25 offset:36864\n"
      "buffer_load_dwordx4 v10, s[24:27], s88 offen lds\n"
      "s_waitcnt lgkmcnt(7)\n"
      "v_mfma_f32_16x16x32_bf16 v[192:195], a[204:207], a[84:87], v[192:195]\n"
      "ds_read_b64_tr_b16 a[222:223], v25 offset:38912\n"
      "v_mfma_f32_16x16x32_bf16 v[196:199], a[204:207], a[88:91], v[196:199]\n"
      "buffer_load_dwordx4 v11, s[24:27], s88 offen offset:1024 lds\n"
      "v_mfma_f32_16x16x32_bf16 v[196:199], a[200:203], a[76:79], v[196:199]\n"
      "v_mfma_f32_16x16x32_bf16 v[196:199], a[196:199], v[228:231], v[196:199]\n"
      "buffer_load_dwordx4 v12, s[24:27], s88 offen offset:2048 lds\n"
      "v_mfma_f32_16x16x32_bf16 v[196:199], a[192:195], v[216:219], v[196:199]\n"
      "v_mfma_f32_16x16x32_bf16 v[200:203], a[192:195], v[220:223], v[200:203]\n"
      "buffer_load_dwordx4 v13, s[24:27], s88 offen offset:3072 lds\n"
      "v_mfma_f32_16x16x32_bf16 v[200:203], a[196:199], v[232:235], v[200:203]\n"
      "v_mfma_f32_16x16x32_bf16 v[200:203], a[200:203], a[80:83], v[200:203]\n"
      "s_waitcnt vmcnt(12)\n"
      "buffer_store_dwordx2 v[34:35], v36, s[36:39], s48 offen sc1\n"
      "v_mfma_f32_16x16x32_bf16 v[200:203], a[204:207], a[92:95], v[200:203]\n"
      "s_add_u32 s48, s48, 0x80\n"
      "s_cmp_eq_u32 s48, 0x380\n"
      "s_cselect_b32 s48, 0, s48\n"
      "s_waitcnt lgkmcnt(6)\n"
      "v_mfma_f32_16x16x32_bf16 v[204:207], a[208:211], v[252:255], v[204:207]\n"
      "ds_read_b64_tr_b16 a[192:193], v18\n"
      "s_waitcnt lgkmcnt(5)\n"
      "v_mfma_f32_16x16x32_bf16 v[204:207], a[212:215], v[224:227], v[204:207]\n"
      "ds_read_b64_tr_b16 a[194:195], v18 offset:2048\n"
      "s_waitcnt lgkmcnt(4)\n"
      "v_mfma_f32_16x16x32_bf16 v[204:207], a[216:219], a[72:75], v[204:207]\n"
      "ds_read_b64_tr_b16 a[196:197], v18 offset:4096\n"
      "v_cvt_pk_bf16_f32 v156, v192, v193\n"
      "s_mul_i32 s90, s83, 0x300\n"
      "s_add_u32 s83, s83, 0x80\n"
      "s_cmp_eq_u32 s83, 0x380\n"
      "s_cselect_b32 s83, 0, s83\n"
      "s_waitcnt lgkmcnt(3)\n"
      "v_mfma_f32_16x16x32_bf16 v[204:207], a[220:223], a[84:87], v[204:207]\n"
      "ds_read_b64_tr_b16 a[198:199], v18 offset:6144\n"
      "v_cvt_pk_bf16_f32 v157, v194, v195\n"
      "v_mfma_f32_16x16x32_bf16 v[208:211], a[220:223], a[88:91], v[208:211]\n"
      "ds_read_b64_tr_b16 a[200:201], v24\n"
      "v_cvt_pk_bf16_f32 v160, v196, v197\n"
      "v_mfma_f32_16x16x32_bf16 v[208:211], a[216:219], a[76:79], v[208:211]\n"
      "ds_read_b64_tr_b16 a[202:203], v24 offset:2048\n"
      "v_mfma_f32_16x16x32_bf16 v[208:211], a[212:215], v[228:231], v[208:211]\n"
      "ds_read_b64_tr_b16 a[204:205], v24 offset:4096\n"
      "v_cvt_pk_bf16_f32 v161, v198, v199\n"
      "v_mfma_f32_16x16x32_bf16 v[208:211], a[208:211], v[216:219], v[208:211]\n"
      "ds_read_b64_tr_b16 a[206:207], v24 offset:6144\n"
      "v_cvt_pk_bf16_f32 v164, v200, v201\n"
      "v_mfma_f32_16x16x32_bf16 v[212:215], a[208:211], v[220:223], v[212:215]\n"
      "ds_read_b64_tr_b16 a[208:209], v19\n"
      "v_cvt_pk_bf16_f32 v165, v202, v203\n"
      "v_mfma_f32_16x16x32_bf16 v[212:215], a[212:215], v[232:235], v[212:215]\n"
      "ds_read_b64_tr_b16 a[210:211], v19 offset:2048\n"
      "s_add_u32 s88, s76, s82\n"
      "s_add_u32 m0, s85, 0xc000\n"
      "v_mfma_f32_16x16x32_bf16 v[212:215], a[216:219], a[80:83], v[212:215]\n"
      "ds_read_b64_tr_b16 a[212:213], v19 offset:4096\n"
      "v_cvt_pk_bf16_f32 v158, v204, v205\n"
      "v_cvt_pk_bf16_f32 v159, v206, v207\n"
      "buffer_load_dwordx4 v10, s[24:27], s88 offen lds\n"
      "v_mfma_f32_16x16x32_bf16 v[212:215], a[220:223], a[92:95], v[212:215]\n"
      "ds_read_b64_tr_b16 a[214:215], v19 offset:6144\n"
      "s_waitcnt lgkmcnt(10)\n"
      "v_mfma_f32_16x16x32_bf16 v[192:195], a[192:195], v[60:63], 0\n"
      "ds_read_b64_tr_b16 a[216:217], v25\n"
      "v_cvt_pk_bf16_f32 v162, v208, v209\n"
      "v_cvt_pk_bf16_f32 v163, v210, v211\n"
      "buffer_load_dwordx4 v11, s[24:27], s88 offen offset:1024 lds\n"
      "s_waitcnt lgkmcnt(9)\n"
      "v_mfma_f32_16x16x32_bf16 v[192:195], a[196:199], v[84:87], v[192:195]\n"
      "ds_read_b64_tr_b16 a[218:219], v25 offset:2048\n"
      "s_waitcnt lgkmcnt(8)\n"
      "v_mfma_f32_16x16x32_bf16 v[192:195], a[200:203], a[0:3], v[192:195]\n"
      "ds_read_b64_tr_b16 a[220:221], v25 offset:4096\n"
      "v_cvt_pk_bf16_f32 v166, v212, v213\n"
      "v_cvt_pk_bf16_f32 v167, v214, v215\n"
      "buffer_load_dwordx4 v12, s[24:27], s88 offen offset:2048 lds\n"
      "s_waitcnt lgkmcnt(7)\n"
      "v_mfma_f32_16x16x32_bf16 v[192:195], a[204:207], a[12:15], v[192:195]\n"
      "ds_read_b64_tr_b16 a[222:223], v25 offset:6144\n"
      "v_mfma_f32_16x16x32_bf16 v[196:199], a[204:207], a[16:19], 0\n"
      "buffer_load_dwordx4 v13, s[24:27], s88 offen offset:3072 lds\n"
      "v_mfma_f32_16x16x32_bf16 v[196:199], a[200:203], a[4:7], v[196:199]\n"
      "v_mfma_f32_16x16x32_bf16 v[196:199], a[196:199], v[88:91], v[196:199]\n"
      "buffer_store_dwordx4 v[156:159], v32, s[36:39], s90 offen sc1\n"
      "v_mfma_f32_16x16x32_bf16 v[196:199], a[192:195], v[64:67], v[196:199]\n"
      "v_mfma_f32_16x16x32_bf16 v[200:203], a[192:195], v[68:71], 0\n"
      "buffer_store_dwordx4 v[160:163], v32, s[36:39], s90 offen offset:1024 sc1\n"
      "v_mfma_f32_16x16x32_bf16 v[200:203], a[196:199], v[92:95], v[200:203]\n"
      "v_mfma_f32_16x16x32_bf16 v[200:203], a[200:203], a[8:11], v[200:203]\n"
      "buffer_store_dwordx4 v[164:167], v32, s[36:39], s90 offen offset:2048 sc1\n"
      "v_mfma_f32_16x16x32_bf16 v[200:203], a[204:207], a[20:23], v[200:203]\n"
      "s_waitcnt vmcnt(12) lgkmcnt(0)\n"
      "s_barrier\n"
      "v_mfma_f32_16x16x32_bf16 v[204:207], a[208:211], v[60:63], 0\n"
      "ds_read_b64_tr_b16 a[192:193], v18 offset:16384\n"
      "v_mfma_f32_16x16x32_bf16 v[204:207], a[212:215], v[84:87], v[204:207]\n"
      "ds_read_b64_tr_b16 a[194:195], v18 offset:18432\n"
      "v_mfma_f32_16x16x32_bf16 v[204:207], a[216:219], a[0:3], v[204:207]\n"
      "ds_read_b64_tr_b16 a[196:197], v18 offset:20480\n"
      "v_mfma_f32_16x16x32_bf16 v[204:207], a[220:223], a[12:15], v[204:207]\n"
      "ds_read_b64_tr_b16 a[198:199], v18 offset:22528\n"
      "v_mfma_f32_16x16x32_bf16 v[208:211], a[220:223], a[16:19], 0\n"
      "ds_read_b64_tr_b16 a[200:201], v24 offset:16384\n"
      "v_mfma_f32_16x16x32_bf16 v[208:211], a[216:219], a[4:7], v[208:211]\n"
      "ds_read_b64_tr_b16 a[202:203], v24 offset:18432\n"
      "v_mfma_f32_16x16x32_bf16 v[208:211], a[212:215], v[88:91], v[208:211]\n"
      "ds_read_b64_tr_b16 a[204:205], v24 offset:20480\n"
      "v_mfma_f32_16x16x32_bf16 v[208:211], a[208:211], v[64:67], v[208:211]\n"
      "ds_read_b64_tr_b16 a[206:207], v24 offset:22528\n"
      "v_mfma_f32_16x16x32_bf16 v[212:215], a[208:211], v[68:71], 0\n"
      "ds_read_b64_tr_b16 a[208:209], v19 offset:16384\n"
      "v_mfma_f32_16x16x32_bf16 v[212:215], a[212:215], v[92:95], v[212:215]\n"
      "ds_read_b64_tr_b16 a[210:211], v19 offset:18432\n"
      "v_mfma_f32_16x16x32_bf16 v[212:215], a[216:219], a[8:11], v[212:215]\n"
      "ds_read_b64_tr_b16 a[212:213], v19 offset:20480\n"
      "v_mfma_f32_16x16x32_bf16 v[212:215], a[220:223], a[20:23], v[212:215]\n"
      "ds_read_b64_tr_b16 a[214:215], v19 offset:22528\n"
      "s_waitcnt lgkmcnt(10)\n"
      "v_mfma_f32_16x16x32_bf16 v[192:195], a[192:195], v[108:111], v[192:195]\n"
      "ds_read_b64_tr_b16 a[216:217], v25 offset:16384\n"
      "s_waitcnt lgkmcnt(9)\n"
      "v_mfma_f32_16x16x32_bf16 v[192:195], a[196:199], v[168:171], v[192:195]\n"
      "ds_read_b64_tr_b16 a[218:219], v25 offset:18432\n"
      "s_waitcnt lgkmcnt(8)\n"
      "v_mfma_f32_16x16x32_bf16 v[192:195], a[200:203], a[24:27], v[192:195]\n"
      "ds_read_b64_tr_b16 a[220:221], v25 offset:20480\n"
      "s_waitcnt lgkmcnt(7)\n"
      "v_mfma_f32_16x16x32_bf16 v[192:195], a[204:207], a[36:39], v[192:195]\n"
      "ds_read_b64_tr_b16 a[222:223], v25 offset:22528\n"
      "s_add_u32 s88, s77, s82\n"
      "s_add_u32 m0, s85, 0x24000\n"
      "v_mfma_f32_16x16x32_bf16 v[196:199], a[204:207], a[40:43], v[196:199]\n"
      "buffer_load_dwordx4 v10, s[24:27], s88 offen lds\n"
      "v_mfma_f32_16x16x32_bf16 v[196:199], a[200:203], a[28:31], v[196:199]\n"
      "v_mfma_f32_16x16x32_bf16 v[196:199], a[196:199], v[172:175], v[196:199]\n"
      "buffer_load_dwordx4 v11, s[24:27], s88 offen offset:1024 lds\n"
      "v_mfma_f32_16x16x32_bf16 v[196:199], a[192:195], v[112:115], v[196:199]\n"
      "v_mfma_f32_16x16x32_bf16 v[200:203], a[192:195], v[116:119], v[200:203]\n"
      "buffer_load_dwordx4 v12, s[24:27], s88 offen offset:2048 lds\n"
      "v_mfma_f32_16x16x32_bf16 v[200:203], a[196:199], v[176:179], v[200:203]\n"
      "v_mfma_f32_16x16x32_bf16 v[200:203], a[200:203], a[32:35], v[200:203]\n"
      "buffer_load_dwordx4 v13, s[24:27], s88 offen offset:3072 lds\n"
      "v_mfma_f32_16x16x32_bf16 v[200:203], a[204:207], a[44:47], v[200:203]\n"
      "s_waitcnt lgkmcnt(6)\n"
      "v_mfma_f32_16x16x32_bf16 v[204:207], a[208:211], v[108:111], v[204:207]\n"
      "ds_read_b64_tr_b16 a[192:193], v14 offset:49152\n"
      "s_waitcnt lgkmcnt(5)\n"
      "v_mfma_f32_16x16x32_bf16 v[204:207], a[212:215], v[168:171], v[204:207]\n"
      "ds_read_b64_tr_b16 a[194:195], v14 offset:51200\n"
      "s_waitcnt lgkmcnt(4)\n"
      "v_mfma_f32_16x16x32_bf16 v[204:207], a[216:219], a[24:27], v[204:207]\n"
      "ds_read_b64_tr_b16 a[196:197], v14 offset:53248\n"
      "s_waitcnt lgkmcnt(3)\n"
      "v_mfma_f32_16x16x32_bf16 v[204:207], a[220:223], a[36:39], v[204:207]\n"
      "ds_read_b64_tr_b16 a[198:199], v14 offset:55296\n"
      "v_mfma_f32_16x16x32_bf16 v[208:211], a[220:223], a[40:43], v[208:211]\n"
      "ds_read_b64_tr_b16 a[200:201], v26 offset:49152\n"
      "v_mfma_f32_16x16x32_bf16 v[208:211], a[216:219], a[28:31], v[208:211]\n"
      "ds_read_b64_tr_b16 a[202:203], v26 offset:51200\n"
      "v_mfma_f32_16x16x32_bf16 v[208:211], a[212:215], v[172:175], v[208:211]\n"
      "ds_read_b64_tr_b16 a[204:205], v26 offset:53248\n"
      "v_mfma_f32_16x16x32_bf16 v[208:211], a[208:211], v[112:115], v[208:211]\n"
      "ds_read_b64_tr_b16 a[206:207], v26 offset:55296\n"
      "v_mfma_f32_16x16x32_bf16 v[212:215], a[208:211], v[116:119], v[212:215]\n"
      "ds_read_b64_tr_b16 a[208:209], v15 offset:49152\n"
      "v_mfma_f32_16x16x32_bf16 v[212:215], a[212:215], v[176:179], v[212:215]\n"
      "ds_read_b64_tr_b16 a[210:211], v15 offset:51200\n"
      "v_mfma_f32_16x16x32_bf16 v[212:215], a[216:219], a[32:35], v[212:215]\n"
      "ds_read_b64_tr_b16 a[212:213], v15 offset:53248\n"
      "v_mfma_f32_16x16x32_bf16 v[212:215], a[220:223], a[44:47], v[212:215]\n"
      "ds_read_b64_tr_b16 a[214:215], v15 offset:55296\n"
      "s_waitcnt lgkmcnt(10)\n"
      "v_mfma_f32_16x16x32_bf16 v[192:195], a[192:195], v[180:183], v[192:195]\n"
      "ds_read_b64_tr_b16 a[216:217], v27 offset:49152\n"
      "s_waitcnt lgkmcnt(9)\n"
      "v_mfma_f32_16x16x32_bf16 v[192:195], a[196:199], v[240:243], v[192:195]\n"
      "ds_read_b64_tr_b16 a[218:219], v27 offset:51200\n"
      "s_add_u32 s82, s82, 0x80\n"
      "s_cmp_eq_u32 s82, 0x380\n"
      "s_cselect_b32 s82, 0, s82\n"
      "s_add_u32 s88, s74, s82\n"
      "s_add_u32 m0, s85, 0x18000\n"
      "s_waitcnt lgkmcnt(8)\n"
      "v_mfma_f32_16x16x32_bf16 v[192:195], a[200:203], a[48:51], v[192:195]\n"
      "ds_read_b64_tr_b16 a[220:221], v27 offset:53248\n"
      "buffer_load_dwordx4 v10, s[24:27], s88 offen lds\n"
      "s_waitcnt lgkmcnt(7)\n"
      "v_mfma_f32_16x16x32_bf16 v[192:195], a[204:207], a[60:63], v[192:195]\n"
      "ds_read_b64_tr_b16 a[222:223], v27 offset:55296\n"
      "v_mfma_f32_16x16x32_bf16 v[196:199], a[204:207], a[64:67], v[196:199]\n"
      "buffer_load_dwordx4 v11, s[24:27], s88 offen offset:1024 lds\n"
      "v_mfma_f32_16x16x32_bf16 v[196:199], a[200:203], a[52:55], v[196:199]\n"
      "v_mfma_f32_16x16x32_bf16 v[196:199], a[196:199], v[244:247], v[196:199]\n"
      "buffer_load_dwordx4 v12, s[24:27], s88 offen offset:2048 lds\n"
      "v_mfma_f32_16x16x32_bf16 v[196:199], a[192:195], v[184:187], v[196:199]\n"
      "v_mfma_f32_16x16x32_bf16 v[200:203], a[192:195], v[188:191], v[200:203]\n"
      "buffer_load_dwordx4 v13, s[24:27], s88 offen offset:3072 lds\n"
      "v_mfma_f32_16x16x32_bf16 v[200:203], a[196:199], v[248:251], v[200:203]\n"
      "v_mfma_f32_16x16x32_bf16 v[200:203], a[200:203], a[56:59], v[200:203]\n"
      "buffer_load_dwordx2 v[54:55], v37, s[36:39], 0 offen sc1\n"
      "v_mfma_f32_16x16x32_bf16 v[200:203], a[204:207], a[68:71], v[200:203]\n"
      "s_waitcnt lgkmcnt(6)\n"
      "v_mfma_f32_16x16x32_bf16 v[204:207], a[208:211], v[180:183], v[204:207]\n"
      "ds_read_b64_tr_b16 a[192:193], v14\n"
      "s_waitcnt lgkmcnt(5)\n"
      "v_mfma_f32_16x16x32_bf16 v[204:207], a[212:215], v[240:243], v[204:207]\n"
      "ds_read_b64_tr_b16 a[194:195], v14 offset:2048\n"
      "s_waitcnt lgkmcnt(4)\n"
      "v_mfma_f32_16x16x32_bf16 v[204:207], a[216:219], a[48:51], v[204:207]\n"
      "ds_read_b64_tr_b16 a[196:197], v14 offset:4096\n"
      "s_waitcnt lgkmcnt(3)\n"
      "v_mfma_f32_16x16x32_bf16 v[204:207], a[220:223], a[60:63], v[204:207]\n"
      "ds_read_b64_tr_b16 a[198:199], v14 offset:6144\n"
      "v_mfma_f32_16x16x32_bf16 v[208:211], a[220:223], a[64:67], v[208:211]\n"
      "ds_read_b64_tr_b16 a[200:201], v26\n"
      "v_mfma_f32_16x16x32_bf16 v[208:211], a[216:219], a[52:55], v[208:211]\n"
      "ds_read_b64_tr_b16 a[202:203], v26 offset:2048\n"
      "v_mfma_f32_16x16x32_bf16 v[208:211], a[212:215], v[244:247], v[208:211]\n"
      "ds_read_b64_tr_b16 a[204:205], v26 offset:4096\n"
      "v_mfma_f32_16x16x32_bf16 v[208:211], a[208:211], v[184:187], v[208:211]\n"
      "ds_read_b64_tr_b16 a[206:207], v26 offset:6144\n"
      "v_mfma_f32_16x16x32_bf16 v[212:215], a[208:211], v[188:191], v[212:215]\n"
      "ds_read_b64_tr_b16 a[208:209], v15\n"
      "v_mfma_f32_16x16x32_bf16 v[212:215], a[212:215], v[248:251], v[212:215]\n"
      "ds_read_b64_tr_b16 a[210:211], v15 offset:2048\n"
      "v_mfma_f32_16x16x32_bf16 v[212:215], a[216:219], a[56:59], v[212:215]\n"
      "ds_read_b64_tr_b16 a[212:213], v15 offset:4096\n"
      "v_mfma_f32_16x16x32_bf16 v[212:215], a[220:223], a[68:71], v[212:215]\n"
      "ds_read_b64_tr_b16 a[214:215], v15 offset:6144\n"
      "s_waitcnt lgkmcnt(10)\n"
      "v_mfma_f32_16x16x32_bf16 v[192:195], a[192:195], v[252:255], v[192:195]\n"
      "ds_read_b64_tr_b16 a[216:217], v27\n"
      "s_waitcnt lgkmcnt(9)\n"
      "v_mfma_f32_16x16x32_bf16 v[192:195], a[196:199], v[224:227], v[192:195]\n"
      "ds_read_b64_tr_b16 a[218:219], v27 offset:2048\n"
      "s_add_u32 s88, s75, s82\n"
      "s_add_u32 m0, s85, 0x8000\n"
      "s_waitcnt lgkmcnt(8)\n"
      "v_mfma_f32_16x16x32_bf16 v[192:195], a[200:203], a[72:75], v[192:195]\n"
      "ds_read_b64_tr_b16 a[220:221], v27 offset:4096\n"
      "buffer_load_dwordx4 v10, s[24:27], s88 offen lds\n"
      "s_waitcnt lgkmcnt(7)\n"
      "v_mfma_f32_16x16x32_bf16 v[192:195], a[204:207], a[84:87], v[192:195]\n"
      "ds_read_b64_tr_b16 a[222:223], v27 offset:6144\n"
      "v_mfma_f32_16x16x32_bf16 v[196:199], a[204:207], a[88:91], v[196:199]\n"
      "buffer_load_dwordx4 v11, s[24:27], s88 offen offset:1024 lds\n"
      "v_mfma_f32_16x16x32_bf16 v[196:199], a[200:203], a[76:79], v[196:199]\n"
      "v_mfma_f32_16x16x32_bf16 v[196:199], a[196:199], v[228:231], v[196:199]\n"
      "buffer_load_dwordx4 v12, s[24:27], s88 offen offset:2048 lds\n"
      "v_mfma_f32_16x16x32_bf16 v[196:199], a[192:195], v[216:219], v[196:199]\n"
      "v_mfma_f32_16x16x32_bf16 v[200:203], a[192:195], v[220:223], v[200:203]\n"
      "buffer_load_dwordx4 v13, s[24:27], s88 offen offset:3072 lds\n"
      "v_mfma_f32_16x16x32_bf16 v[200:203], a[196:199], v[232:235], v[200:203]\n"
      "v_mfma_f32_16x16x32_bf16 v[200:203], a[200:203], a[80:83], v[200:203]\n"
      "s_waitcnt vmcnt(13)\n"
      "buffer_store_dwordx2 v[34:35], v36, s[36:39], s48 offen sc1\n"
      "v_mfma_f32_16x16x32_bf16 v[200:203], a[204:207], a[92:95], v[200:203]\n"
      "s_add_u32 s48, s48, 0x80\n"
      "s_cmp_eq_u32 s48, 0x380\n"
      "s_cselect_b32 s48, 0, s48\n"
      "s_waitcnt lgkmcnt(6)\n"
      "v_mfma_f32_16x16x32_bf16 v[204:207], a[208:211], v[252:255], v[204:207]\n"
      "ds_read_b64_tr_b16 a[192:193], v16\n"
      "s_waitcnt lgkmcnt(5)\n"
      "v_mfma_f32_16x16x32_bf16 v[204:207], a[212:215], v[224:227], v[204:207]\n"
      "ds_read_b64_tr_b16 a[194:195], v16 offset:2048\n"
      "s_waitcnt lgkmcnt(4)\n"
      "v_mfma_f32_16x16x32_bf16 v[204:207], a[216:219], a[72:75], v[204:207]\n"
      "ds_read_b64_tr_b16 a[196:197], v16 offset:4096\n"
      "v_cvt_pk_bf16_f32 v144, v192, v193\n"
      "s_mul_i32 s90, s83, 0x300\n"
      "s_add_u32 s83, s83, 0x80\n"
      "s_cmp_eq_u32 s83, 0x380\n"
      "s_cselect_b32 s83, 0, s83\n"
      "s_waitcnt lgkmcnt(3)\n"
      "v_mfma_f32_16x16x32_bf16 v[204:207], a[220:223], a[84:87], v[204:207]\n"
      "ds_read_b64_tr_b16 a[198:199], v16 offset:6144\n"
      "v_cvt_pk_bf16_f32 v145, v194, v195\n"
      "v_mfma_f32_16x16x32_bf16 v[208:211], a[220:223], a[88:91], v[208:211]\n"
      "ds_read_b64_tr_b16 a[200:201], v28\n"
      "v_cvt_pk_bf16_f32 v148, v196, v197\n"
      "v_mfma_f32_16x16x32_bf16 v[208:211], a[216:219], a[76:79], v[208:211]\n"
      "ds_read_b64_tr_b16 a[202:203], v28 offset:2048\n"
      "v_mfma_f32_16x16x32_bf16 v[208:211], a[212:215], v[228:231], v[208:211]\n"
      "ds_read_b64_tr_b16 a[204:205], v28 offset:4096\n"
      "v_cvt_pk_bf16_f32 v149, v198, v199\n"
      "v_mfma_f32_16x16x32_bf16 v[208:211], a[208:211], v[216:219], v[208:211]\n"
      "ds_read_b64_tr_b16 a[206:207], v28 offset:6144\n"
      "v_cvt_pk_bf16_f32 v152, v200, v201\n"
      "v_mfma_f32_16x16x32_bf16 v[212:215], a[208:211], v[220:223], v[212:215]\n"
      "ds_read_b64_tr_b16 a[208:209], v17\n"
      "v_cvt_pk_bf16_f32 v153, v202, v203\n"
      "v_mfma_f32_16x16x32_bf16 v[212:215], a[212:215], v[232:235], v[212:215]\n"
      "ds_read_b64_tr_b16 a[210:211], v17 offset:2048\n"
      "s_add_u32 s88, s76, s82\n"
      "s_add_u32 m0, s85, 0\n"
      "v_mfma_f32_16x16x32_bf16 v[212:215], a[216:219], a[80:83], v[212:215]\n"
      "ds_read_b64_tr_b16 a[212:213], v17 offset:4096\n"
      "v_cvt_pk_bf16_f32 v146, v204, v205\n"
      "v_cvt_pk_bf16_f32 v147, v206, v207\n"
      "buffer_load_dwordx4 v10, s[24:27], s88 offen lds\n"
      "v_mfma_f32_16x16x32_bf16 v[212:215], a[220:223], a[92:95], v[212:215]\n"
      "ds_read_b64_tr_b16 a[214:215], v17 offset:6144\n"
      "s_waitcnt lgkmcnt(10)\n"
      "v_mfma_f32_16x16x32_bf16 v[192:195], a[192:195], v[60:63], 0\n"
      "ds_read_b64_tr_b16 a[216:217], v29\n"
      "v_cvt_pk_bf16_f32 v150, v208, v209\n"
      "v_cvt_pk_bf16_f32 v151, v210, v211\n"
      "buffer_load_dwordx4 v11, s[24:27], s88 offen offset:1024 lds\n"
      "s_waitcnt lgkmcnt(9)\n"
      "v_mfma_f32_16x16x32_bf16 v[192:195], a[196:199], v[84:87], v[192:195]\n"
      "ds_read_b64_tr_b16 a[218:219], v29 offset:2048\n"
      "s_waitcnt lgkmcnt(8)\n"
      "v_mfma_f32_16x16x32_bf16 v[192:195], a[200:203], a[0:3], v[192:195]\n"
      "ds_read_b64_tr_b16 a[220:221], v29 offset:4096\n"
      "v_cvt_pk_bf16_f32 v154, v212, v213\n"
      "v_cvt_pk_bf16_f32 v155, v214, v215\n"
      "buffer_load_dwordx4 v12, s[24:27], s88 offen offset:2048 lds\n"
      "s_waitcnt lgkmcnt(7)\n"
      "v_mfma_f32_16x16x32_bf16 v[192:195], a[204:207], a[12:15], v[192:195]\n"
      "ds_read_b64_tr_b16 a[222:223], v29 offset:6144\n"
      "v_mfma_f32_16x16x32_bf16 v[196:199], a[204:207], a[16:19], 0\n"
      "buffer_load_dwordx4 v13, s[24:27], s88 offen offset:3072 lds\n"
      "v_mfma_f32_16x16x32_bf16 v[196:199], a[200:203], a[4:7], v[196:199]\n"
      "v_mfma_f32_16x16x32_bf16 v[196:199], a[196:199], v[88:91], v[196:199]\n"
      "buffer_store_dwordx4 v[144:147], v32, s[36:39], s90 offen sc1\n"
      "v_mfma_f32_16x16x32_bf16 v[196:199], a[192:195], v[64:67], v[196:199]\n"
      "v_mfma_f32_16x16x32_bf16 v[200:203], a[192:195], v[68:71], 0\n"
      "buffer_store_dwordx4 v[148:151], v32, s[36:39], s90 offen offset:1024 sc1\n"
      "v_mfma_f32_16x16x32_bf16 v[200:203], a[196:199], v[92:95], v[200:203]\n"
      "v_mfma_f32_16x16x32_bf16 v[200:203], a[200:203], a[8:11], v[200:203]\n"
      "buffer_store_dwordx4 v[152:155], v32, s[36:39], s90 offen offset:2048 sc1\n"
      "v_mfma_f32_16x16x32_bf16 v[200:203], a[204:207], a[20:23], v[200:203]\n"
      "s_waitcnt vmcnt(13) lgkmcnt(0)\n"
      "s_barrier\n"
      "v_mfma_f32_16x16x32_bf16 v[204:207], a[208:211], v[60:63], 0\n"
      "ds_read_b64_tr_b16 a[192:193], v14 offset:16384\n"
      "s_waitcnt vmcnt(12)\n"
      "v_cmp_eq_u64 vcc, v[54:55], v[34:35]\n"
      "v_mfma_f32_16x16x32_bf16 v[204:207], a[212:215], v[84:87], v[204:207]\n"
      "ds_read_b64_tr_b16 a[194:195], v14 offset:18432\n"
      "s_cmp_eq_u32 s99, 0\n"
      "s_cselect_b32 s99, 7, s99\n"
      "s_sub_u32 s99, s99, 1\n"
      "s_lshl_b32 s98, 1, s99\n"
      "s_cmp_eq_u32 s50, 7\n"
      "s_cselect_b32 s98, 0, s98\n"
      "v_mfma_f32_16x16x32_bf16 v[204:207], a[216:219], a[0:3], v[204:207]\n"
      "ds_read_b64_tr_b16 a[196:197], v14 offset:20480\n"
      "v_mfma_f32_16x16x32_bf16 v[204:207], a[220:223], a[12:15], v[204:207]\n"
      "ds_read_b64_tr_b16 a[198:199], v14 offset:22528\n"
      "s_and_b32 s92, vcc_lo, s98\n"
      "s_cmp_eq_u32 s92, s98\n"
      "s_cbranch_scc0 20f\n"
      "21:\n"
      "v_mfma_f32_16x16x32_bf16 v[208:211], a[220:223], a[16:19], 0\n"
      "ds_read_b64_tr_b16 a[200:201], v26 offset:16384\n"
      "v_mfma_f32_16x16x32_bf16 v[208:211], a[216:219], a[4:7], v[208:211]\n"
      "ds_read_b64_tr_b16 a[202:203], v26 offset:18432\n"
      "v_mfma_f32_16x16x32_bf16 v[208:211], a[212:215], v[88:91], v[208:211]\n"
      "ds_read_b64_tr_b16 a[204:205], v26 offset:20480\n"
      "v_mfma_f32_16x16x32_bf16 v[208:211], a[208:211], v[64:67], v[208:211]\n"
      "ds_read_b64_tr_b16 a[206:207], v26 offset:22528\n"
      "v_mfma_f32_16x16x32_bf16 v[212:215], a[208:211], v[68:71], 0\n"
      "ds_read_b64_tr_b16 a[208:209], v15 offset:16384\n"
      "v_mfma_f32_16x16x32_bf16 v[212:215], a[212:215], v[92:95], v[212:215]\n"
      "ds_read_b64_tr_b16 a[210:211], v15 offset:18432\n"
      "s_add_u32 s88, s77, s82\n"
      "s_add_u32 m0, s85, 0x4000\n"
      "v_mfma_f32_16x16x32_bf16 v[212:215], a[216:219], a[8:11], v[212:215]\n"
      "ds_read_b64_tr_b16 a[212:213], v15 offset:20480\n"
      "buffer_load_dwordx4 v10, s[24:27], s88 offen lds\n"
      "v_mfma_f32_16x16x32_bf16 v[212:215], a[220:223], a[20:23], v[212:215]\n"
      "ds_read_b64_tr_b16 a[214:215], v15 offset:22528\n"
      "s_waitcnt lgkmcnt(10)\n"
      "v_mfma_f32_16x16x32_bf16 v[192:195], a[192:195], v[108:111], v[192:195]\n"
      "ds_read_b64_tr_b16 a[216:217], v27 offset:16384\n"
      "buffer_load_dwordx4 v11, s[24:27], s88 offen offset:1024 lds\n"
      "s_waitcnt lgkmcnt(9)\n"
      "v_mfma_f32_16x16x32_bf16 v[192:195], a[196:199], v[168:171], v[192:195]\n"
      "ds_read_b64_tr_b16 a[218:219], v27 offset:18432\n"
      "s_waitcnt lgkmcnt(8)\n"
      "v_mfma_f32_16x16x32_bf16 v[192:195], a[200:203], a[24:27], v[192:195]\n"
      "ds_read_b64_tr_b16 a[220:221], v27 offset:20480\n"
      "buffer_load_dwordx4 v12, s[24:27], s88 offen offset:2048 lds\n"
      "s_waitcnt lgkmcnt(7)\n"
      "v_mfma_f32_16x16x32_bf16 v[192:195], a[204:207], a[36:39], v[192:195]\n"
      "ds_read_b64_tr_b16 a[222:223], v27 offset:22528\n"
      "v_mfma_f32_16x16x32_bf16 v[196:199], a[204:207], a[40:43], v[196:199]\n"
      "buffer_load_dwordx4 v13, s[24:27], s88 offen offset:3072 lds\n"
      "v_mfma_f32_16x16x32_bf16 v[196:199], a[200:203], a[28:31], v[196:199]\n"
      "s_mul_i32 s89, s99, 0x3000\n"
      "s_add_u32 s89, s89, s51\n"
      "v_mfma_f32_16x16x32_bf16 v[196:199], a[196:199], v[172:175], v[196:199]\n"
      "buffer_load_dwordx4 a[96:99], v33, s[36:39], s89 offen sc1\n"
      "v_mfma_f32_16x16x32_bf16 v[196:199], a[192:195], v[112:115], v[196:199]\n"
      "v_mfma_f32_16x16x32_bf16 v[200:203], a[192:195], v[116:119], v[200:203]\n"
      "buffer_load_dwordx4 a[100:103], v33, s[36:39], s89 offen offset:1024 sc1\n"
      "v_mfma_f32_16x16x32_bf16 v[200:203], a[196:199], v[176:179], v[200:203]\n"
      "v_mfma_f32_16x16x32_bf16 v[200:203], a[200:203], a[32:35], v[200:203]\n"
      "buffer_load_dwordx4 a[104:107], v33, s[36:39], s89 offen offset:2048 sc1\n"
      "v_mfma_f32_16x16x32_bf16 v[200:203], a[204:207], a[44:47], v[200:203]\n"
      "s_waitcnt lgkmcnt(6)\n"
      "v_mfma_f32_16x16x32_bf16 v[204:207], a[208:211], v[108:111], v[204:207]\n"
      "ds_read_b64_tr_b16 a[192:193], v18 offset:49152\n"
      "s_waitcnt lgkmcnt(5)\n"
      "v_mfma_f32_16x16x32_bf16 v[204:207], a[212:215], v[168:171], v[204:207]\n"
      "ds_read_b64_tr_b16 a[194:195], v18 offset:51200\n"
      "s_waitcnt lgkmcnt(4)\n"
      "v_mfma_f32_16x16x32_bf16 v[204:207], a[216:219], a[24:27], v[204:207]\n"
      "ds_read_b64_tr_b16 a[196:197], v18 offset:53248\n"
      "s_waitcnt lgkmcnt(3)\n"
      "v_mfma_f32_16x16x32_bf16 v[204:207], a[220:223], a[36:39], v[204:207]\n"
      "ds_read_b64_tr_b16 a[198:199], v18 offset:55296\n"
      "v_mfma_f32_16x16x32_bf16 v[208:211], a[220:223], a[40:43], v[208:211]\n"
      "ds_read_b64_tr_b16 a[200:201], v24 offset:49152\n"
      "v_mfma_f32_16x16x32_bf16 v[208:211], a[216:219], a[28:31], v[208:211]\n"
      "ds_read_b64_tr_b16 a[202:203], v24 offset:51200\n"
      "v_mfma_f32_16x16x32_bf16 v[208:211], a[212:215], v[172:175], v[208:211]\n"
      "ds_read_b64_tr_b16 a[204:205], v24 offset:53248\n"
      "v_mfma_f32_16x16x32_bf16 v[208:211], a[208:211], v[112:115], v[208:211]\n"
      "ds_read_b64_tr_b16 a[206:207], v24 offset:55296\n"
      "v_mfma_f32_16x16x32_bf16 v[212:215], a[208:211], v[116:119], v[212:215]\n"
      "ds_read_b64_tr_b16 a[208:209], v19 offset:49152\n"
      "v_mfma_f32_16x16x32_bf16 v[212:215], a[212:215], v[176:179], v[212:215]\n"
      "ds_read_b64_tr_b16 a[210:211], v19 offset:51200\n"
      "v_mfma_f32_16x16x32_bf16 v[212:215], a[216:219], a[32:35], v[212:215]\n"
      "ds_read_b64_tr_b16 a[212:213], v19 offset:53248\n"
      "v_mfma_f32_16x16x32_bf16 v[212:215], a[220:223], a[44:47], v[212:215]\n"
      "ds_read_b64_tr_b16 a[214:215], v19 offset:55296\n"
      "s_waitcnt lgkmcnt(10)\n"
      "v_mfma_f32_16x16x32_bf16 v[192:195], a[192:195], v[180:183], v[192:195]\n"
      "ds_read_b64_tr_b16 a[216:217], v25 offset:49152\n"
      "s_waitcnt lgkmcnt(9)\n"
      "v_mfma_f32_16x16x32_bf16 v[192:195], a[196:199], v[240:243], v[192:195]\n"
      "ds_read_b64_tr_b16 a[218:219], v25 offset:51200\n"
      "s_add_u32 s82, s82, 0x80\n"
      "s_cmp_eq_u32 s82, 0x380\n"
      "s_cselect_b32 s82, 0, s82\n"
      "s_add_u32 s88, s74, s82\n"
      "s_add_u32 m0, s85, 0x1c000\n"
      "s_waitcnt lgkmcnt(8)\n"
      "v_mfma_f32_16x16x32_bf16 v[192:195], a[200:203], a[48:51], v[192:195]\n"
      "ds_read_b64_tr_b16 a[220:221], v25 offset:53248\n"
      "buffer_load_dwordx4 v10, s[24:27], s88 offen lds\n"
      "s_waitcnt lgkmcnt(7)\n"
      "v_mfma_f32_16x16x32_bf16 v[192:195], a[204:207], a[60:63], v[192:195]\n"
      "ds_read_b64_tr_b16 a[222:223], v25 offset:55296\n"
      "v_mfma_f32_16x16x32_bf16 v[196:199], a[204:207], a[64:67], v[196:199]\n"
      "buffer_load_dwordx4 v11, s[24:27], s88 offen offset:1024 lds\n"
      "v_mfma_f32_16x16x32_bf16 v[196:199], a[200:203], a[52:55], v[196:199]\n"
      "v_mfma_f32_16x16x32_bf16 v[196:199], a[196:199], v[244:247], v[196:199]\n"
      "buffer_load_dwordx4 v12, s[24:27], s88 offen offset:2048 lds\n"
      "v_mfma_f32_16x16x32_bf16 v[196:199], a[192:195], v[184:187], v[196:199]\n"
      "v_mfma_f32_16x16x32_bf16 v[200:203], a[192:195], v[188:191], v[200:203]\n"
      "buffer_load_dwordx4 v13, s[24:27], s88 offen offset:3072 lds\n"
      "v_mfma_f32_16x16x32_bf16 v[200:203], a[196:199], v[248:251], v[200:203]\n"
      "v_mfma_f32_16x16x32_bf16 v[200:203], a[200:203], a[56:59], v[200:203]\n"
      "buffer_load_dwordx2 v[54:55], v37, s[36:39], 0 offen sc1\n"
      "v_mfma_f32_16x16x32_bf16 v[200:203], a[204:207], a[68:71], v[200:203]\n"
      "s_waitcnt lgkmcnt(6)\n"
      "v_mfma_f32_16x16x32_bf16 v[204:207], a[208:211], v[180:183], v[204:207]\n"
      "ds_read_b64_tr_b16 a[192:193], v16 offset:16384\n"
      "s_waitcnt lgkmcnt(5)\n"
      "v_mfma_f32_16x16x32_bf16 v[204:207], a[212:215], v[240:243], v[204:207]\n"
      "ds_read_b64_tr_b16 a[194:195], v16 offset:18432\n"
      "s_waitcnt lgkmcnt(4)\n"
      "v_mfma_f32_16x16x32_bf16 v[204:207], a[216:219], a[48:51], v[204:207]\n"
      "ds_read_b64_tr_b16 a[196:197], v16 offset:20480\n"
      "s_waitcnt lgkmcnt(3)\n"
      "v_mfma_f32_16x16x32_bf16 v[204:207], a[220:223], a[60:63], v[204:207]\n"
      "ds_read_b64_tr_b16 a[198:199], v16 offset:22528\n"
      "v_mfma_f32_16x16x32_bf16 v[208:211], a[220:223], a[64:67], v[208:211]\n"
      "ds_read_b64_tr_b16 a[200:201], v28 offset:16384\n"
      "v_mfma_f32_16x16x32_bf16 v[208:211], a[216:219], a[52:55], v[208:211]\n"
      "ds_read_b64_tr_b16 a[202:203], v28 offset:18432\n"
      "v_mfma_f32_16x16x32_bf16 v[208:211], a[212:215], v[244:247], v[208:211]\n"
      "ds_read_b64_tr_b16 a[204:205], v28 offset:20480\n"
      "v_mfma_f32_16x16x32_bf16 v[208:211], a[208:211], v[184:187], v[208:211]\n"
      "ds_read_b64_tr_b16 a[206:207], v28 offset:22528\n"
      "v_mfma_f32_16x16x32_bf16 v[212:215], a[208:211], v[188:191], v[212:215]\n"
      "ds_read_b64_tr_b16 a[208:209], v17 offset:16384\n"
      "v_mfma_f32_16x16x32_bf16 v[212:215], a[212:215], v[248:251], v[212:215]\n"
      "ds_read_b64_tr_b16 a[210:211], v17 offset:18432\n"
      "v_mfma_f32_16x16x32_bf16 v[212:215], a[216:219], a[56:59], v[212:215]\n"
      "ds_read_b64_tr_b16 a[212:213], v17 offset:20480\n"
      "v_mfma_f32_16x16x32_bf16 v[212:215], a[220:223], a[68:71], v[212:215]\n"
      "ds_read_b64_tr_b16 a[214:215], v17 offset:22528\n"
      "s_waitcnt lgkmcnt(10)\n"
      "v_mfma_f32_16x16x32_bf16 v[192:195], a[192:195], v[252:255], v[192:195]\n"
      "ds_read_b64_tr_b16 a[216:217], v29 offset:16384\n"
      "s_waitcnt lgkmcnt(9)\n"
      "v_mfma_f32_16x16x32_bf16 v[192:195], a[196:199], v[224:227], v[192:195]\n"
      "ds_read_b64_tr_b16 a[218:219], v29 offset:18432\n"
      "s_add_u32 s88, s75, s82\n"
      "s_add_u32 m0, s85, 0x10000\n"
      "s_waitcnt lgkmcnt(8)\n"
      "v_mfma_f32_16x16x32_bf16 v[192:195], a[200:203], a[72:75], v[192:195]\n"
      "ds_read_b64_tr_b16 a[220:221], v29 offset:20480\n"
      "buffer_load_dwordx4 v10, s[24:27], s88 offen lds\n"
      "s_waitcnt lgkmcnt(7)\n"
      "v_mfma_f32_16x16x32_bf16 v[192:195], a[204:207], a[84:87], v[192:195]\n"
      "ds_read_b64_tr_b16 a[222:223], v29 offset:22528\n"
      "v_mfma_f32_16x16x32_bf16 v[196:199], a[204:207], a[88:91], v[196:199]\n"
      "buffer_load_dwordx4 v11, s[24:27], s88 offen offset:1024 lds\n"
      "v_mfma_f32_16x16x32_bf16 v[196:199], a[200:203], a[76:79], v[196:199]\n"
      "v_mfma_f32_16x16x32_bf16 v[196:199], a[196:199], v[228:231], v[196:199]\n"
      "buffer_load_dwordx4 v12, s[24:27], s88 offen offset:2048 lds\n"
      "v_mfma_f32_16x16x32_bf16 v[196:199], a[192:195], v[216:219], v[196:199]\n"
      "v_mfma_f32_16x16x32_bf16 v[200:203], a[192:195], v[220:223], v[200:203]\n"
      "buffer_load_dwordx4 v13, s[24:27], s88 offen offset:3072 lds\n"
      "v_mfma_f32_16x16x32_bf16 v[200:203], a[196:199], v[232:235], v[200:203]\n"
      "v_mfma_f32_16x16x32_bf16 v[200:203], a[200:203], a[80:83], v[200:203]\n"
      "s_waitcnt vmcnt(16)\n"
      "buffer_store_dwordx2 v[34:35], v36, s[36:39], s48 offen sc1\n"
      "v_mfma_f32_16x16x32_bf16 v[200:203], a[204:207], a[92:95], v[200:203]\n"
      "s_add_u32 s48, s48, 0x80\n"
      "s_cmp_eq_u32 s48, 0x380\n"
      "s_cselect_b32 s48, 0, s48\n"
      "s_waitcnt lgkmcnt(6)\n"
      "v_mfma_f32_16x16x32_bf16 v[204:207], a[208:211], v[252:255], v[204:207]\n"
      "ds_read_b64_tr_b16 a[192:193], v14 offset:32768\n"
      "s_waitcnt lgkmcnt(5)\n"
      "v_mfma_f32_16x16x32_bf16 v[204:207], a[212:215], v[224:227], v[204:207]\n"
      "ds_read_b64_tr_b16 a[194:195], v14 offset:34816\n"
      "s_waitcnt lgkmcnt(4)\n"
      "v_mfma_f32_16x16x32_bf16 v[204:207], a[216:219], a[72:75], v[204:207]\n"
      "ds_read_b64_tr_b16 a[196:197], v14 offset:36864\n"
      "v_cvt_pk_bf16_f32 v156, v192, v193\n"
      "s_mul_i32 s90, s83, 0x300\n"
      "s_add_u32 s83, s83, 0x80\n"
      "s_cmp_eq_u32 s83, 0x380\n"
      "s_cselect_b32 s83, 0, s83\n"
      "s_waitcnt lgkmcnt(3)\n"
      "v_mfma_f32_16x16x32_bf16 v[204:207], a[220:223], a[84:87], v[204:207]\n"
      "ds_read_b64_tr_b16 a[198:199], v14 offset:38912\n"
      "v_cvt_pk_bf16_f32 v157, v194, v195\n"
      "v_mfma_f32_16x16x32_bf16 v[208:211], a[220:223], a[88:91], v[208:211]\n"
      "ds_read_b64_tr_b16 a[200:201], v26 offset:32768\n"
      "v_cvt_pk_bf16_f32 v160, v196, v197\n"
      "v_mfma_f32_16x16x32_bf16 v[208:211], a[216:219], a[76:79], v[208:211]\n"
      "ds_read_b64_tr_b16 a[202:203], v26 offset:34816\n"
      "v_mfma_f32_16x16x32_bf16 v[208:211], a[212:215], v[228:231], v[208:211]\n"
      "ds_read_b64_tr_b16 a[204:205], v26 offset:36864\n"
      "v_cvt_pk_bf16_f32 v161, v198, v199\n"
      "v_mfma_f32_16x16x32_bf16 v[208:211], a[208:211], v[216:219], v[208:211]\n"
      "ds_read_b64_tr_b16 a[206:207], v26 offset:38912\n"
      "v_cvt_pk_bf16_f32 v164, v200, v201\n"
      "v_mfma_f32_16x16x32_bf16 v[212:215], a[208:211], v[220:223], v[212:215]\n"
      "ds_read_b64_tr_b16 a[208:209], v15 offset:32768\n"
      "v_cvt_pk_bf16_f32 v165, v202, v203\n"
      "v_mfma_f32_16x16x32_bf16 v[212:215], a[212:215], v[232:235], v[212:215]\n"
      "ds_read_b64_tr_b16 a[210:211], v15 offset:34816\n"
      "v_mfma_f32_16x16x32_bf16 v[212:215], a[216:219], a[80:83], v[212:215]\n"
      "ds_read_b64_tr_b16 a[212:213], v15 offset:36864\n"
      "v_cvt_pk_bf16_f32 v158, v204, v205\n"
      "v_cvt_pk_bf16_f32 v159, v206, v207\n"
      "v_mfma_f32_16x16x32_bf16 v[212:215], a[220:223], a[92:95], v[212:215]\n"
      "ds_read_b64_tr_b16 a[214:215], v15 offset:38912\n"
      "s_waitcnt vmcnt(12)\n"
      "v_mfma_f32_16x16x32_bf16 v[120:123], v[50:53], a[96:99], 0\n"
      "ds_read_b64_tr_b16 a[216:217], v27 offset:32768\n"
      "v_cvt_pk_bf16_f32 v162, v208, v209\n"
      "v_cvt_pk_bf16_f32 v163, v210, v211\n"
      "v_mfma_f32_16x16x32_bf16 v[132:135], v[48:51], a[96:99], 0\n"
      "ds_read_b64_tr_b16 a[218:219], v27 offset:34816\n"
      "s_waitcnt vmcnt(11)\n"
      "v_mfma_f32_16x16x32_bf16 v[124:127], v[50:53], a[100:103], 0\n"
      "ds_read_b64_tr_b16 a[220:221], v27 offset:36864\n"
      "v_cvt_pk_bf16_f32 v166, v212, v213\n"
      "v_cvt_pk_bf16_f32 v167, v214, v215\n"
      "v_mfma_f32_16x16x32_bf16 v[136:139], v[48:51], a[100:103], 0\n"
      "ds_read_b64_tr_b16 a[222:223], v27 offset:38912\n"
      "s_add_u32 s88, s76, s82\n"
      "s_add_u32 m0, s85, 0x20000\n"
      "s_waitcnt vmcnt(10)\n"
      "v_mfma_f32_16x16x32_bf16 v[128:131], v[50:53], a[104:107], 0\n"
      "buffer_load_dwordx4 v10, s[24:27], s88 offen lds\n"
      "v_mfma_f32_16x16x32_bf16 v[140:143], v[48:51], a[104:107], 0\n"
      "s_waitcnt lgkmcnt(14)\n"
      "v_mfma_f32_16x16x32_bf16 v[192:195], a[192:195], v[60:63], 0\n"
      "buffer_load_dwordx4 v11, s[24:27], s88 offen offset:1024 lds\n"
      "s_waitcnt lgkmcnt(12)\n"
      "v_mfma_f32_16x16x32_bf16 v[192:195], a[196:199], v[84:87], v[192:195]\n"
      "s_waitcnt lgkmcnt(10)\n"
      "v_mfma_f32_16x16x32_bf16 v[192:195], a[200:203], a[0:3], v[192:195]\n"
      "buffer_load_dwordx4 v12, s[24:27], s88 offen offset:2048 lds\n"
      "s_waitcnt lgkmcnt(8)\n"
      "v_mfma_f32_16x16x32_bf16 v[192:195], a[204:207], a[12:15], v[192:195]\n"
      "v_mfma_f32_16x16x32_bf16 v[196:199], a[204:207], a[16:19], 0\n"
      "buffer_load_dwordx4 v13, s[24:27], s88 offen offset:3072 lds\n"
      "v_mfma_f32_16x16x32_bf16 v[196:199], a[200:203], a[4:7], v[196:199]\n"
      "v_mfma_f32_16x16x32_bf16 v[196:199], a[196:199], v[88:91], v[196:199]\n"
      "buffer_store_dwordx4 v[156:159], v32, s[36:39], s90 offen sc1\n"
      "v_mfma_f32_16x16x32_bf16 v[196:199], a[192:195], v[64:67], v[196:199]\n"
      "v_mfma_f32_16x16x32_bf16 v[200:203], a[192:195], v[68:71], 0\n"
      "buffer_store_dwordx4 v[160:163], v32, s[36:39], s90 offen offset:1024 sc1\n"
      "v_mfma_f32_16x16x32_bf16 v[200:203], a[196:199], v[92:95], v[200:203]\n"
      "v_mfma_f32_16x16x32_bf16 v[200:203], a[200:203], a[8:11], v[200:203]\n"
      "buffer_store_dwordx4 v[164:167], v32, s[36:39], s90 offen offset:2048 sc1\n"
      "v_mfma_f32_16x16x32_bf16 v[200:203], a[204:207], a[20:23], v[200:203]\n"
      "s_waitcnt vmcnt(13) lgkmcnt(0)\n"
      "s_barrier\n"
      "v_mfma_f32_16x16x32_bf16 v[204:207], a[208:211], v[60:63], 0\n"
      "ds_read_b64_tr_b16 a[192:193], v18 offset:32768\n"
      "s_waitcnt vmcnt(12)\n"
      "v_cmp_eq_u64 vcc, v[54:55], v[34:35]\n"
      "v_mfma_f32_16x16x32_bf16 v[204:207], a[212:215], v[84:87], v[204:207]\n"
      "ds_read_b64_tr_b16 a[194:195], v18 offset:34816\n"
      "s_cmp_eq_u32 s99, 0\n"
      "s_cselect_b32 s99, 7, s99\n"
      "s_sub_u32 s99, s99, 1\n"
      "s_lshl_b32 s98, 1, s99\n"
      "s_cmp_eq_u32 s50, 7\n"
      "s_cselect_b32 s98, 0, s98\n"
      "v_mfma_f32_16x16x32_bf16 v[204:207], a[216:219], a[0:3], v[204:207]\n"
      "ds_read_b64_tr_b16 a[196:197], v18 offset:36864\n"
      "v_mfma_f32_16x16x32_bf16 v[204:207], a[220:223], a[12:15], v[204:207]\n"
      "ds_read_b64_tr_b16 a[198:199], v18 offset:38912\n"
      "s_and_b32 s92, vcc_lo, s98\n"
      "s_cmp_eq_u32 s92, s98\n"
      "s_cbranch_scc0 22f\n"
      "23:\n"
      "v_mfma_f32_16x16x32_bf16 v[208:211], a[220:223], a[16:19], 0\n"
      "ds_read_b64_tr_b16 a[200:201], v24 offset:32768\n"
      "v_mfma_f32_16x16x32_bf16 v[208:211], a[216:219], a[4:7], v[208:211]\n"
      "ds_read_b64_tr_b16 a[202:203], v24 offset:34816\n"
      "v_mfma_f32_16x16x32_bf16 v[208:211], a[212:215], v[88:91], v[208:211]\n"
      "ds_read_b64_tr_b16 a[204:205], v24 offset:36864\n"
      "v_mfma_f32_16x16x32_bf16 v[208:211], a[208:211], v[64:67], v[208:211]\n"
      "ds_read_b64_tr_b16 a[206:207], v24 offset:38912\n"
      "v_mfma_f32_16x16x32_bf16 v[212:215], a[208:211], v[68:71], 0\n"
      "ds_read_b64_tr_b16 a[208:209], v19 offset:32768\n"
      "v_mfma_f32_16x16x32_bf16 v[212:215], a[212:215], v[92:95], v[212:215]\n"
      "ds_read_b64_tr_b16 a[210:211], v19 offset:34816\n"
      "s_add_u32 s88, s77, s82\n"
      "s_add_u32 m0, s85, 0x14000\n"
      "v_mfma_f32_16x16x32_bf16 v[212:215], a[216:219], a[8:11], v[212:215]\n"
      "ds_read_b64_tr_b16 a[212:213], v19 offset:36864\n"
      "buffer_load_dwordx4 v10, s[24:27], s88 offen lds\n"
      "v_mfma_f32_16x16x32_bf16 v[212:215], a[220:223], a[20:23], v[212:215]\n"
      "ds_read_b64_tr_b16 a[214:215], v19 offset:38912\n"
      "s_waitcnt lgkmcnt(10)\n"
      "v_mfma_f32_16x16x32_bf16 v[192:195], a[192:195], v[108:111], v[192:195]\n"
      "ds_read_b64_tr_b16 a[216:217], v25 offset:32768\n"
      "buffer_load_dwordx4 v11, s[24:27], s88 offen offset:1024 lds\n"
      "s_waitcnt lgkmcnt(9)\n"
      "v_mfma_f32_16x16x32_bf16 v[192:195], a[196:199], v[168:171], v[192:195]\n"
      "ds_read_b64_tr_b16 a[218:219], v25 offset:34816\n"
      "s_waitcnt lgkmcnt(8)\n"
      "v_mfma_f32_16x16x32_bf16 v[192:195], a[200:203], a[24:27], v[192:195]\n"
      "ds_read_b64_tr_b16 a[220:221], v25 offset:36864\n"
      "buffer_load_dwordx4 v12, s[24:27], s88 offen offset:2048 lds\n"
      "s_waitcnt lgkmcnt(7)\n"
      "v_mfma_f32_16x16x32_bf16 v[192:195], a[204:207], a[36:39], v[192:195]\n"
      "ds_read_b64_tr_b16 a[222:223], v25 offset:38912\n"
      "v_mfma_f32_16x16x32_bf16 v[196:199], a[204:207], a[40:43], v[196:199]\n"
      "buffer_load_dwordx4 v13, s[24:27], s88 offen offset:3072 lds\n"
      "v_mfma_f32_16x16x32_bf16 v[196:199], a[200:203], a[28:31], v[196:199]\n"
      "s_mul_i32 s89, s99, 0x3000\n"
      "s_add_u32 s89, s89, s51\n"
      "v_mfma_f32_16x16x32_bf16 v[196:199], a[196:199], v[172:175], v[196:199]\n"
      "buffer_load_dwordx4 a[108:111], v33, s[36:39], s89 offen sc1\n"
      "v_mfma_f32_16x16x32_bf16 v[196:199], a[192:195], v[112:115], v[196:199]\n"
      "v_mfma_f32_16x16x32_bf16 v[200:203], a[192:195], v[116:119], v[200:203]\n"
      "buffer_load_dwordx4 a[112:115], v33, s[36:39], s89 offen offset:1024 sc1\n"
      "v_mfma_f32_16x16x32_bf16 v[200:203], a[196:199], v[176:179], v[200:203]\n"
      "v_mfma_f32_16x16x32_bf16 v[200:203], a[200:203], a[32:35], v[200:203]\n"
      "buffer_load_dwordx4 a[116:119], v33, s[36:39], s89 offen offset:2048 sc1\n"
      "v_mfma_f32_16x16x32_bf16 v[200:203], a[204:207], a[44:47], v[200:203]\n"
      "s_waitcnt lgkmcnt(6)\n"
      "v_mfma_f32_16x16x32_bf16 v[204:207], a[208:211], v[108:111], v[204:207]\n"
      "ds_read_b64_tr_b16 a[192:193], v18\n"
      "s_waitcnt lgkmcnt(5)\n"
      "v_mfma_f32_16x16x32_bf16 v[204:207], a[212:215], v[168:171], v[204:207]\n"
      "ds_read_b64_tr_b16 a[194:195], v18 offset:2048\n"
      "s_waitcnt lgkmcnt(4)\n"
      "v_mfma_f32_16x16x32_bf16 v[204:207], a[216:219], a[24:27], v[204:207]\n"
      "ds_read_b64_tr_b16 a[196:197], v18 offset:4096\n"
      "s_waitcnt lgkmcnt(3)\n"
      "v_mfma_f32_16x16x32_bf16 v[204:207], a[220:223], a[36:39], v[204:207]\n"
      "ds_read_b64_tr_b16 a[198:199], v18 offset:6144\n"
      "v_mfma_f32_16x16x32_bf16 v[208:211], a[220:223], a[40:43], v[208:211]\n"
      "ds_read_b64_tr_b16 a[200:201], v24\n"
      "v_mfma_f32_16x16x32_bf16 v[208:211], a[216:219], a[28:31], v[208:211]\n"
      "ds_read_b64_tr_b16 a[202:203], v24 offset:2048\n"
      "v_mfma_f32_16x16x32_bf16 v[208:211], a[212:215], v[172:175], v[208:211]\n"
      "ds_read_b64_tr_b16 a[204:205], v24 offset:4096\n"
      "v_mfma_f32_16x16x32_bf16 v[208:211], a[208:211], v[112:115], v[208:211]\n"
      "ds_read_b64_tr_b16 a[206:207], v24 offset:6144\n"
      "v_mfma_f32_16x16x32_bf16 v[212:215], a[208:211], v[116:119], v[212:215]\n"
      "ds_read_b64_tr_b16 a[208:209], v19\n"
      "v_mfma_f32_16x16x32_bf16 v[212:215], a[212:215], v[176:179], v[212:215]\n"
      "ds_read_b64_tr_b16 a[210:211], v19 offset:2048\n"
      "v_mfma_f32_16x16x32_bf16 v[212:215], a[216:219], a[32:35], v[212:215]\n"
      "ds_read_b64_tr_b16 a[212:213], v19 offset:4096\n"
      "v_mfma_f32_16x16x32_bf16 v[212:215], a[220:223], a[44:47], v[212:215]\n"
      "ds_read_b64_tr_b16 a[214:215], v19 offset:6144\n"
      "s_waitcnt lgkmcnt(10)\n"
      "v_mfma_f32_16x16x32_bf16 v[192:195], a[192:195], v[180:183], v[192:195]\n"
      "ds_read_b64_tr_b16 a[216:217], v25\n"
      "s_waitcnt lgkmcnt(9)\n"
      "v_mfma_f32_16x16x32_bf16 v[192:195], a[196:199], v[240:243], v[192:195]\n"
      "ds_read_b64_tr_b16 a[218:219], v25 offset:2048\n"
      "s_add_u32 s82, s82, 0x80\n"
      "s_cmp_eq_u32 s82, 0x380\n"
      "s_cselect_b32 s82, 0, s82\n"
      "s_add_u32 s88, s74, s82\n"
      "s_add_u32 m0, s85, 0xc000\n"
      "s_waitcnt lgkmcnt(8)\n"
      "v_mfma_f32_16x16x32_bf16 v[192:195], a[200:203], a[48:51], v[192:195]\n"
      "ds_read_b64_tr_b16 a[220:221], v25 offset:4096\n"
      "buffer_load_dwordx4 v10, s[24:27], s88 offen lds\n"
      "s_waitcnt lgkmcnt(7)\n"
      "v_mfma_f32_16x16x32_bf16 v[192:195], a[204:207], a[60:63], v[192:195]\n"
      "ds_read_b64_tr_b16 a[222:223], v25 offset:6144\n"
      "v_mfma_f32_16x16x32_bf16 v[196:199], a[204:207], a[64:67], v[196:199]\n"
      "buffer_load_dwordx4 v11, s[24:27], s88 offen offset:1024 lds\n"
      "v_mfma_f32_16x16x32_bf16 v[196:199], a[200:203], a[52:55], v[196:199]\n"
      "v_mfma_f32_16x16x32_bf16 v[196:199], a[196:199], v[244:247], v[196:199]\n"
      "buffer_load_dwordx4 v12, s[24:27], s88 offen offset:2048 lds\n"
      "v_mfma_f32_16x16x32_bf16 v[196:199], a[192:195], v[184:187], v[196:199]\n"
      "v_mfma_f32_16x16x32_bf16 v[200:203], a[192:195], v[188:191], v[200:203]\n"
      "buffer_load_dwordx4 v13, s[24:27], s88 offen offset:3072 lds\n"
      "v_mfma_f32_16x16x32_bf16 v[200:203], a[196:199], v[248:251], v[200:203]\n"
      "v_mfma_f32_16x16x32_bf16 v[200:203], a[200:203], a[56:59], v[200:203]\n"
      "buffer_load_dwordx2 v[54:55], v37, s[36:39], 0 offen sc1\n"
      "v_mfma_f32_16x16x32_bf16 v[200:203], a[204:207], a[68:71], v[200:203]\n"
      "s_waitcnt lgkmcnt(6)\n"
      "v_mfma_f32_16x16x32_bf16 v[204:207], a[208:211], v[180:183], v[204:207]\n"
      "ds_read_b64_tr_b16 a[192:193], v18 offset:16384\n"
      "s_waitcnt lgkmcnt(5)\n"
      "v_mfma_f32_16x16x32_bf16 v[204:207], a[212:215], v[240:243], v[204:207]\n"
      "ds_read_b64_tr_b16 a[194:195], v18 offset:18432\n"
      "s_waitcnt lgkmcnt(4)\n"
      "v_mfma_f32_16x16x32_bf16 v[204:207], a[216:219], a[48:51], v[204:207]\n"
      "ds_read_b64_tr_b16 a[196:197], v18 offset:20480\n"
      "s_waitcnt lgkmcnt(3)\n"
      "v_mfma_f32_16x16x32_bf16 v[204:207], a[220:223], a[60:63], v[204:207]\n"
      "ds_read_b64_tr_b16 a[198:199], v18 offset:22528\n"
      "v_mfma_f32_16x16x32_bf16 v[208:211], a[220:223], a[64:67], v[208:211]\n"
      "ds_read_b64_tr_b16 a[200:201], v24 offset:16384\n"
      "v_mfma_f32_16x16x32_bf16 v[208:211], a[216:219], a[52:55], v[208:211]\n"
      "ds_read_b64_tr_b16 a[202:203], v24 offset:18432\n"
      "v_mfma_f32_16x16x32_bf16 v[208:211], a[212:215], v[244:247], v[208:211]\n"
      "ds_read_b64_tr_b16 a[204:205], v24 offset:20480\n"
      "v_mfma_f32_16x16x32_bf16 v[208:211], a[208:211], v[184:187], v[208:211]\n"
      "ds_read_b64_tr_b16 a[206:207], v24 offset:22528\n"
      "v_mfma_f32_16x16x32_bf16 v[212:215], a[208:211], v[188:191], v[212:215]\n"
      "ds_read_b64_tr_b16 a[208:209], v19 offset:16384\n"
      "v_mfma_f32_16x16x32_bf16 v[212:215], a[212:215], v[248:251], v[212:215]\n"
      "ds_read_b64_tr_b16 a[210:211], v19 offset:18432\n"
      "v_mfma_f32_16x16x32_bf16 v[212:215], a[216:219], a[56:59], v[212:215]\n"
      "ds_read_b64_tr_b16 a[212:213], v19 offset:20480\n"
      "v_mfma_f32_16x16x32_bf16 v[212:215], a[220:223], a[68:71], v[212:215]\n"
      "ds_read_b64_tr_b16 a[214:215], v19 offset:22528\n"
      "s_waitcnt lgkmcnt(10)\n"
      "v_mfma_f32_16x16x32_bf16 v[192:195], a[192:195], v[252:255], v[192:195]\n"
      "ds_read_b64_tr_b16 a[216:217], v25 offset:16384\n"
      "s_waitcnt lgkmcnt(9)\n"
      "v_mfma_f32_16x16x32_bf16 v[192:195], a[196:199], v[224:227], v[192:195]\n"
      "ds_read_b64_tr_b16 a[218:219], v25 offset:18432\n"
      "s_add_u32 s88, s75, s82\n"
      "s_add_u32 m0, s85, 0x24000\n"
      "s_waitcnt lgkmcnt(8)\n"
      "v_mfma_f32_16x16x32_bf16 v[192:195], a[200:203], a[72:75], v[192:195]\n"
      "ds_read_b64_tr_b16 a[220:221], v25 offset:20480\n"
      "buffer_load_dwordx4 v10, s[24:27], s88 offen lds\n"
      "s_waitcnt lgkmcnt(7)\n"
      "v_mfma_f32_16x16x32_bf16 v[192:195], a[204:207], a[84:87], v[192:195]\n"
      "ds_read_b64_tr_b16 a[222:223], v25 offset:22528\n"
      "v_mfma_f32_16x16x32_bf16 v[196:199], a[204:207], a[88:91], v[196:199]\n"
      "buffer_load_dwordx4 v11, s[24:27], s88 offen offset:1024 lds\n"
      "v_mfma_f32_16x16x32_bf16 v[196:199], a[200:203], a[76:79], v[196:199]\n"
      "v_mfma_f32_16x16x32_bf16 v[196:199], a[196:199], v[228:231], v[196:199]\n"
      "buffer_load_dwordx4 v12, s[24:27], s88 offen offset:2048 lds\n"
      "v_mfma_f32_16x16x32_bf16 v[196:199], a[192:195], v[216:219], v[196:199]\n"
      "v_mfma_f32_16x16x32_bf16 v[200:203], a[192:195], v[220:223], v[200:203]\n"
      "buffer_load_dwordx4 v13, s[24:27], s88 offen offset:3072 lds\n"
      "v_mfma_f32_16x16x32_bf16 v[200:203], a[196:199], v[232:235], v[200:203]\n"
      "v_mfma_f32_16x16x32_bf16 v[200:203], a[200:203], a[80:83], v[200:203]\n"
      "s_waitcnt vmcnt(16)\n"
      "buffer_store_dwordx2 v[34:35], v36, s[36:39], s48 offen sc1\n"
      "v_mfma_f32_16x16x32_bf16 v[200:203], a[204:207], a[92:95], v[200:203]\n"
      "s_add_u32 s48, s48, 0x80\n"
      "s_cmp_eq_u32 s48, 0x380\n"
      "s_cselect_b32 s48, 0, s48\n"
      "s_waitcnt lgkmcnt(6)\n"
      "v_mfma_f32_16x16x32_bf16 v[204:207], a[208:211], v[252:255], v[204:207]\n"
      "ds_read_b64_tr_b16 a[192:193], v14 offset:49152\n"
      "s_waitcnt lgkmcnt(5)\n"
      "v_mfma_f32_16x16x32_bf16 v[204:207], a[212:215], v[224:227], v[204:207]\n"
      "ds_read_b64_tr_b16 a[194:195], v14 offset:51200\n"
      "s_waitcnt lgkmcnt(4)\n"
      "v_mfma_f32_16x16x32_bf16 v[204:207], a[216:219], a[72:75], v[204:207]\n"
      "ds_read_b64_tr_b16 a[196:197], v14 offset:53248\n"
      "v_cvt_pk_bf16_f32 v144, v192, v193\n"
      "s_mul_i32 s90, s83, 0x300\n"
      "s_add_u32 s83, s83, 0x80\n"
      "s_cmp_eq_u32 s83, 0x380\n"
      "s_cselect_b32 s83, 0, s83\n"
      "s_waitcnt lgkmcnt(3)\n"
      "v_mfma_f32_16x16x32_bf16 v[204:207], a[220:223], a[84:87], v[204:207]\n"
      "ds_read_b64_tr_b16 a[198:199], v14 offset:55296\n"
      "v_cvt_pk_bf16_f32 v145, v194, v195\n"
      "v_mfma_f32_16x16x32_bf16 v[208:211], a[220:223], a[88:91], v[208:211]\n"
      "ds_read_b64_tr_b16 a[200:201], v26 offset:49152\n"
      "v_cvt_pk_bf16_f32 v148, v196, v197\n"
      "v_mfma_f32_16x16x32_bf16 v[208:211], a[216:219], a[76:79], v[208:211]\n"
      "ds_read_b64_tr_b16 a[202:203], v26 offset:51200\n"
      "v_mfma_f32_16x16x32_bf16 v[208:211], a[212:215], v[228:231], v[208:211]\n"
      "ds_read_b64_tr_b16 a[204:205], v26 offset:53248\n"
      "v_cvt_pk_bf16_f32 v149, v198, v199\n"
      "v_mfma_f32_16x16x32_bf16 v[208:211], a[208:211], v[216:219], v[208:211]\n"
      "ds_read_b64_tr_b16 a[206:207], v26 offset:55296\n"
      "v_cvt_pk_bf16_f32 v152, v200, v201\n"
      "v_mfma_f32_16x16x32_bf16 v[212:215], a[208:211], v[220:223], v[212:215]\n"
      "ds_read_b64_tr_b16 a[208:209], v15 offset:49152\n"
      "v_cvt_pk_bf16_f32 v153, v202, v203\n"
      "v_mfma_f32_16x16x32_bf16 v[212:215], a[212:215], v[232:235], v[212:215]\n"
      "ds_read_b64_tr_b16 a[210:211], v15 offset:51200\n"
      "s_add_u32 s88, s76, s82\n"
      "s_add_u32 m0, s85, 0x18000\n"
      "v_mfma_f32_16x16x32_bf16 v[212:215], a[216:219], a[80:83], v[212:215]\n"
      "ds_read_b64_tr_b16 a[212:213], v15 offset:53248\n"
      "v_cvt_pk_bf16_f32 v146, v204, v205\n"
      "v_cvt_pk_bf16_f32 v147, v206, v207\n"
      "buffer_load_dwordx4 v10, s[24:27], s88 offen lds\n"
      "v_mfma_f32_16x16x32_bf16 v[212:215], a[220:223], a[92:95], v[212:215]\n"
      "ds_read_b64_tr_b16 a[214:215], v15 offset:55296\n"
      "s_waitcnt lgkmcnt(10)\n"
      "v_mfma_f32_16x16x32_bf16 v[192:195], a[192:195], v[60:63], 0\n"
      "ds_read_b64_tr_b16 a[216:217], v27 offset:49152\n"
      "v_cvt_pk_bf16_f32 v150, v208, v209\n"
      "v_cvt_pk_bf16_f32 v151, v210, v211\n"
      "buffer_load_dwordx4 v11, s[24:27], s88 offen offset:1024 lds\n"
      "s_waitcnt lgkmcnt(9)\n"
      "v_mfma_f32_16x16x32_bf16 v[192:195], a[196:199], v[84:87], v[192:195]\n"
      "ds_read_b64_tr_b16 a[218:219], v27 offset:51200\n"
      "s_waitcnt lgkmcnt(8)\n"
      "v_mfma_f32_16x16x32_bf16 v[192:195], a[200:203], a[0:3], v[192:195]\n"
      "ds_read_b64_tr_b16 a[220:221], v27 offset:53248\n"
      "v_cvt_pk_bf16_f32 v154, v212, v213\n"
      "v_cvt_pk_bf16_f32 v155, v214, v215\n"
      "buffer_load_dwordx4 v12, s[24:27], s88 offen offset:2048 lds\n"
      "s_waitcnt lgkmcnt(7)\n"
      "v_mfma_f32_16x16x32_bf16 v[192:195], a[204:207], a[12:15], v[192:195]\n"
      "ds_read_b64_tr_b16 a[222:223], v27 offset:55296\n"
      "v_mfma_f32_16x16x32_bf16 v[196:199], a[204:207], a[16:19], 0\n"
      "buffer_load_dwordx4 v13, s[24:27], s88 offen offset:3072 lds\n"
      "v_mfma_f32_16x16x32_bf16 v[196:199], a[200:203], a[4:7], v[196:199]\n"
      "v_mfma_f32_16x16x32_bf16 v[196:199], a[196:199], v[88:91], v[196:199]\n"
      "buffer_store_dwordx4 v[144:147], v32, s[36:39], s90 offen sc1\n"
      "v_mfma_f32_16x16x32_bf16 v[196:199], a[192:195], v[64:67], v[196:199]\n"
      "v_mfma_f32_16x16x32_bf16 v[200:203], a[192:195], v[68:71], 0\n"
      "buffer_store_dwordx4 v[148:151], v32, s[36:39], s90 offen offset:1024 sc1\n"
      "v_mfma_f32_16x16x32_bf16 v[200:203], a[196:199], v[92:95], v[200:203]\n"
      "v_mfma_f32_16x16x32_bf16 v[200:203], a[200:203], a[8:11], v[200:203]\n"
      "buffer_store_dwordx4 v[152:155], v32, s[36:39], s90 offen offset:2048 sc1\n"
      "v_mfma_f32_16x16x32_bf16 v[200:203], a[204:207], a[20:23], v[200:203]\n"
      "s_waitcnt vmcnt(13)\n"
      "s_barrier\n"
      "s_waitcnt lgkmcnt(6)\n"
      "v_mfma_f32_16x16x32_bf16 v[204:207], a[208:211], v[60:63], 0\n"
      "ds_read_b64_tr_b16 a[192:193], v14\n"
      "s_waitcnt vmcnt(12)\n"
      "v_cmp_eq_u64 vcc, v[54:55], v[34:35]\n"
      "s_waitcnt lgkmcnt(5)\n"
      "v_mfma_f32_16x16x32_bf16 v[204:207], a[212:215], v[84:87], v[204:207]\n"
      "ds_read_b64_tr_b16 a[194:195], v14 offset:2048\n"
      "s_cmp_eq_u32 s99, 0\n"
      "s_cselect_b32 s99, 7, s99\n"
      "s_sub_u32 s99, s99, 1\n"
      "s_lshl_b32 s98, 1, s99\n"
      "s_cmp_eq_u32 s50, 7\n"
      "s_cselect_b32 s98, 0, s98\n"
      "s_waitcnt lgkmcnt(4)\n"
      "v_mfma_f32_16x16x32_bf16 v[204:207], a[216:219], a[0:3], v[204:207]\n"
      "ds_read_b64_tr_b16 a[196:197], v14 offset:4096\n"
      "s_waitcnt lgkmcnt(3)\n"
      "v_mfma_f32_16x16x32_bf16 v[204:207], a[220:223], a[12:15], v[204:207]\n"
      "ds_read_b64_tr_b16 a[198:199], v14 offset:6144\n"
      "s_and_b32 s92, vcc_lo, s98\n"
      "s_cmp_eq_u32 s92, s98\n"
      "s_cbranch_scc0 24f\n"
      "25:\n"
      "v_mfma_f32_16x16x32_bf16 v[208:211], a[220:223], a[16:19], 0\n"
      "ds_read_b64_tr_b16 a[200:201], v26\n"
      "v_mfma_f32_16x16x32_bf16 v[208:211], a[216:219], a[4:7], v[208:211]\n"
      "ds_read_b64_tr_b16 a[202:203], v26 offset:2048\n"
      "v_mfma_f32_16x16x32_bf16 v[208:211], a[212:215], v[88:91], v[208:211]\n"
      "ds_read_b64_tr_b16 a[204:205], v26 offset:4096\n"
      "v_mfma_f32_16x16x32_bf16 v[208:211], a[208:211], v[64:67], v[208:211]\n"
      "ds_read_b64_tr_b16 a[206:207], v26 offset:6144\n"
      "v_mfma_f32_16x16x32_bf16 v[212:215], a[208:211], v[68:71], 0\n"
      "ds_read_b64_tr_b16 a[208:209], v15\n"
      "v_mfma_f32_16x16x32_bf16 v[212:215], a[212:215], v[92:95], v[212:215]\n"
      "ds_read_b64_tr_b16 a[210:211], v15 offset:2048\n"
      "s_add_u32 s88, s77, s82\n"
      "s_add_u32 m0, s85, 0x8000\n"
      "v_mfma_f32_16x16x32_bf16 v[212:215], a[216:219], a[8:11], v[212:215]\n"
      "ds_read_b64_tr_b16 a[212:213], v15 offset:4096\n"
      "buffer_load_dwordx4 v10, s[24:27], s88 offen lds\n"
      "v_mfma_f32_16x16x32_bf16 v[212:215], a[220:223], a[20:23], v[212:215]\n"
      "ds_read_b64_tr_b16 a[214:215], v15 offset:6144\n"
      "s_waitcnt lgkmcnt(10)\n"
      "v_mfma_f32_16x16x32_bf16 v[192:195], a[192:195], v[108:111], v[192:195]\n"
      "ds_read_b64_tr_b16 a[216:217], v27\n"
      "buffer_load_dwordx4 v11, s[24:27], s88 offen offset:1024 lds\n"
      "s_waitcnt lgkmcnt(9)\n"
      "v_mfma_f32_16x16x32_bf16 v[192:195], a[196:199], v[168:171], v[192:195]\n"
      "ds_read_b64_tr_b16 a[218:219], v27 offset:2048\n"
      "s_waitcnt lgkmcnt(8)\n"
      "v_mfma_f32_16x16x32_bf16 v[192:195], a[200:203], a[24:27], v[192:195]\n"
      "ds_read_b64_tr_b16 a[220:221], v27 offset:4096\n"
      "buffer_load_dwordx4 v12, s[24:27], s88 offen offset:2048 lds\n"
      "s_waitcnt lgkmcnt(7)\n"
      "v_mfma_f32_16x16x32_bf16 v[192:195], a[204:207], a[36:39], v[192:195]\n"
      "ds_read_b64_tr_b16 a[222:223], v27 offset:6144\n"
      "v_mfma_f32_16x16x32_bf16 v[196:199], a[204:207], a[40:43], v[196:199]\n"
      "buffer_load_dwordx4 v13, s[24:27], s88 offen offset:3072 lds\n"
      "v_mfma_f32_16x16x32_bf16 v[196:199], a[200:203], a[28:31], v[196:199]\n"
      "s_mul_i32 s89, s99, 0x3000\n"
      "s_add_u32 s89, s89, s51\n"
      "v_mfma_f32_16x16x32_bf16 v[196:199], a[196:199], v[172:175], v[196:199]\n"
      "buffer_load_dwordx4 a[120:123], v33, s[36:39], s89 offen sc1\n"
      "v_mfma_f32_16x16x32_bf16 v[196:199], a[192:195], v[112:115], v[196:199]\n"
      "v_mfma_f32_16x16x32_bf16 v[200:203], a[192:195], v[116:119], v[200:203]\n"
      "buffer_load_dwordx4 a[124:127], v33, s[36:39], s89 offen offset:1024 sc1\n"
      "v_mfma_f32_16x16x32_bf16 v[200:203], a[196:199], v[176:179], v[200:203]\n"
      "v_mfma_f32_16x16x32_bf16 v[200:203], a[200:203], a[32:35], v[200:203]\n"
      "buffer_load_dwordx4 a[128:131], v33, s[36:39], s89 offen offset:2048 sc1\n"
      "v_mfma_f32_16x16x32_bf16 v[200:203], a[204:207], a[44:47], v[200:203]\n"
      "s_waitcnt lgkmcnt(6)\n"
      "v_mfma_f32_16x16x32_bf16 v[204:207], a[208:211], v[108:111], v[204:207]\n"
      "ds_read_b64_tr_b16 a[192:193], v16\n"
      "s_waitcnt lgkmcnt(5)\n"
      "v_mfma_f32_16x16x32_bf16 v[204:207], a[212:215], v[168:171], v[204:207]\n"
      "ds_read_b64_tr_b16 a[194:195], v16 offset:2048\n"
      "s_waitcnt lgkmcnt(4)\n"
      "v_mfma_f32_16x16x32_bf16 v[204:207], a[216:219], a[24:27], v[204:207]\n"
      "ds_read_b64_tr_b16 a[196:197], v16 offset:4096\n"
      "s_waitcnt lgkmcnt(3)\n"
      "v_mfma_f32_16x16x32_bf16 v[204:207], a[220:223], a[36:39], v[204:207]\n"
      "ds_read_b64_tr_b16 a[198:199], v16 offset:6144\n"
      "v_mfma_f32_16x16x32_bf16 v[208:211], a[220:223], a[40:43], v[208:211]\n"
      "ds_read_b64_tr_b16 a[200:201], v28\n"
      "v_mfma_f32_16x16x32_bf16 v[208:211], a[216:219], a[28:31], v[208:211]\n"
      "ds_read_b64_tr_b16 a[202:203], v28 offset:2048\n"
      "v_mfma_f32_16x16x32_bf16 v[208:211], a[212:215], v[172:175], v[208:211]\n"
      "ds_read_b64_tr_b16 a[204:205], v28 offset:4096\n"
      "v_mfma_f32_16x16x32_bf16 v[208:211], a[208:211], v[112:115], v[208:211]\n"
      "ds_read_b64_tr_b16 a[206:207], v28 offset:6144\n"
      "v_mfma_f32_16x16x32_bf16 v[212:215], a[208:211], v[116:119], v[212:215]\n"
      "ds_read_b64_tr_b16 a[208:209], v17\n"
      "v_mfma_f32_16x16x32_bf16 v[212:215], a[212:215], v[176:179], v[212:215]\n"
      "ds_read_b64_tr_b16 a[210:211], v17 offset:2048\n"
      "v_mfma_f32_16x16x32_bf16 v[212:215], a[216:219], a[32:35], v[212:215]\n"
      "ds_read_b64_tr_b16 a[212:213], v17 offset:4096\n"
      "v_mfma_f32_16x16x32_bf16 v[212:215], a[220:223], a[44:47], v[212:215]\n"
      "ds_read_b64_tr_b16 a[214:215], v17 offset:6144\n"
      "s_waitcnt lgkmcnt(10)\n"
      "v_mfma_f32_16x16x32_bf16 v[192:195], a[192:195], v[180:183], v[192:195]\n"
      "ds_read_b64_tr_b16 a[216:217], v29\n"
      "s_waitcnt lgkmcnt(9)\n"
      "v_mfma_f32_16x16x32_bf16 v[192:195], a[196:199], v[240:243], v[192:195]\n"
      "ds_read_b64_tr_b16 a[218:219], v29 offset:2048\n"
      "s_waitcnt lgkmcnt(8)\n"
      "v_mfma_f32_16x16x32_bf16 v[192:195], a[200:203], a[48:51], v[192:195]\n"
      "ds_read_b64_tr_b16 a[220:221], v29 offset:4096\n"
      "s_waitcnt lgkmcnt(7)\n"
      "v_mfma_f32_16x16x32_bf16 v[192:195], a[204:207], a[60:63], v[192:195]\n"
      "ds_read_b64_tr_b16 a[222:223], v29 offset:6144\n"
      "v_mfma_f32_16x16x32_bf16 v[196:199], a[204:207], a[64:67], v[196:199]\n"
      "v_mfma_f32_16x16x32_bf16 v[196:199], a[200:203], a[52:55], v[196:199]\n"
      "v_mfma_f32_16x16x32_bf16 v[196:199], a[196:199], v[244:247], v[196:199]\n"
      "v_mfma_f32_16x16x32_bf16 v[196:199], a[192:195], v[184:187], v[196:199]\n"
      "v_mfma_f32_16x16x32_bf16 v[200:203], a[192:195], v[188:191], v[200:203]\n"
      "v_mfma_f32_16x16x32_bf16 v[200:203], a[196:199], v[248:251], v[200:203]\n"
      "v_mfma_f32_16x16x32_bf16 v[200:203], a[200:203], a[56:59], v[200:203]\n"
      "buffer_load_dwordx2 v[54:55], v37, s[36:39], 0 offen sc1\n"
      "v_mfma_f32_16x16x32_bf16 v[200:203], a[204:207], a[68:71], v[200:203]\n"
      "s_waitcnt lgkmcnt(6)\n"
      "v_mfma_f32_16x16x32_bf16 v[204:207], a[208:211], v[180:183], v[204:207]\n"
      "ds_read_b64_tr_b16 a[192:193], v14 offset:16384\n"
      "s_waitcnt lgkmcnt(5)\n"
      "v_mfma_f32_16x16x32_bf16 v[204:207], a[212:215], v[240:243], v[204:207]\n"
      "ds_read_b64_tr_b16 a[194:195], v14 offset:18432\n"
      "s_waitcnt lgkmcnt(4)\n"
      "v_mfma_f32_16x16x32_bf16 v[204:207], a[216:219], a[48:51], v[204:207]\n"
      "ds_read_b64_tr_b16 a[196:197], v14 offset:20480\n"
      "s_waitcnt lgkmcnt(3)\n"
      "v_mfma_f32_16x16x32_bf16 v[204:207], a[220:223], a[60:63], v[204:207]\n"
      "ds_read_b64_tr_b16 a[198:199], v14 offset:22528\n"
      "v_mfma_f32_16x16x32_bf16 v[208:211], a[220:223], a[64:67], v[208:211]\n"
      "ds_read_b64_tr_b16 a[200:201], v26 offset:16384\n"
      "v_mfma_f32_16x16x32_bf16 v[208:211], a[216:219], a[52:55], v[208:211]\n"
      "ds_read_b64_tr_b16 a[202:203], v26 offset:18432\n"
      "v_mfma_f32_16x16x32_bf16 v[208:211], a[212:215], v[244:247], v[208:211]\n"
      "ds_read_b64_tr_b16 a[204:205], v26 offset:20480\n"
      "v_mfma_f32_16x16x32_bf16 v[208:211], a[208:211], v[184:187], v[208:211]\n"
      "ds_read_b64_tr_b16 a[206:207], v26 offset:22528\n"
      "v_mfma_f32_16x16x32_bf16 v[212:215], a[208:211], v[188:191], v[212:215]\n"
      "ds_read_b64_tr_b16 a[208:209], v15 offset:16384\n"
      "v_mfma_f32_16x16x32_bf16 v[212:215], a[212:215], v[248:251], v[212:215]\n"
      "ds_read_b64_tr_b16 a[210:211], v15 offset:18432\n"
      "v_mfma_f32_16x16x32_bf16 v[212:215], a[216:219], a[56:59], v[212:215]\n"
      "ds_read_b64_tr_b16 a[212:213], v15 offset:20480\n"
      "v_mfma_f32_16x16x32_bf16 v[212:215], a[220:223], a[68:71], v[212:215]\n"
      "ds_read_b64_tr_b16 a[214:215], v15 offset:22528\n"
      "s_waitcnt lgkmcnt(10)\n"
      "v_mfma_f32_16x16x32_bf16 v[192:195], a[192:195], v[252:255], v[192:195]\n"
      "ds_read_b64_tr_b16 a[216:217], v27 offset:16384\n"
      "s_waitcnt lgkmcnt(9)\n"
      "v_mfma_f32_16x16x32_bf16 v[192:195], a[196:199], v[224:227], v[192:195]\n"
      "ds_read_b64_tr_b16 a[218:219], v27 offset:18432\n"
      "s_waitcnt lgkmcnt(8)\n"
      "v_mfma_f32_16x16x32_bf16 v[192:195], a[200:203], a[72:75], v[192:195]\n"
      "ds_read_b64_tr_b16 a[220:221], v27 offset:20480\n"
      "s_waitcnt lgkmcnt(7)\n"
      "v_mfma_f32_16x16x32_bf16 v[192:195], a[204:207], a[84:87], v[192:195]\n"
      "ds_read_b64_tr_b16 a[222:223], v27 offset:22528\n"
      "v_mfma_f32_16x16x32_bf16 v[196:199], a[204:207], a[88:91], v[196:199]\n"
      "v_mfma_f32_16x16x32_bf16 v[196:199], a[200:203], a[76:79], v[196:199]\n"
      "v_mfma_f32_16x16x32_bf16 v[196:199], a[196:199], v[228:231], v[196:199]\n"
      "v_mfma_f32_16x16x32_bf16 v[196:199], a[192:195], v[216:219], v[196:199]\n"
      "v_mfma_f32_16x16x32_bf16 v[200:203], a[192:195], v[220:223], v[200:203]\n"
      "v_mfma_f32_16x16x32_bf16 v[200:203], a[196:199], v[232:235], v[200:203]\n"
      "v_mfma_f32_16x16x32_bf16 v[200:203], a[200:203], a[80:83], v[200:203]\n"
      "s_waitcnt vmcnt(8)\n"
      "buffer_store_dwordx2 v[34:35], v36, s[36:39], s48 offen sc1\n"
      "v_mfma_f32_16x16x32_bf16 v[200:203], a[204:207], a[92:95], v[200:203]\n"
      "s_add_u32 s48, s48, 0x80\n"
      "s_cmp_eq_u32 s48, 0x380\n"
      "s_cselect_b32 s48, 0, s48\n"
      "s_waitcnt lgkmcnt(6)\n"
      "v_mfma_f32_16x16x32_bf16 v[204:207], a[208:211], v[252:255], v[204:207]\n"
      "ds_read_b64_tr_b16 a[192:193], v18 offset:49152\n"
      "s_waitcnt lgkmcnt(5)\n"
      "v_mfma_f32_16x16x32_bf16 v[204:207], a[212:215], v[224:227], v[204:207]\n"
      "ds_read_b64_tr_b16 a[194:195], v18 offset:51200\n"
      "s_waitcnt lgkmcnt(4)\n"
      "v_mfma_f32_16x16x32_bf16 v[204:207], a[216:219], a[72:75], v[204:207]\n"
      "ds_read_b64_tr_b16 a[196:197], v18 offset:53248\n"
      "v_cvt_pk_bf16_f32 v156, v192, v193\n"
      "s_mul_i32 s90, s83, 0x300\n"
      "s_add_u32 s83, s83, 0x80\n"
      "s_cmp_eq_u32 s83, 0x380\n"
      "s_cselect_b32 s83, 0, s83\n"
      "s_waitcnt lgkmcnt(3)\n"
      "v_mfma_f32_16x16x32_bf16 v[204:207], a[220:223], a[84:87], v[204:207]\n"
      "ds_read_b64_tr_b16 a[198:199], v18 offset:55296\n"
      "v_cvt_pk_bf16_f32 v157, v194, v195\n"
      "v_mfma_f32_16x16x32_bf16 v[208:211], a[220:223], a[88:91], v[208:211]\n"
      "ds_read_b64_tr_b16 a[200:201], v24 offset:49152\n"
      "v_cvt_pk_bf16_f32 v160, v196, v197\n"
      "v_mfma_f32_16x16x32_bf16 v[208:211], a[216:219], a[76:79], v[208:211]\n"
      "ds_read_b64_tr_b16 a[202:203], v24 offset:51200\n"
      "v_mfma_f32_16x16x32_bf16 v[208:211], a[212:215], v[228:231], v[208:211]\n"
      "ds_read_b64_tr_b16 a[204:205], v24 offset:53248\n"
      "v_cvt_pk_bf16_f32 v161, v198, v199\n"
      "v_mfma_f32_16x16x32_bf16 v[208:211], a[208:211], v[216:219], v[208:211]\n"
      "ds_read_b64_tr_b16 a[206:207], v24 offset:55296\n"
      "v_cvt_pk_bf16_f32 v164, v200, v201\n"
      "v_mfma_f32_16x16x32_bf16 v[212:215], a[208:211], v[220:223], v[212:215]\n"
      "ds_read_b64_tr_b16 a[208:209], v19 offset:49152\n"
      "v_cvt_pk_bf16_f32 v165, v202, v203\n"
      "v_mfma_f32_16x16x32_bf16 v[212:215], a[212:215], v[232:235], v[212:215]\n"
      "ds_read_b64_tr_b16 a[210:211], v19 offset:51200\n"
      "v_mfma_f32_16x16x32_bf16 v[212:215], a[216:219], a[80:83], v[212:215]\n"
      "ds_read_b64_tr_b16 a[212:213], v19 offset:53248\n"
      "v_cvt_pk_bf16_f32 v158, v204, v205\n"
      "v_cvt_pk_bf16_f32 v159, v206, v207\n"
      "v_mfma_f32_16x16x32_bf16 v[212:215], a[220:223], a[92:95], v[212:215]\n"
      "ds_read_b64_tr_b16 a[214:215], v19 offset:55296\n"
      "s_waitcnt lgkmcnt(10)\n"
      "v_mfma_f32_16x16x32_bf16 v[120:123], a[192:195], v[60:63], v[120:123]\n"
      "ds_read_b64_tr_b16 a[216:217], v25 offset:49152\n"
      "v_cvt_pk_bf16_f32 v162, v208, v209\n"
      "v_cvt_pk_bf16_f32 v163, v210, v211\n"
      "s_waitcnt lgkmcnt(9)\n"
      "v_mfma_f32_16x16x32_bf16 v[120:123], a[196:199], v[84:87], v[120:123]\n"
      "ds_read_b64_tr_b16 a[218:219], v25 offset:51200\n"
      "s_waitcnt lgkmcnt(8)\n"
      "v_mfma_f32_16x16x32_bf16 v[120:123], a[200:203], a[0:3], v[120:123]\n"
      "ds_read_b64_tr_b16 a[220:221], v25 offset:53248\n"
      "v_cvt_pk_bf16_f32 v166, v212, v213\n"
      "v_cvt_pk_bf16_f32 v167, v214, v215\n"
      "s_waitcnt lgkmcnt(7)\n"
      "v_mfma_f32_16x16x32_bf16 v[120:123], a[204:207], a[12:15], v[120:123]\n"
      "ds_read_b64_tr_b16 a[222:223], v25 offset:55296\n"
      "v_mfma_f32_16x16x32_bf16 v[124:127], a[204:207], a[16:19], v[124:127]\n"
      "v_mfma_f32_16x16x32_bf16 v[124:127], a[200:203], a[4:7], v[124:127]\n"
      "v_mfma_f32_16x16x32_bf16 v[124:127], a[196:199], v[88:91], v[124:127]\n"
      "buffer_store_dwordx4 v[156:159], v32, s[36:39], s90 offen sc1\n"
      "v_mfma_f32_16x16x32_bf16 v[124:127], a[192:195], v[64:67], v[124:127]\n"
      "v_mfma_f32_16x16x32_bf16 v[128:131], a[192:195], v[68:71], v[128:131]\n"
      "buffer_store_dwordx4 v[160:163], v32, s[36:39], s90 offen offset:1024 sc1\n"
      "v_mfma_f32_16x16x32_bf16 v[128:131], a[196:199], v[92:95], v[128:131]\n"
      "v_mfma_f32_16x16x32_bf16 v[128:131], a[200:203], a[8:11], v[128:131]\n"
      "buffer_store_dwordx4 v[164:167], v32, s[36:39], s90 offen offset:2048 sc1\n"
      "v_mfma_f32_16x16x32_bf16 v[128:131], a[204:207], a[20:23], v[128:131]\n"
      "s_waitcnt vmcnt(8)\n"
      "s_barrier\n"
      "s_waitcnt lgkmcnt(6)\n"
      "v_mfma_f32_16x16x32_bf16 v[132:135], a[208:211], v[60:63], v[132:135]\n"
      "ds_read_b64_tr_b16 a[192:193], v16 offset:16384\n"
      "s_waitcnt vmcnt(4)\n"
      "v_cmp_eq_u64 vcc, v[54:55], v[34:35]\n"
      "s_waitcnt lgkmcnt(5)\n"
      "v_mfma_f32_16x16x32_bf16 v[132:135], a[212:215], v[84:87], v[132:135]\n"
      "ds_read_b64_tr_b16 a[194:195], v16 offset:18432\n"
      "s_cmp_eq_u32 s99, 0\n"
      "s_cselect_b32 s99, 7, s99\n"
      "s_sub_u32 s99, s99, 1\n"
      "s_lshl_b32 s98, 1, s99\n"
      "s_cmp_eq_u32 s50, 7\n"
      "s_cselect_b32 s98, 0, s98\n"
      "s_or_b32 s98, s98, s40\n"
      "s_waitcnt lgkmcnt(4)\n"
      "v_mfma_f32_16x16x32_bf16 v[132:135], a[216:219], a[0:3], v[132:135]\n"
      "ds_read_b64_tr_b16 a[196:197], v16 offset:20480\n"
      "s_waitcnt lgkmcnt(3)\n"
      "v_mfma_f32_16x16x32_bf16 v[132:135], a[220:223], a[12:15], v[132:135]\n"
      "ds_read_b64_tr_b16 a[198:199], v16 offset:22528\n"
      "s_and_b32 s92, vcc_lo, s98\n"
      "s_cmp_eq_u32 s92, s98\n"
      "s_cbranch_scc0 26f\n"
      "27:\n"
      "v_mfma_f32_16x16x32_bf16 v[136:139], a[220:223], a[16:19], v[136:139]\n"
      "ds_read_b64_tr_b16 a[200:201], v28 offset:16384\n"
      "v_mfma_f32_16x16x32_bf16 v[136:139], a[216:219], a[4:7], v[136:139]\n"
      "ds_read_b64_tr_b16 a[202:203], v28 offset:18432\n"
      "v_mfma_f32_16x16x32_bf16 v[136:139], a[212:215], v[88:91], v[136:139]\n"
      "ds_read_b64_tr_b16 a[204:205], v28 offset:20480\n"
      "v_mfma_f32_16x16x32_bf16 v[136:139], a[208:211], v[64:67], v[136:139]\n"
      "ds_read_b64_tr_b16 a[206:207], v28 offset:22528\n"
      "v_mfma_f32_16x16x32_bf16 v[140:143], a[208:211], v[68:71], v[140:143]\n"
      "ds_read_b64_tr_b16 a[208:209], v17 offset:16384\n"
      "v_mfma_f32_16x16x32_bf16 v[140:143], a[212:215], v[92:95], v[140:143]\n"
      "ds_read_b64_tr_b16 a[210:211], v17 offset:18432\n"
      "v_mfma_f32_16x16x32_bf16 v[140:143], a[216:219], a[8:11], v[140:143]\n"
      "ds_read_b64_tr_b16 a[212:213], v17 offset:20480\n"
      "v_mfma_f32_16x16x32_bf16 v[140:143], a[220:223], a[20:23], v[140:143]\n"
      "ds_read_b64_tr_b16 a[214:215], v17 offset:22528\n"
      "v_mfma_f32_16x16x32_bf16 v[120:123], v[50:53], a[108:111], v[120:123]\n"
      "ds_read_b64_tr_b16 a[216:217], v29 offset:16384\n"
      "v_mfma_f32_16x16x32_bf16 v[132:135], v[48:51], a[108:111], v[132:135]\n"
      "ds_read_b64_tr_b16 a[218:219], v29 offset:18432\n"
      "v_mfma_f32_16x16x32_bf16 v[124:127], v[50:53], a[112:115], v[124:127]\n"
      "ds_read_b64_tr_b16 a[220:221], v29 offset:20480\n"
      "v_mfma_f32_16x16x32_bf16 v[136:139], v[48:51], a[112:115], v[136:139]\n"
      "ds_read_b64_tr_b16 a[222:223], v29 offset:22528\n"
      "s_mul_i32 s89, s99, 0x3000\n"
      "s_add_u32 s89, s89, s51\n"
      "v_mfma_f32_16x16x32_bf16 v[128:131], v[50:53], a[116:119], v[128:131]\n"
      "buffer_load_dwordx4 a[132:135], v33, s[36:39], s89 offen sc1\n"
      "v_mfma_f32_16x16x32_bf16 v[140:143], v[48:51], a[116:119], v[140:143]\n"
      "s_waitcnt lgkmcnt(14)\n"
      "v_mfma_f32_16x16x32_bf16 v[120:123], a[192:195], v[108:111], v[120:123]\n"
      "buffer_load_dwordx4 a[136:139], v33, s[36:39], s89 offen offset:1024 sc1\n"
      "s_waitcnt lgkmcnt(12)\n"
      "v_mfma_f32_16x16x32_bf16 v[120:123], a[196:199], v[168:171], v[120:123]\n"
      "s_waitcnt lgkmcnt(10)\n"
      "v_mfma_f32_16x16x32_bf16 v[120:123], a[200:203], a[24:27], v[120:123]\n"
      "buffer_load_dwordx4 a[140:143], v33, s[36:39], s89 offen offset:2048 sc1\n"
      "s_waitcnt lgkmcnt(8)\n"
      "v_mfma_f32_16x16x32_bf16 v[120:123], a[204:207], a[36:39], v[120:123]\n"
      "s_mov_b32 s89, s41\n"
      "v_mfma_f32_16x16x32_bf16 v[124:127], a[204:207], a[40:43], v[124:127]\n"
      "buffer_load_dwordx4 a[156:159], v33, s[36:39], s89 offen sc1\n"
      "v_mfma_f32_16x16x32_bf16 v[124:127], a[200:203], a[28:31], v[124:127]\n"
      "v_mfma_f32_16x16x32_bf16 v[124:127], a[196:199], v[172:175], v[124:127]\n"
      "buffer_load_dwordx4 a[160:163], v33, s[36:39], s89 offen offset:1024 sc1\n"
      "v_mfma_f32_16x16x32_bf16 v[124:127], a[192:195], v[112:115], v[124:127]\n"
      "v_mfma_f32_16x16x32_bf16 v[128:131], a[192:195], v[116:119], v[128:131]\n"
      "buffer_load_dwordx4 a[164:167], v33, s[36:39], s89 offen offset:2048 sc1\n"
      "v_mfma_f32_16x16x32_bf16 v[128:131], a[196:199], v[176:179], v[128:131]\n"
      "v_mfma_f32_16x16x32_bf16 v[128:131], a[200:203], a[32:35], v[128:131]\n"
      "buffer_load_dwordx2 v[54:55], v37, s[36:39], 0 offen sc1\n"
      "v_mfma_f32_16x16x32_bf16 v[128:131], a[204:207], a[44:47], v[128:131]\n"
      "s_waitcnt lgkmcnt(6)\n"
      "v_mfma_f32_16x16x32_bf16 v[132:135], a[208:211], v[108:111], v[132:135]\n"
      "ds_read_b64_tr_b16 a[192:193], v14 offset:32768\n"
      "s_waitcnt lgkmcnt(5)\n"
      "v_mfma_f32_16x16x32_bf16 v[132:135], a[212:215], v[168:171], v[132:135]\n"
      "ds_read_b64_tr_b16 a[194:195], v14 offset:34816\n"
      "s_waitcnt lgkmcnt(4)\n"
      "v_mfma_f32_16x16x32_bf16 v[132:135], a[216:219], a[24:27], v[132:135]\n"
      "ds_read_b64_tr_b16 a[196:197], v14 offset:36864\n"
      "s_waitcnt lgkmcnt(3)\n"
      "v_mfma_f32_16x16x32_bf16 v[132:135], a[220:223], a[36:39], v[132:135]\n"
      "ds_read_b64_tr_b16 a[198:199], v14 offset:38912\n"
      "v_mfma_f32_16x16x32_bf16 v[136:139], a[220:223], a[40:43], v[136:139]\n"
      "ds_read_b64_tr_b16 a[200:201], v26 offset:32768\n"
      "v_mfma_f32_16x16x32_bf16 v[136:139], a[216:219], a[28:31], v[136:139]\n"
      "ds_read_b64_tr_b16 a[202:203], v26 offset:34816\n"
      "v_mfma_f32_16x16x32_bf16 v[136:139], a[212:215], v[172:175], v[136:139]\n"
      "ds_read_b64_tr_b16 a[204:205], v26 offset:36864\n"
      "v_mfma_f32_16x16x32_bf16 v[136:139], a[208:211], v[112:115], v[136:139]\n"
      "ds_read_b64_tr_b16 a[206:207], v26 offset:38912\n"
      "v_mfma_f32_16x16x32_bf16 v[140:143], a[208:211], v[116:119], v[140:143]\n"
      "ds_read_b64_tr_b16 a[208:209], v15 offset:32768\n"
      "v_mfma_f32_16x16x32_bf16 v[140:143], a[212:215], v[176:179], v[140:143]\n"
      "ds_read_b64_tr_b16 a[210:211], v15 offset:34816\n"
      "v_mfma_f32_16x16x32_bf16 v[140:143], a[216:219], a[32:35], v[140:143]\n"
      "ds_read_b64_tr_b16 a[212:213], v15 offset:36864\n"
      "v_mfma_f32_16x16x32_bf16 v[140:143], a[220:223], a[44:47], v[140:143]\n"
      "ds_read_b64_tr_b16 a[214:215], v15 offset:38912\n"
      "v_mfma_f32_16x16x32_bf16 v[120:123], v[50:53], a[120:123], v[120:123]\n"
      "ds_read_b64_tr_b16 a[216:217], v27 offset:32768\n"
      "v_mfma_f32_16x16x32_bf16 v[132:135], v[48:51], a[120:123], v[132:135]\n"
      "ds_read_b64_tr_b16 a[218:219], v27 offset:34816\n"
      "v_mfma_f32_16x16x32_bf16 v[124:127], v[50:53], a[124:127], v[124:127]\n"
      "ds_read_b64_tr_b16 a[220:221], v27 offset:36864\n"
      "v_mfma_f32_16x16x32_bf16 v[136:139], v[48:51], a[124:127], v[136:139]\n"
      "ds_read_b64_tr_b16 a[222:223], v27 offset:38912\n"
      "v_mfma_f32_16x16x32_bf16 v[128:131], v[50:53], a[128:131], v[128:131]\n"
      "v_mfma_f32_16x16x32_bf16 v[140:143], v[48:51], a[128:131], v[140:143]\n"
      "s_waitcnt lgkmcnt(14)\n"
      "v_mfma_f32_16x16x32_bf16 v[120:123], a[192:195], v[180:183], v[120:123]\n"
      "s_waitcnt lgkmcnt(12)\n"
      "v_mfma_f32_16x16x32_bf16 v[120:123], a[196:199], v[240:243], v[120:123]\n"
      "s_waitcnt lgkmcnt(10)\n"
      "v_mfma_f32_16x16x32_bf16 v[120:123], a[200:203], a[48:51], v[120:123]\n"
      "s_waitcnt lgkmcnt(8)\n"
      "v_mfma_f32_16x16x32_bf16 v[120:123], a[204:207], a[60:63], v[120:123]\n"
      "v_mfma_f32_16x16x32_bf16 v[124:127], a[204:207], a[64:67], v[124:127]\n"
      "v_mfma_f32_16x16x32_bf16 v[124:127], a[200:203], a[52:55], v[124:127]\n"
      "v_mfma_f32_16x16x32_bf16 v[124:127], a[196:199], v[244:247], v[124:127]\n"
      "v_mfma_f32_16x16x32_bf16 v[124:127], a[192:195], v[184:187], v[124:127]\n"
      "v_mfma_f32_16x16x32_bf16 v[128:131], a[192:195], v[188:191], v[128:131]\n"
      "v_mfma_f32_16x16x32_bf16 v[128:131], a[196:199], v[248:251], v[128:131]\n"
      "v_mfma_f32_16x16x32_bf16 v[128:131], a[200:203], a[56:59], v[128:131]\n"
      "s_waitcnt vmcnt(7)\n"
      "buffer_store_dwordx2 v[34:35], v36, s[36:39], s48 offen sc1\n"
      "v_mfma_f32_16x16x32_bf16 v[128:131], a[204:207], a[68:71], v[128:131]\n"
      "s_add_u32 s48, s48, 0x80\n"
      "s_cmp_eq_u32 s48, 0x380\n"
      "s_cselect_b32 s48, 0, s48\n"
      "s_waitcnt lgkmcnt(6)\n"
      "v_mfma_f32_16x16x32_bf16 v[132:135], a[208:211], v[180:183], v[132:135]\n"
      "ds_read_b64_tr_b16 a[192:193], v18 offset:32768\n"
      "s_waitcnt vmcnt(1)\n"
      "v_cmp_eq_u64 vcc, v[54:55], v[34:35]\n"
      "s_waitcnt lgkmcnt(5)\n"
      "v_mfma_f32_16x16x32_bf16 v[132:135], a[212:215], v[240:243], v[132:135]\n"
      "ds_read_b64_tr_b16 a[194:195], v18 offset:34816\n"
      "s_cmp_eq_u32 s99, 0\n"
      "s_cselect_b32 s99, 7, s99\n"
      "s_sub_u32 s99, s99, 1\n"
      "s_lshl_b32 s98, 1, s99\n"
      "s_cmp_eq_u32 s50, 7\n"
      "s_cselect_b32 s98, 0, s98\n"
      "s_waitcnt lgkmcnt(4)\n"
      "v_mfma_f32_16x16x32_bf16 v[132:135], a[216:219], a[48:51], v[132:135]\n"
      "ds_read_b64_tr_b16 a[196:197], v18 offset:36864\n"
      "s_waitcnt lgkmcnt(3)\n"
      "v_mfma_f32_16x16x32_bf16 v[132:135], a[220:223], a[60:63], v[132:135]\n"
      "ds_read_b64_tr_b16 a[198:199], v18 offset:38912\n"
      "s_and_b32 s92, vcc_lo, s98\n"
      "s_cmp_eq_u32 s92, s98\n"
      "s_cbranch_scc0 28f\n"
      "29:\n"
      "v_mfma_f32_16x16x32_bf16 v[136:139], a[220:223], a[64:67], v[136:139]\n"
      "ds_read_b64_tr_b16 a[200:201], v24 offset:32768\n"
      "v_mfma_f32_16x16x32_bf16 v[136:139], a[216:219], a[52:55], v[136:139]\n"
      "ds_read_b64_tr_b16 a[202:203], v24 offset:34816\n"
      "s_mul_i32 s89, s99, 0x3000\n"
      "s_add_u32 s89, s89, s51\n"
      "v_mfma_f32_16x16x32_bf16 v[136:139], a[212:215], v[244:247], v[136:139]\n"
      "ds_read_b64_tr_b16 a[204:205], v24 offset:36864\n"
      "buffer_load_dwordx4 a[144:147], v33, s[36:39], s89 offen sc1\n"
      "v_mfma_f32_16x16x32_bf16 v[136:139], a[208:211], v[184:187], v[136:139]\n"
      "ds_read_b64_tr_b16 a[206:207], v24 offset:38912\n"
      "v_mfma_f32_16x16x32_bf16 v[140:143], a[208:211], v[188:191], v[140:143]\n"
      "ds_read_b64_tr_b16 a[208:209], v19 offset:32768\n"
      "buffer_load_dwordx4 a[148:151], v33, s[36:39], s89 offen offset:1024 sc1\n"
      "v_mfma_f32_16x16x32_bf16 v[140:143], a[212:215], v[248:251], v[140:143]\n"
      "ds_read_b64_tr_b16 a[210:211], v19 offset:34816\n"
      "v_mfma_f32_16x16x32_bf16 v[140:143], a[216:219], a[56:59], v[140:143]\n"
      "ds_read_b64_tr_b16 a[212:213], v19 offset:36864\n"
      "buffer_load_dwordx4 a[152:155], v33, s[36:39], s89 offen offset:2048 sc1\n"
      "v_mfma_f32_16x16x32_bf16 v[140:143], a[220:223], a[68:71], v[140:143]\n"
      "ds_read_b64_tr_b16 a[214:215], v19 offset:38912\n"
      "s_cmp_eq_u32 s99, 0\n"
      "s_cselect_b32 s99, 7, s99\n"
      "s_sub_u32 s99, s99, 1\n"
      "s_lshl_b32 s98, 1, s99\n"
      "s_cmp_eq_u32 s50, 0\n"
      "s_cselect_b32 s88, s99, 7\n"
      "s_cselect_b32 s91, 7, s99\n"
      "s_lshl_b32 s93, 1, s88\n"
      "s_lshl_b32 s98, 1, s91\n"
      "s_or_b32 s94, s93, s98\n"
      "s_and_b32 s92, vcc_lo, s94\n"
      "s_cmp_eq_u32 s92, s94\n"
      "s_cselect_b32 s94, 1, 0\n"
      "s_cbranch_scc0 16f\n"
      "s_mul_i32 s89, s88, 0x3000\n"
      "s_add_u32 s89, s89, s51\n"
      "s_cmp_eq_u32 s40, 0\n"
      "s_cselect_b32 s89, s89, 0x40000000\n"
      "buffer_load_dwordx4 a[96:99], v33, s[36:39], s89 offen sc1\n"
      "buffer_load_dwordx4 a[100:103], v33, s[36:39], s89 offen offset:1024 sc1\n"
      "buffer_load_dwordx4 a[104:107], v33, s[36:39], s89 offen offset:2048 sc1\n"
      "s_mul_i32 s89, s91, 0x3000\n"
      "s_add_u32 s89, s89, s51\n"
      "buffer_load_dwordx4 a[168:171], v33, s[36:39], s89 offen sc1\n"
      "buffer_load_dwordx4 a[172:175], v33, s[36:39], s89 offen offset:1024 sc1\n"
      "buffer_load_dwordx4 a[176:179], v33, s[36:39], s89 offen offset:2048 sc1\n"
      "16:\n"
      "s_waitcnt lgkmcnt(10)\n"
      "v_mfma_f32_16x16x32_bf16 v[120:123], a[192:195], v[252:255], v[120:123]\n"
      "ds_read_b64_tr_b16 a[216:217], v25 offset:32768\n"
      "s_waitcnt lgkmcnt(9)\n"
      "v_mfma_f32_16x16x32_bf16 v[120:123], a[196:199], v[224:227], v[120:123]\n"
      "ds_read_b64_tr_b16 a[218:219], v25 offset:34816\n"
      "s_waitcnt lgkmcnt(8)\n"
      "v_mfma_f32_16x16x32_bf16 v[120:123], a[200:203], a[72:75], v[120:123]\n"
      "ds_read_b64_tr_b16 a[220:221], v25 offset:36864\n"
      "s_waitcnt lgkmcnt(7)\n"
      "v_mfma_f32_16x16x32_bf16 v[120:123], a[204:207], a[84:87], v[120:123]\n"
      "ds_read_b64_tr_b16 a[222:223], v25 offset:38912\n"
      "v_mfma_f32_16x16x32_bf16 v[124:127], a[204:207], a[88:91], v[124:127]\n"
      "v_mfma_f32_16x16x32_bf16 v[124:127], a[200:203], a[76:79], v[124:127]\n"
      "v_mfma_f32_16x16x32_bf16 v[124:127], a[196:199], v[228:231], v[124:127]\n"
      "v_mfma_f32_16x16x32_bf16 v[124:127], a[192:195], v[216:219], v[124:127]\n"
      "v_mfma_f32_16x16x32_bf16 v[128:131], a[192:195], v[220:223], v[128:131]\n"
      "v_mfma_f32_16x16x32_bf16 v[128:131], a[196:199], v[232:235], v[128:131]\n"
      "v_mfma_f32_16x16x32_bf16 v[128:131], a[200:203], a[80:83], v[128:131]\n"
      "v_mfma_f32_16x16x32_bf16 v[128:131], a[204:207], a[92:95], v[128:131]\n"
      "s_waitcnt lgkmcnt(6)\n"
      "v_mfma_f32_16x16x32_bf16 v[132:135], a[208:211], v[252:255], v[132:135]\n"
      "s_waitcnt lgkmcnt(4)\n"
      "v_mfma_f32_16x16x32_bf16 v[132:135], a[212:215], v[224:227], v[132:135]\n"
      "s_waitcnt lgkmcnt(2)\n"
      "v_mfma_f32_16x16x32_bf16 v[132:135], a[216:219], a[72:75], v[132:135]\n"
      "s_waitcnt lgkmcnt(0)\n"
      "v_mfma_f32_16x16x32_bf16 v[132:135], a[220:223], a[84:87], v[132:135]\n"
      "v_mfma_f32_16x16x32_bf16 v[136:139], a[220:223], a[88:91], v[136:139]\n"
      "v_mfma_f32_16x16x32_bf16 v[136:139], a[216:219], a[76:79], v[136:139]\n"
      "v_mfma_f32_16x16x32_bf16 v[136:139], a[212:215], v[228:231], v[136:139]\n"
      "v_mfma_f32_16x16x32_bf16 v[136:139], a[208:211], v[216:219], v[136:139]\n"
      "v_mfma_f32_16x16x32_bf16 v[140:143], a[208:211], v[220:223], v[140:143]\n"
      "v_mfma_f32_16x16x32_bf16 v[140:143], a[212:215], v[232:235], v[140:143]\n"
      "buffer_load_dwordx2 v[54:55], v37, s[36:39], 0 offen sc1\n"
      "v_mfma_f32_16x16x32_bf16 v[140:143], a[216:219], a[80:83], v[140:143]\n"
      "v_mfma_f32_16x16x32_bf16 v[140:143], a[220:223], a[92:95], v[140:143]\n"
      "v_mfma_f32_16x16x32_bf16 v[120:123], v[50:53], a[132:135], v[120:123]\n"
      "v_mfma_f32_16x16x32_bf16 v[132:135], v[48:51], a[132:135], v[132:135]\n"
      "v_mfma_f32_16x16x32_bf16 v[124:127], v[50:53], a[136:139], v[124:127]\n"
      "v_mfma_f32_16x16x32_bf16 v[136:139], v[48:51], a[136:139], v[136:139]\n"
      "v_mfma_f32_16x16x32_bf16 v[128:131], v[50:53], a[140:143], v[128:131]\n"
      "v_mfma_f32_16x16x32_bf16 v[140:143], v[48:51], a[140:143], v[140:143]\n"
      "v_mfma_f32_16x16x32_bf16 v[120:123], v[50:53], a[156:159], v[120:123]\n"
      "v_mfma_f32_16x16x32_bf16 v[132:135], v[48:51], a[156:159], v[132:135]\n"
      "v_mfma_f32_16x16x32_bf16 v[124:127], v[50:53], a[160:163], v[124:127]\n"
      "v_mfma_f32_16x16x32_bf16 v[136:139], v[48:51], a[160:163], v[136:139]\n"
      "v_mfma_f32_16x16x32_bf16 v[128:131], v[50:53], a[164:167], v[128:131]\n"
      "v_mfma_f32_16x16x32_bf16 v[140:143], v[48:51], a[164:167], v[140:143]\n"
      "s_waitcnt vmcnt(1)\n"
      "s_cmp_eq_u32 s94, 1\n"
      "s_cbranch_scc1 17f\n"
      "s_waitcnt vmcnt(0)\n"
      "s_cmp_eq_u32 s40, 0\n"
      "s_cbranch_scc0 14f\n"
      "v_cmp_eq_u64 vcc, v[54:55], v[34:35]\n"
      "s_nop 4\n"
      "s_and_b32 s92, vcc_lo, s93\n"
      "s_cmp_eq_u32 s92, s93\n"
      "s_cbranch_scc0 30f\n"
      "31:\n"
      "s_mul_i32 s89, s88, 0x3000\n"
      "s_add_u32 s89, s89, s51\n"
      "buffer_load_dwordx4 a[96:99], v33, s[36:39], s89 offen sc1\n"
      "buffer_load_dwordx4 a[100:103], v33, s[36:39], s89 offen offset:1024 sc1\n"
      "buffer_load_dwordx4 a[104:107], v33, s[36:39], s89 offen offset:2048 sc1\n"
      "14:\n"
      "v_cmp_eq_u64 vcc, v[54:55], v[34:35]\n"
      "s_nop 4\n"
      "s_and_b32 s92, vcc_lo, s98\n"
      "s_cmp_eq_u32 s92, s98\n"
      "s_cbranch_scc0 32f\n"
      "33:\n"
      "s_mul_i32 s89, s91, 0x3000\n"
      "s_add_u32 s89, s89, s51\n"
      "buffer_load_dwordx4 a[168:171], v33, s[36:39], s89 offen sc1\n"
      "buffer_load_dwordx4 a[172:175], v33, s[36:39], s89 offen offset:1024 sc1\n"
      "buffer_load_dwordx4 a[176:179], v33, s[36:39], s89 offen offset:2048 sc1\n"
      "17:\n"
      "v_mfma_f32_16x16x32_bf16 v[120:123], v[50:53], a[144:147], v[120:123]\n"
      "v_mfma_f32_16x16x32_bf16 v[132:135], v[48:51], a[144:147], v[132:135]\n"
      "v_mfma_f32_16x16x32_bf16 v[124:127], v[50:53], a[148:151], v[124:127]\n"
      "v_mfma_f32_16x16x32_bf16 v[136:139], v[48:51], a[148:151], v[136:139]\n"
      "v_mfma_f32_16x16x32_bf16 v[128:131], v[50:53], a[152:155], v[128:131]\n"
      "v_mfma_f32_16x16x32_bf16 v[140:143], v[48:51], a[152:155], v[140:143]\n"
      "s_cmp_eq_u32 s40, 0\n"
      "s_cbranch_scc0 15f\n"
      "s_waitcnt vmcnt(5)\n"
      "v_mfma_f32_16x16x32_bf16 v[120:123], v[50:53], a[96:99], v[120:123]\n"
      "v_mfma_f32_16x16x32_bf16 v[132:135], v[48:51], a[96:99], v[132:135]\n"
      "s_waitcnt vmcnt(4)\n"
      "v_mfma_f32_16x16x32_bf16 v[124:127], v[50:53], a[100:103], v[124:127]\n"
      "v_mfma_f32_16x16x32_bf16 v[136:139], v[48:51], a[100:103], v[136:139]\n"
      "s_waitcnt vmcnt(3)\n"
      "v_mfma_f32_16x16x32_bf16 v[128:131], v[50:53], a[104:107], v[128:131]\n"
      "v_mfma_f32_16x16x32_bf16 v[140:143], v[48:51], a[104:107], v[140:143]\n"
      "15:\n"
      "s_waitcnt vmcnt(2)\n"
      "v_mfma_f32_16x16x32_bf16 v[120:123], v[50:53], a[168:171], v[120:123]\n"
      "v_mfma_f32_16x16x32_bf16 v[132:135], v[48:51], a[168:171], v[132:135]\n"
      "s_waitcnt vmcnt(1)\n"
      "v_mfma_f32_16x16x32_bf16 v[124:127], v[50:53], a[172:175], v[124:127]\n"
      "v_mfma_f32_16x16x32_bf16 v[136:139], v[48:51], a[172:175], v[136:139]\n"
      "s_waitcnt vmcnt(0)\n"
      "v_mfma_f32_16x16x32_bf16 v[128:131], v[50:53], a[176:179], v[128:131]\n"
      "v_mfma_f32_16x16x32_bf16 v[140:143], v[48:51], a[176:179], v[140:143]\n"
      "s_nop 0\n"
      "v_cvt_pk_bf16_f32 v144, v120, v121\n"
      "v_cvt_pk_bf16_f32 v145, v122, v123\n"
      "v_cvt_pk_bf16_f32 v146, v132, v133\n"
      "v_cvt_pk_bf16_f32 v147, v134, v135\n"
      "s_nop 0\n"
      "v_permlane16_swap_b32 v144, v146\n"
      "v_permlane16_swap_b32 v145, v147\n"
      "ds_write_b128 v46, v[144:147]\n"
      "v_cvt_pk_bf16_f32 v148, v124, v125\n"
      "v_cvt_pk_bf16_f32 v149, v126, v127\n"
      "v_cvt_pk_bf16_f32 v150, v136, v137\n"
      "v_cvt_pk_bf16_f32 v151, v138, v139\n"
      "s_nop 0\n"
      "v_permlane16_swap_b32 v148, v150\n"
      "v_permlane16_swap_b32 v149, v151\n"
      "ds_write_b128 v46, v[148:151] offset:2048\n"
      "v_cvt_pk_bf16_f32 v152, v128, v129\n"
      "v_cvt_pk_bf16_f32 v153, v130, v131\n"
      "v_cvt_pk_bf16_f32 v154, v140, v141\n"
      "v_cvt_pk_bf16_f32 v155, v142, v143\n"
      "s_nop 0\n"
      "v_permlane16_swap_b32 v152, v154\n"
      "v_permlane16_swap_b32 v153, v155\n"
      "ds_write_b128 v46, v[152:155] offset:4096\n"
      "s_waitcnt lgkmcnt(0)\n"
      "s_barrier\n"
      "ds_read_b128 v[156:159], v47\n"
      "ds_read_b128 v[160:163], v47 offset:1024\n"
      "ds_read_b128 v[164:167], v47 offset:2048\n"
      "s_waitcnt lgkmcnt(2)\n"
      "buffer_store_dwordx4 v[156:159], v38, s[28:31], 0 offen sc1\n"
      "v_add_u32 v38, 0x3800, v38\n"
      "s_waitcnt lgkmcnt(1)\n"
      "buffer_store_dwordx4 v[160:163], v38, s[28:31], 0 offen sc1\n"
      "v_add_u32 v38, 0x3800, v38\n"
      "s_waitcnt lgkmcnt(0)\n"
      "buffer_store_dwordx4 v[164:167], v38, s[28:31], 0 offen sc1\n"
      "s_cmp_eq_u32 s50, 0\n"
      "s_cbranch_scc0 8f\n"
      "buffer_store_dwordx2 v[34:35], v36, s[36:39], 0 offen offset:896 sc1\n"
      "s_branch 8f\n"
      "7:\n"
      "s_barrier\n"
      "s_waitcnt lgkmcnt(13)\n"
      "v_mfma_f32_16x16x32_bf16 v[204:207], a[208:211], v[60:63], 0\n"
      "ds_read_b64_tr_b16 a[192:193], v14\n"
      "s_waitcnt lgkmcnt(12)\n"
      "v_mfma_f32_16x16x32_bf16 v[204:207], a[212:215], v[84:87], v[204:207]\n"
      "ds_read_b64_tr_b16 a[194:195], v14 offset:2048\n"
      "s_waitcnt lgkmcnt(11)\n"
      "v_mfma_f32_16x16x32_bf16 v[204:207], a[216:219], a[0:3], v[204:207]\n"
      "ds_read_b64_tr_b16 a[196:197], v14 offset:4096\n"
      "s_waitcnt lgkmcnt(10)\n"
      "v_mfma_f32_16x16x32_bf16 v[204:207], a[220:223], a[12:15], v[204:207]\n"
      "ds_read_b64_tr_b16 a[198:199], v14 offset:6144\n"
      "v_mfma_f32_16x16x32_bf16 v[208:211], a[220:223], a[16:19], 0\n"
      "ds_read_b64_tr_b16 a[200:201], v26\n"
      "v_mfma_f32_16x16x32_bf16 v[208:211], a[216:219], a[4:7], v[208:211]\n"
      "ds_read_b64_tr_b16 a[202:203], v26 offset:2048\n"
      "v_mfma_f32_16x16x32_bf16 v[208:211], a[212:215], v[88:91], v[208:211]\n"
      "ds_read_b64_tr_b16 a[204:205], v26 offset:4096\n"
      "v_mfma_f32_16x16x32_bf16 v[208:211], a[208:211], v[64:67], v[208:211]\n"
      "ds_read_b64_tr_b16 a[206:207], v26 offset:6144\n"
      "s_add_u32 s82, s82, 0x80\n"
      "s_cmp_eq_u32 s82, 0x380\n"
      "s_cselect_b32 s82, 0, s82\n"
      "s_add_u32 s88, s74, s82\n"
      "s_add_u32 m0, s85, 0xc000\n"
      "v_mfma_f32_16x16x32_bf16 v[212:215], a[208:211], v[68:71], 0\n"
      "ds_read_b64_tr_b16 a[208:209], v15\n"
      "buffer_load_dwordx4 v10, s[24:27], s88 offen lds\n"
      "v_mfma_f32_16x16x32_bf16 v[212:215], a[212:215], v[92:95], v[212:215]\n"
      "ds_read_b64_tr_b16 a[210:211], v15 offset:2048\n"
      "v_mfma_f32_16x16x32_bf16 v[212:215], a[216:219], a[8:11], v[212:215]\n"
      "ds_read_b64_tr_b16 a[212:213], v15 offset:4096\n"
      "buffer_load_dwordx4 v11, s[24:27], s88 offen offset:1024 lds\n"
      "v_mfma_f32_16x16x32_bf16 v[212:215], a[220:223], a[20:23], v[212:215]\n"
      "ds_read_b64_tr_b16 a[214:215], v15 offset:6144\n"
      "s_waitcnt lgkmcnt(10)\n"
      "v_mfma_f32_16x16x32_bf16 v[192:195], a[192:195], v[108:111], v[192:195]\n"
      "ds_read_b64_tr_b16 a[216:217], v27\n"
      "buffer_load_dwordx4 v12, s[24:27], s88 offen offset:2048 lds\n"
      "s_waitcnt lgkmcnt(9)\n"
      "v_mfma_f32_16x16x32_bf16 v[192:195], a[196:199], v[168:171], v[192:195]\n"
      "ds_read_b64_tr_b16 a[218:219], v27 offset:2048\n"
      "s_waitcnt lgkmcnt(8)\n"
      "v_mfma_f32_16x16x32_bf16 v[192:195], a[200:203], a[24:27], v[192:195]\n"
      "ds_read_b64_tr_b16 a[220:221], v27 offset:4096\n"
      "buffer_load_dwordx4 v13, s[24:27], s88 offen offset:3072 lds\n"
      "s_waitcnt lgkmcnt(7)\n"
      "v_mfma_f32_16x16x32_bf16 v[192:195], a[204:207], a[36:39], v[192:195]\n"
      "ds_read_b64_tr_b16 a[222:223], v27 offset:6144\n"
      "s_add_u32 s88, s75, s82\n"
      "s_add_u32 m0, s85, 0x24000\n"
      "v_mfma_f32_16x16x32_bf16 v[196:199], a[204:207], a[40:43], v[196:199]\n"
      "ds_read_b128 a[48:51], v45 offset:8192\n"
      "buffer_load_dwordx4 v10, s[24:27], s88 offen lds\n"
      "v_mfma_f32_16x16x32_bf16 v[196:199], a[200:203], a[28:31], v[196:199]\n"
      "ds_read_b128 a[60:63], v45 offset:9216\n"
      "v_mfma_f32_16x16x32_bf16 v[196:199], a[196:199], v[172:175], v[196:199]\n"
      "ds_read_b128 a[52:55], v31 offset:24576\n"
      "buffer_load_dwordx4 v11, s[24:27], s88 offen offset:1024 lds\n"
      "v_mfma_f32_16x16x32_bf16 v[196:199], a[192:195], v[112:115], v[196:199]\n"
      "ds_read_b128 a[64:67], v31 offset:25600\n"
      "v_mfma_f32_16x16x32_bf16 v[200:203], a[192:195], v[116:119], v[200:203]\n"
      "ds_read_b128 a[56:59], v31 offset:32768\n"
      "buffer_load_dwordx4 v12, s[24:27], s88 offen offset:2048 lds\n"
      "v_mfma_f32_16x16x32_bf16 v[200:203], a[196:199], v[176:179], v[200:203]\n"
      "ds_read_b128 a[68:71], v31 offset:33792\n"
      "v_mfma_f32_16x16x32_bf16 v[200:203], a[200:203], a[32:35], v[200:203]\n"
      "buffer_load_dwordx4 v13, s[24:27], s88 offen offset:3072 lds\n"
      "v_mfma_f32_16x16x32_bf16 v[200:203], a[204:207], a[44:47], v[200:203]\n"
      "s_waitcnt vmcnt(12) lgkmcnt(0)\n"
      "s_barrier\n"
      "v_mfma_f32_16x16x32_bf16 v[204:207], a[208:211], v[108:111], v[204:207]\n"
      "ds_read_b64_tr_b16 a[192:193], v16\n"
      "v_mfma_f32_16x16x32_bf16 v[204:207], a[212:215], v[168:171], v[204:207]\n"
      "ds_read_b64_tr_b16 a[194:195], v16 offset:2048\n"
      "v_mfma_f32_16x16x32_bf16 v[204:207], a[216:219], a[24:27], v[204:207]\n"
      "ds_read_b64_tr_b16 a[196:197], v16 offset:4096\n"
      "v_mfma_f32_16x16x32_bf16 v[204:207], a[220:223], a[36:39], v[204:207]\n"
      "ds_read_b64_tr_b16 a[198:199], v16 offset:6144\n"
      "v_mfma_f32_16x16x32_bf16 v[208:211], a[220:223], a[40:43], v[208:211]\n"
      "ds_read_b64_tr_b16 a[200:201], v28\n"
      "v_mfma_f32_16x16x32_bf16 v[208:211], a[216:219], a[28:31], v[208:211]\n"
      "ds_read_b64_tr_b16 a[202:203], v28 offset:2048\n"
      "v_mfma_f32_16x16x32_bf16 v[208:211], a[212:215], v[172:175], v[208:211]\n"
      "ds_read_b64_tr_b16 a[204:205], v28 offset:4096\n"
      "v_mfma_f32_16x16x32_bf16 v[208:211], a[208:211], v[112:115], v[208:211]\n"
      "ds_read_b64_tr_b16 a[206:207], v28 offset:6144\n"
      "s_add_u32 s88, s76, s82\n"
      "s_add_u32 m0, s85, 0x18000\n"
      "v_mfma_f32_16x16x32_bf16 v[212:215], a[208:211], v[116:119], v[212:215]\n"
      "ds_read_b64_tr_b16 a[208:209], v17\n"
      "buffer_load_dwordx4 v10, s[24:27], s88 offen lds\n"
      "v_mfma_f32_16x16x32_bf16 v[212:215], a[212:215], v[176:179], v[212:215]\n"
      "ds_read_b64_tr_b16 a[210:211], v17 offset:2048\n"
      "v_mfma_f32_16x16x32_bf16 v[212:215], a[216:219], a[32:35], v[212:215]\n"
      "ds_read_b64_tr_b16 a[212:213], v17 offset:4096\n"
      "buffer_load_dwordx4 v11, s[24:27], s88 offen offset:1024 lds\n"
      "v_mfma_f32_16x16x32_bf16 v[212:215], a[220:223], a[44:47], v[212:215]\n"
      "ds_read_b64_tr_b16 a[214:215], v17 offset:6144\n"
      "s_waitcnt lgkmcnt(10)\n"
      "v_mfma_f32_16x16x32_bf16 v[192:195], a[192:195], v[180:183], v[192:195]\n"
      "ds_read_b64_tr_b16 a[216:217], v29\n"
      "buffer_load_dwordx4 v12, s[24:27], s88 offen offset:2048 lds\n"
      "s_waitcnt lgkmcnt(9)\n"
      "v_mfma_f32_16x16x32_bf16 v[192:195], a[196:199], v[240:243], v[192:195]\n"
      "ds_read_b64_tr_b16 a[218:219], v29 offset:2048\n"
      "s_waitcnt lgkmcnt(8)\n"
      "v_mfma_f32_16x16x32_bf16 v[192:195], a[200:203], a[48:51], v[192:195]\n"
      "ds_read_b64_tr_b16 a[220:221], v29 offset:4096\n"
      "buffer_load_dwordx4 v13, s[24:27], s88 offen offset:3072 lds\n"
      "s_waitcnt lgkmcnt(7)\n"
      "v_mfma_f32_16x16x32_bf16 v[192:195], a[204:207], a[60:63], v[192:195]\n"
      "ds_read_b64_tr_b16 a[222:223], v29 offset:6144\n"
      "s_add_u32 s88, s77, s82\n"
      "s_add_u32 m0, s85, 0x8000\n"
      "v_mfma_f32_16x16x32_bf16 v[196:199], a[204:207], a[64:67], v[196:199]\n"
      "ds_read_b128 a[72:75], v31\n"
      "buffer_load_dwordx4 v10, s[24:27], s88 offen lds\n"
      "v_mfma_f32_16x16x32_bf16 v[196:199], a[200:203], a[52:55], v[196:199]\n"
      "ds_read_b128 a[84:87], v31 offset:1024\n"
      "v_mfma_f32_16x16x32_bf16 v[196:199], a[196:199], v[244:247], v[196:199]\n"
      "ds_read_b128 a[76:79], v31 offset:8192\n"
      "buffer_load_dwordx4 v11, s[24:27], s88 offen offset:1024 lds\n"
      "v_mfma_f32_16x16x32_bf16 v[196:199], a[192:195], v[184:187], v[196:199]\n"
      "ds_read_b128 a[88:91], v31 offset:9216\n"
      "v_mfma_f32_16x16x32_bf16 v[200:203], a[192:195], v[188:191], v[200:203]\n"
      "ds_read_b128 a[80:83], v31 offset:16384\n"
      "buffer_load_dwordx4 v12, s[24:27], s88 offen offset:2048 lds\n"
      "v_mfma_f32_16x16x32_bf16 v[200:203], a[196:199], v[248:251], v[200:203]\n"
      "ds_read_b128 a[92:95], v31 offset:17408\n"
      "v_mfma_f32_16x16x32_bf16 v[200:203], a[200:203], a[56:59], v[200:203]\n"
      "buffer_load_dwordx4 v13, s[24:27], s88 offen offset:3072 lds\n"
      "v_mfma_f32_16x16x32_bf16 v[200:203], a[204:207], a[68:71], v[200:203]\n"
      "s_waitcnt vmcnt(16) lgkmcnt(0)\n"
      "s_barrier\n"
      "v_mfma_f32_16x16x32_bf16 v[204:207], a[208:211], v[180:183], v[204:207]\n"
      "ds_read_b64_tr_b16 a[192:193], v14 offset:16384\n"
      "v_mfma_f32_16x16x32_bf16 v[204:207], a[212:215], v[240:243], v[204:207]\n"
      "ds_read_b64_tr_b16 a[194:195], v14 offset:18432\n"
      "v_mfma_f32_16x16x32_bf16 v[204:207], a[216:219], a[48:51], v[204:207]\n"
      "ds_read_b64_tr_b16 a[196:197], v14 offset:20480\n"
      "v_mfma_f32_16x16x32_bf16 v[204:207], a[220:223], a[60:63], v[204:207]\n"
      "ds_read_b64_tr_b16 a[198:199], v14 offset:22528\n"
      "v_mfma_f32_16x16x32_bf16 v[208:211], a[220:223], a[64:67], v[208:211]\n"
      "ds_read_b64_tr_b16 a[200:201], v26 offset:16384\n"
      "v_add_co_u32 v34, vcc, 0x7f4a7c15, v34\n"
      "v_mfma_f32_16x16x32_bf16 v[208:211], a[216:219], a[52:55], v[208:211]\n"
      "ds_read_b64_tr_b16 a[202:203], v26 offset:18432\n"
      "v_mfma_f32_16x16x32_bf16 v[208:211], a[212:215], v[244:247], v[208:211]\n"
      "ds_read_b64_tr_b16 a[204:205], v26 offset:20480\n"
      "v_mfma_f32_16x16x32_bf16 v[208:211], a[208:211], v[184:187], v[208:211]\n"
      "ds_read_b64_tr_b16 a[206:207], v26 offset:22528\n"
      "s_add_u32 s82, s82, 0x80\n"
      "s_cmp_eq_u32 s82, 0x380\n"
      "s_cselect_b32 s82, 0, s82\n"
      "s_add_u32 s88, s74, s82\n"
      "s_add_u32 m0, s85, 0\n"
      "v_mfma_f32_16x16x32_bf16 v[212:215], a[208:211], v[188:191], v[212:215]\n"
      "ds_read_b64_tr_b16 a[208:209], v15 offset:16384\n"
      "buffer_load_dwordx4 v10, s[24:27], s88 offen lds\n"
      "v_mfma_f32_16x16x32_bf16 v[212:215], a[212:215], v[248:251], v[212:215]\n"
      "ds_read_b64_tr_b16 a[210:211], v15 offset:18432\n"
      "v_mfma_f32_16x16x32_bf16 v[212:215], a[216:219], a[56:59], v[212:215]\n"
      "ds_read_b64_tr_b16 a[212:213], v15 offset:20480\n"
      "v_mov_b32 v4, 0x9e3779b9\n"
      "buffer_load_dwordx4 v11, s[24:27], s88 offen offset:1024 lds\n"
      "v_mfma_f32_16x16x32_bf16 v[212:215], a[220:223], a[68:71], v[212:215]\n"
      "ds_read_b64_tr_b16 a[214:215], v15 offset:22528\n"
      "s_waitcnt lgkmcnt(10)\n"
      "v_mfma_f32_16x16x32_bf16 v[192:195], a[192:195], v[252:255], v[192:195]\n"
      "ds_read_b64_tr_b16 a[216:217], v27 offset:16384\n"
      "buffer_load_dwordx4 v12, s[24:27], s88 offen offset:2048 lds\n"
      "s_waitcnt lgkmcnt(9)\n"
      "v_mfma_f32_16x16x32_bf16 v[192:195], a[196:199], v[224:227], v[192:195]\n"
      "ds_read_b64_tr_b16 a[218:219], v27 offset:18432\n"
      "s_waitcnt lgkmcnt(8)\n"
      "v_mfma_f32_16x16x32_bf16 v[192:195], a[200:203], a[72:75], v[192:195]\n"
      "ds_read_b64_tr_b16 a[220:221], v27 offset:20480\n"
      "buffer_load_dwordx4 v13, s[24:27], s88 offen offset:3072 lds\n"
      "s_waitcnt lgkmcnt(7)\n"
      "v_mfma_f32_16x16x32_bf16 v[192:195], a[204:207], a[84:87], v[192:195]\n"
      "ds_read_b64_tr_b16 a[222:223], v27 offset:22528\n"
      "s_add_u32 s88, s75, s82\n"
      "s_add_u32 m0, s85, 0x4000\n"
      "v_mfma_f32_16x16x32_bf16 v[196:199], a[204:207], a[88:91], v[196:199]\n"
      "buffer_load_dwordx4 v10, s[24:27], s88 offen lds\n"
      "v_mfma_f32_16x16x32_bf16 v[196:199], a[200:203], a[76:79], v[196:199]\n"
      "v_addc_co_u32 v35, vcc, v35, v4, vcc\n"
      "v_mfma_f32_16x16x32_bf16 v[196:199], a[196:199], v[228:231], v[196:199]\n"
      "buffer_load_dwordx4 v11, s[24:27], s88 offen offset:1024 lds\n"
      "v_mfma_f32_16x16x32_bf16 v[196:199], a[192:195], v[216:219], v[196:199]\n"
      "v_mfma_f32_16x16x32_bf16 v[200:203], a[192:195], v[220:223], v[200:203]\n"
      "buffer_load_dwordx4 v12, s[24:27], s88 offen offset:2048 lds\n"
      "v_mfma_f32_16x16x32_bf16 v[200:203], a[196:199], v[232:235], v[200:203]\n"
      "v_mfma_f32_16x16x32_bf16 v[200:203], a[200:203], a[80:83], v[200:203]\n"
      "buffer_load_dwordx4 v13, s[24:27], s88 offen offset:3072 lds\n"
      "v_mfma_f32_16x16x32_bf16 v[200:203], a[204:207], a[92:95], v[200:203]\n"
      "s_waitcnt vmcnt(20)\n"
      "s_barrier\n"
      "s_waitcnt lgkmcnt(6)\n"
      "v_mfma_f32_16x16x32_bf16 v[204:207], a[208:211], v[252:255], v[204:207]\n"
      "ds_read_b64_tr_b16 a[192:193], v18 offset:49152\n"
      "s_waitcnt lgkmcnt(5)\n"
      "v_mfma_f32_16x16x32_bf16 v[204:207], a[212:215], v[224:227], v[204:207]\n"
      "ds_read_b64_tr_b16 a[194:195], v18 offset:51200\n"
      "s_waitcnt lgkmcnt(4)\n"
      "v_mfma_f32_16x16x32_bf16 v[204:207], a[216:219], a[72:75], v[204:207]\n"
      "ds_read_b64_tr_b16 a[196:197], v18 offset:53248\n"
      "v_cvt_pk_bf16_f32 v144, v192, v193\n"
      "s_mul_i32 s90, s83, 0x300\n"
      "s_add_u32 s83, s83, 0x80\n"
      "s_cmp_eq_u32 s83, 0x380\n"
      "s_cselect_b32 s83, 0, s83\n"
      "s_waitcnt lgkmcnt(3)\n"
      "v_mfma_f32_16x16x32_bf16 v[204:207], a[220:223], a[84:87], v[204:207]\n"
      "ds_read_b64_tr_b16 a[198:199], v18 offset:55296\n"
      "v_cvt_pk_bf16_f32 v145, v194, v195\n"
      "v_mfma_f32_16x16x32_bf16 v[208:211], a[220:223], a[88:91], v[208:211]\n"
      "ds_read_b64_tr_b16 a[200:201], v24 offset:49152\n"
      "v_cvt_pk_bf16_f32 v148, v196, v197\n"
      "v_mfma_f32_16x16x32_bf16 v[208:211], a[216:219], a[76:79], v[208:211]\n"
      "ds_read_b64_tr_b16 a[202:203], v24 offset:51200\n"
      "v_mfma_f32_16x16x32_bf16 v[208:211], a[212:215], v[228:231], v[208:211]\n"
      "ds_read_b64_tr_b16 a[204:205], v24 offset:53248\n"
      "v_cvt_pk_bf16_f32 v149, v198, v199\n"
      "v_mfma_f32_16x16x32_bf16 v[208:211], a[208:211], v[216:219], v[208:211]\n"
      "ds_read_b64_tr_b16 a[206:207], v24 offset:55296\n"
      "v_cvt_pk_bf16_f32 v152, v200, v201\n"
      "v_mfma_f32_16x16x32_bf16 v[212:215], a[208:211], v[220:223], v[212:215]\n"
      "ds_read_b64_tr_b16 a[208:209], v19 offset:49152\n"
      "v_cvt_pk_bf16_f32 v153, v202, v203\n"
      "v_mfma_f32_16x16x32_bf16 v[212:215], a[212:215], v[232:235], v[212:215]\n"
      "ds_read_b64_tr_b16 a[210:211], v19 offset:51200\n"
      "s_add_u32 s88, s76, s82\n"
      "s_add_u32 m0, s85, 0x1c000\n"
      "v_mfma_f32_16x16x32_bf16 v[212:215], a[216:219], a[80:83], v[212:215]\n"
      "ds_read_b64_tr_b16 a[212:213], v19 offset:53248\n"
      "v_cvt_pk_bf16_f32 v146, v204, v205\n"
      "v_cvt_pk_bf16_f32 v147, v206, v207\n"
      "buffer_load_dwordx4 v10, s[24:27], s88 offen lds\n"
      "v_mfma_f32_16x16x32_bf16 v[212:215], a[220:223], a[92:95], v[212:215]\n"
      "ds_read_b64_tr_b16 a[214:215], v19 offset:55296\n"
      "s_waitcnt lgkmcnt(10)\n"
      "v_mfma_f32_16x16x32_bf16 v[192:195], a[192:195], v[60:63], 0\n"
      "ds_read_b64_tr_b16 a[216:217], v25 offset:49152\n"
      "v_cvt_pk_bf16_f32 v150, v208, v209\n"
      "v_cvt_pk_bf16_f32 v151, v210, v211\n"
      "buffer_load_dwordx4 v11, s[24:27], s88 offen offset:1024 lds\n"
      "s_waitcnt lgkmcnt(9)\n"
      "v_mfma_f32_16x16x32_bf16 v[192:195], a[196:199], v[84:87], v[192:195]\n"
      "ds_read_b64_tr_b16 a[218:219], v25 offset:51200\n"
      "s_waitcnt lgkmcnt(8)\n"
      "v_mfma_f32_16x16x32_bf16 v[192:195], a[200:203], a[0:3], v[192:195]\n"
      "ds_read_b64_tr_b16 a[220:221], v25 offset:53248\n"
      "v_cvt_pk_bf16_f32 v154, v212, v213\n"
      "v_cvt_pk_bf16_f32 v155, v214, v215\n"
      "buffer_load_dwordx4 v12, s[24:27], s88 offen offset:2048 lds\n"
      "s_waitcnt lgkmcnt(7)\n"
      "v_mfma_f32_16x16x32_bf16 v[192:195], a[204:207], a[12:15], v[192:195]\n"
      "ds_read_b64_tr_b16 a[222:223], v25 offset:55296\n"
      "v_mfma_f32_16x16x32_bf16 v[196:199], a[204:207], a[16:19], 0\n"
      "buffer_load_dwordx4 v13, s[24:27], s88 offen offset:3072 lds\n"
      "v_mfma_f32_16x16x32_bf16 v[196:199], a[200:203], a[4:7], v[196:199]\n"
      "v_mfma_f32_16x16x32_bf16 v[196:199], a[196:199], v[88:91], v[196:199]\n"
      "buffer_store_dwordx4 v[144:147], v32, s[36:39], s90 offen sc1\n"
      "v_mfma_f32_16x16x32_bf16 v[196:199], a[192:195], v[64:67], v[196:199]\n"
      "v_mfma_f32_16x16x32_bf16 v[200:203], a[192:195], v[68:71], 0\n"
      "buffer_store_dwordx4 v[148:151], v32, s[36:39], s90 offen offset:1024 sc1\n"
      "v_mfma_f32_16x16x32_bf16 v[200:203], a[196:199], v[92:95], v[200:203]\n"
      "v_mfma_f32_16x16x32_bf16 v[200:203], a[200:203], a[8:11], v[200:203]\n"
      "buffer_store_dwordx4 v[152:155], v32, s[36:39], s90 offen offset:2048 sc1\n"
      "v_mfma_f32_16x16x32_bf16 v[200:203], a[204:207], a[20:23], v[200:203]\n"
      "s_waitcnt vmcnt(11) lgkmcnt(0)\n"
      "s_barrier\n"
      "v_mfma_f32_16x16x32_bf16 v[204:207], a[208:211], v[60:63], 0\n"
      "ds_read_b64_tr_b16 a[192:193], v16 offset:16384\n"
      "v_mfma_f32_16x16x32_bf16 v[204:207], a[212:215], v[84:87], v[204:207]\n"
      "ds_read_b64_tr_b16 a[194:195], v16 offset:18432\n"
      "v_mfma_f32_16x16x32_bf16 v[204:207], a[216:219], a[0:3], v[204:207]\n"
      "ds_read_b64_tr_b16 a[196:197], v16 offset:20480\n"
      "v_mfma_f32_16x16x32_bf16 v[204:207], a[220:223], a[12:15], v[204:207]\n"
      "ds_read_b64_tr_b16 a[198:199], v16 offset:22528\n"
      "v_mfma_f32_16x16x32_bf16 v[208:211], a[220:223], a[16:19], 0\n"
      "ds_read_b64_tr_b16 a[200:201], v28 offset:16384\n"
      "v_mfma_f32_16x16x32_bf16 v[208:211], a[216:219], a[4:7], v[208:211]\n"
      "ds_read_b64_tr_b16 a[202:203], v28 offset:18432\n"
      "v_mfma_f32_16x16x32_bf16 v[208:211], a[212:215], v[88:91], v[208:211]\n"
      "ds_read_b64_tr_b16 a[204:205], v28 offset:20480\n"
      "v_mfma_f32_16x16x32_bf16 v[208:211], a[208:211], v[64:67], v[208:211]\n"
      "ds_read_b64_tr_b16 a[206:207], v28 offset:22528\n"
      "v_mfma_f32_16x16x32_bf16 v[212:215], a[208:211], v[68:71], 0\n"
      "ds_read_b64_tr_b16 a[208:209], v17 offset:16384\n"
      "v_mfma_f32_16x16x32_bf16 v[212:215], a[212:215], v[92:95], v[212:215]\n"
      "ds_read_b64_tr_b16 a[210:211], v17 offset:18432\n"
      "v_mfma_f32_16x16x32_bf16 v[212:215], a[216:219], a[8:11], v[212:215]\n"
      "ds_read_b64_tr_b16 a[212:213], v17 offset:20480\n"
      "v_mfma_f32_16x16x32_bf16 v[212:215], a[220:223], a[20:23], v[212:215]\n"
      "ds_read_b64_tr_b16 a[214:215], v17 offset:22528\n"
      "s_waitcnt lgkmcnt(10)\n"
      "v_mfma_f32_16x16x32_bf16 v[192:195], a[192:195], v[108:111], v[192:195]\n"
      "ds_read_b64_tr_b16 a[216:217], v29 offset:16384\n"
      "s_waitcnt lgkmcnt(9)\n"
      "v_mfma_f32_16x16x32_bf16 v[192:195], a[196:199], v[168:171], v[192:195]\n"
      "ds_read_b64_tr_b16 a[218:219], v29 offset:18432\n"
      "s_waitcnt lgkmcnt(8)\n"
      "v_mfma_f32_16x16x32_bf16 v[192:195], a[200:203], a[24:27], v[192:195]\n"
      "ds_read_b64_tr_b16 a[220:221], v29 offset:20480\n"
      "s_waitcnt lgkmcnt(7)\n"
      "v_mfma_f32_16x16x32_bf16 v[192:195], a[204:207], a[36:39], v[192:195]\n"
      "ds_read_b64_tr_b16 a[222:223], v29 offset:22528\n"
      "s_add_u32 s88, s77, s82\n"
      "s_add_u32 m0, s85, 0x10000\n"
      "v_mfma_f32_16x16x32_bf16 v[196:199], a[204:207], a[40:43], v[196:199]\n"
      "buffer_load_dwordx4 v10, s[24:27], s88 offen lds\n"
      "v_mfma_f32_16x16x32_bf16 v[196:199], a[200:203], a[28:31], v[196:199]\n"
      "v_mfma_f32_16x16x32_bf16 v[196:199], a[196:199], v[172:175], v[196:199]\n"
      "buffer_load_dwordx4 v11, s[24:27], s88 offen offset:1024 lds\n"
      "v_mfma_f32_16x16x32_bf16 v[196:199], a[192:195], v[112:115], v[196:199]\n"
      "v_mfma_f32_16x16x32_bf16 v[200:203], a[192:195], v[116:119], v[200:203]\n"
      "buffer_load_dwordx4 v12, s[24:27], s88 offen offset:2048 lds\n"
      "v_mfma_f32_16x16x32_bf16 v[200:203], a[196:199], v[176:179], v[200:203]\n"
      "v_mfma_f32_16x16x32_bf16 v[200:203], a[200:203], a[32:35], v[200:203]\n"
      "buffer_load_dwordx4 v13, s[24:27], s88 offen offset:3072 lds\n"
      "v_mfma_f32_16x16x32_bf16 v[200:203], a[204:207], a[44:47], v[200:203]\n"
      "s_waitcnt lgkmcnt(6)\n"
      "v_mfma_f32_16x16x32_bf16 v[204:207], a[208:211], v[108:111], v[204:207]\n"
      "ds_read_b64_tr_b16 a[192:193], v14 offset:32768\n"
      "s_waitcnt lgkmcnt(5)\n"
      "v_mfma_f32_16x16x32_bf16 v[204:207], a[212:215], v[168:171], v[204:207]\n"
      "ds_read_b64_tr_b16 a[194:195], v14 offset:34816\n"
      "s_waitcnt lgkmcnt(4)\n"
      "v_mfma_f32_16x16x32_bf16 v[204:207], a[216:219], a[24:27], v[204:207]\n"
      "ds_read_b64_tr_b16 a[196:197], v14 offset:36864\n"
      "s_waitcnt lgkmcnt(3)\n"
      "v_mfma_f32_16x16x32_bf16 v[204:207], a[220:223], a[36:39], v[204:207]\n"
      "ds_read_b64_tr_b16 a[198:199], v14 offset:38912\n"
      "v_mfma_f32_16x16x32_bf16 v[208:211], a[220:223], a[40:43], v[208:211]\n"
      "ds_read_b64_tr_b16 a[200:201], v26 offset:32768\n"
      "v_mfma_f32_16x16x32_bf16 v[208:211], a[216:219], a[28:31], v[208:211]\n"
      "ds_read_b64_tr_b16 a[202:203], v26 offset:34816\n"
      "v_mfma_f32_16x16x32_bf16 v[208:211], a[212:215], v[172:175], v[208:211]\n"
      "ds_read_b64_tr_b16 a[204:205], v26 offset:36864\n"
      "v_mfma_f32_16x16x32_bf16 v[208:211], a[208:211], v[112:115], v[208:211]\n"
      "ds_read_b64_tr_b16 a[206:207], v26 offset:38912\n"
      "v_mfma_f32_16x16x32_bf16 v[212:215], a[208:211], v[116:119], v[212:215]\n"
      "ds_read_b64_tr_b16 a[208:209], v15 offset:32768\n"
      "v_mfma_f32_16x16x32_bf16 v[212:215], a[212:215], v[176:179], v[212:215]\n"
      "ds_read_b64_tr_b16 a[210:211], v15 offset:34816\n"
      "v_mfma_f32_16x16x32_bf16 v[212:215], a[216:219], a[32:35], v[212:215]\n"
      "ds_read_b64_tr_b16 a[212:213], v15 offset:36864\n"
      "v_mfma_f32_16x16x32_bf16 v[212:215], a[220:223], a[44:47], v[212:215]\n"
      "ds_read_b64_tr_b16 a[214:215], v15 offset:38912\n"
      "s_waitcnt lgkmcnt(10)\n"
      "v_mfma_f32_16x16x32_bf16 v[192:195], a[192:195], v[180:183], v[192:195]\n"
      "ds_read_b64_tr_b16 a[216:217], v27 offset:32768\n"
      "s_waitcnt lgkmcnt(9)\n"
      "v_mfma_f32_16x16x32_bf16 v[192:195], a[196:199], v[240:243], v[192:195]\n"
      "ds_read_b64_tr_b16 a[218:219], v27 offset:34816\n"
      "s_waitcnt lgkmcnt(8)\n"
      "v_mfma_f32_16x16x32_bf16 v[192:195], a[200:203], a[48:51], v[192:195]\n"
      "ds_read_b64_tr_b16 a[220:221], v27 offset:36864\n"
      "s_waitcnt lgkmcnt(7)\n"
      "v_mfma_f32_16x16x32_bf16 v[192:195], a[204:207], a[60:63], v[192:195]\n"
      "ds_read_b64_tr_b16 a[222:223], v27 offset:38912\n"
      "s_add_u32 s82, s82, 0x80\n"
      "s_cmp_eq_u32 s82, 0x380\n"
      "s_cselect_b32 s82, 0, s82\n"
      "s_add_u32 s88, s74, s82\n"
      "s_add_u32 m0, s85, 0x20000\n"
      "v_mfma_f32_16x16x32_bf16 v[196:199], a[204:207], a[64:67], v[196:199]\n"
      "buffer_load_dwordx4 v10, s[24:27], s88 offen lds\n"
      "v_mfma_f32_16x16x32_bf16 v[196:199], a[200:203], a[52:55], v[196:199]\n"
      "v_mfma_f32_16x16x32_bf16 v[196:199], a[196:199], v[244:247], v[196:199]\n"
      "buffer_load_dwordx4 v11, s[24:27], s88 offen offset:1024 lds\n"
      "v_mfma_f32_16x16x32_bf16 v[196:199], a[192:195], v[184:187], v[196:199]\n"
      "v_mfma_f32_16x16x32_bf16 v[200:203], a[192:195], v[188:191], v[200:203]\n"
      "buffer_load_dwordx4 v12, s[24:27], s88 offen offset:2048 lds\n"
      "v_mfma_f32_16x16x32_bf16 v[200:203], a[196:199], v[248:251], v[200:203]\n"
      "v_mfma_f32_16x16x32_bf16 v[200:203], a[200:203], a[56:59], v[200:203]\n"
      "buffer_load_dwordx4 v13, s[24:27], s88 offen offset:3072 lds\n"
      "v_mfma_f32_16x16x32_bf16 v[200:203], a[204:207], a[68:71], v[200:203]\n"
      "s_waitcnt lgkmcnt(6)\n"
      "v_mfma_f32_16x16x32_bf16 v[204:207], a[208:211], v[180:183], v[204:207]\n"
      "ds_read_b64_tr_b16 a[192:193], v18 offset:32768\n"
      "s_waitcnt lgkmcnt(5)\n"
      "v_mfma_f32_16x16x32_bf16 v[204:207], a[212:215], v[240:243], v[204:207]\n"
      "ds_read_b64_tr_b16 a[194:195], v18 offset:34816\n"
      "s_waitcnt lgkmcnt(4)\n"
      "v_mfma_f32_16x16x32_bf16 v[204:207], a[216:219], a[48:51], v[204:207]\n"
      "ds_read_b64_tr_b16 a[196:197], v18 offset:36864\n"
      "s_waitcnt lgkmcnt(3)\n"
      "v_mfma_f32_16x16x32_bf16 v[204:207], a[220:223], a[60:63], v[204:207]\n"
      "ds_read_b64_tr_b16 a[198:199], v18 offset:38912\n"
      "v_mfma_f32_16x16x32_bf16 v[208:211], a[220:223], a[64:67], v[208:211]\n"
      "ds_read_b64_tr_b16 a[200:201], v24 offset:32768\n"
      "v_mfma_f32_16x16x32_bf16 v[208:211], a[216:219], a[52:55], v[208:211]\n"
      "ds_read_b64_tr_b16 a[202:203], v24 offset:34816\n"
      "v_mfma_f32_16x16x32_bf16 v[208:211], a[212:215], v[244:247], v[208:211]\n"
      "ds_read_b64_tr_b16 a[204:205], v24 offset:36864\n"
      "v_mfma_f32_16x16x32_bf16 v[208:211], a[208:211], v[184:187], v[208:211]\n"
      "ds_read_b64_tr_b16 a[206:207], v24 offset:38912\n"
      "v_mfma_f32_16x16x32_bf16 v[212:215], a[208:211], v[188:191], v[212:215]\n"
      "ds_read_b64_tr_b16 a[208:209], v19 offset:32768\n"
      "v_mfma_f32_16x16x32_bf16 v[212:215], a[212:215], v[248:251], v[212:215]\n"
      "ds_read_b64_tr_b16 a[210:211], v19 offset:34816\n"
      "v_mfma_f32_16x16x32_bf16 v[212:215], a[216:219], a[56:59], v[212:215]\n"
      "ds_read_b64_tr_b16 a[212:213], v19 offset:36864\n"
      "v_mfma_f32_16x16x32_bf16 v[212:215], a[220:223], a[68:71], v[212:215]\n"
      "ds_read_b64_tr_b16 a[214:215], v19 offset:38912\n"
      "s_waitcnt lgkmcnt(10)\n"
      "v_mfma_f32_16x16x32_bf16 v[192:195], a[192:195], v[252:255], v[192:195]\n"
      "ds_read_b64_tr_b16 a[216:217], v25 offset:32768\n"
      "s_waitcnt lgkmcnt(9)\n"
      "v_mfma_f32_16x16x32_bf16 v[192:195], a[196:199], v[224:227], v[192:195]\n"
      "ds_read_b64_tr_b16 a[218:219], v25 offset:34816\n"
      "s_add_u32 s88, s75, s82\n"
      "s_add_u32 m0, s85, 0x14000\n"
      "s_waitcnt lgkmcnt(8)\n"
      "v_mfma_f32_16x16x32_bf16 v[192:195], a[200:203], a[72:75], v[192:195]\n"
      "ds_read_b64_tr_b16 a[220:221], v25 offset:36864\n"
      "buffer_load_dwordx4 v10, s[24:27], s88 offen lds\n"
      "s_waitcnt lgkmcnt(7)\n"
      "v_mfma_f32_16x16x32_bf16 v[192:195], a[204:207], a[84:87], v[192:195]\n"
      "ds_read_b64_tr_b16 a[222:223], v25 offset:38912\n"
      "v_mfma_f32_16x16x32_bf16 v[196:199], a[204:207], a[88:91], v[196:199]\n"
      "buffer_load_dwordx4 v11, s[24:27], s88 offen offset:1024 lds\n"
      "v_mfma_f32_16x16x32_bf16 v[196:199], a[200:203], a[76:79], v[196:199]\n"
      "v_mfma_f32_16x16x32_bf16 v[196:199], a[196:199], v[228:231], v[196:199]\n"
      "buffer_load_dwordx4 v12, s[24:27], s88 offen offset:2048 lds\n"
      "v_mfma_f32_16x16x32_bf16 v[196:199], a[192:195], v[216:219], v[196:199]\n"
      "v_mfma_f32_16x16x32_bf16 v[200:203], a[192:195], v[220:223], v[200:203]\n"
      "buffer_load_dwordx4 v13, s[24:27], s88 offen offset:3072 lds\n"
      "v_mfma_f32_16x16x32_bf16 v[200:203], a[196:199], v[232:235], v[200:203]\n"
      "v_mfma_f32_16x16x32_bf16 v[200:203], a[200:203], a[80:83], v[200:203]\n"
      "s_waitcnt vmcnt(12)\n"
      "buffer_store_dwordx2 v[34:35], v36, s[36:39], s48 offen sc1\n"
      "v_mfma_f32_16x16x32_bf16 v[200:203], a[204:207], a[92:95], v[200:203]\n"
      "s_add_u32 s48, s48, 0x80\n"
      "s_cmp_eq_u32 s48, 0x380\n"
      "s_cselect_b32 s48, 0, s48\n"
      "s_waitcnt lgkmcnt(6)\n"
      "v_mfma_f32_16x16x32_bf16 v[204:207], a[208:211], v[252:255], v[204:207]\n"
      "ds_read_b64_tr_b16 a[192:193], v18\n"
      "s_waitcnt lgkmcnt(5)\n"
      "v_mfma_f32_16x16x32_bf16 v[204:207], a[212:215], v[224:227], v[204:207]\n"
      "ds_read_b64_tr_b16 a[194:195], v18 offset:2048\n"
      "s_waitcnt lgkmcnt(4)\n"
      "v_mfma_f32_16x16x32_bf16 v[204:207], a[216:219], a[72:75], v[204:207]\n"
      "ds_read_b64_tr_b16 a[196:197], v18 offset:4096\n"
      "v_cvt_pk_bf16_f32 v156, v192, v193\n"
      "s_mul_i32 s90, s83, 0x300\n"
      "s_add_u32 s83, s83, 0x80\n"
      "s_cmp_eq_u32 s83, 0x380\n"
      "s_cselect_b32 s83, 0, s83\n"
      "s_waitcnt lgkmcnt(3)\n"
      "v_mfma_f32_16x16x32_bf16 v[204:207], a[220:223], a[84:87], v[204:207]\n"
      "ds_read_b64_tr_b16 a[198:199], v18 offset:6144\n"
      "v_cvt_pk_bf16_f32 v157, v194, v195\n"
      "v_mfma_f32_16x16x32_bf16 v[208:211], a[220:223], a[88:91], v[208:211]\n"
      "ds_read_b64_tr_b16 a[200:201], v24\n"
      "v_cvt_pk_bf16_f32 v160, v196, v197\n"
      "v_mfma_f32_16x16x32_bf16 v[208:211], a[216:219], a[76:79], v[208:211]\n"
      "ds_read_b64_tr_b16 a[202:203], v24 offset:2048\n"
      "v_mfma_f32_16x16x32_bf16 v[208:211], a[212:215], v[228:231], v[208:211]\n"
      "ds_read_b64_tr_b16 a[204:205], v24 offset:4096\n"
      "v_cvt_pk_bf16_f32 v161, v198, v199\n"
      "v_mfma_f32_16x16x32_bf16 v[208:211], a[208:211], v[216:219], v[208:211]\n"
      "ds_read_b64_tr_b16 a[206:207], v24 offset:6144\n"
      "v_cvt_pk_bf16_f32 v164, v200, v201\n"
      "v_mfma_f32_16x16x32_bf16 v[212:215], a[208:211], v[220:223], v[212:215]\n"
      "ds_read_b64_tr_b16 a[208:209], v19\n"
      "v_cvt_pk_bf16_f32 v165, v202, v203\n"
      "v_mfma_f32_16x16x32_bf16 v[212:215], a[212:215], v[232:235], v[212:215]\n"
      "ds_read_b64_tr_b16 a[210:211], v19 offset:2048\n"
      "s_add_u32 s88, s76, s82\n"
      "s_add_u32 m0, s85, 0xc000\n"
      "v_mfma_f32_16x16x32_bf16 v[212:215], a[216:219], a[80:83], v[212:215]\n"
      "ds_read_b64_tr_b16 a[212:213], v19 offset:4096\n"
      "v_cvt_pk_bf16_f32 v158, v204, v205\n"
      "v_cvt_pk_bf16_f32 v159, v206, v207\n"
      "buffer_load_dwordx4 v10, s[24:27], s88 offen lds\n"
      "v_mfma_f32_16x16x32_bf16 v[212:215], a[220:223], a[92:95], v[212:215]\n"
      "ds_read_b64_tr_b16 a[214:215], v19 offset:6144\n"
      "s_waitcnt lgkmcnt(10)\n"
      "v_mfma_f32_16x16x32_bf16 v[192:195], a[192:195], v[60:63], 0\n"
      "ds_read_b64_tr_b16 a[216:217], v25\n"
      "v_cvt_pk_bf16_f32 v162, v208, v209\n"
      "v_cvt_pk_bf16_f32 v163, v210, v211\n"
      "buffer_load_dwordx4 v11, s[24:27], s88 offen offset:1024 lds\n"
      "s_waitcnt lgkmcnt(9)\n"
      "v_mfma_f32_16x16x32_bf16 v[192:195], a[196:199], v[84:87], v[192:195]\n"
      "ds_read_b64_tr_b16 a[218:219], v25 offset:2048\n"
      "s_waitcnt lgkmcnt(8)\n"
      "v_mfma_f32_16x16x32_bf16 v[192:195], a[200:203], a[0:3], v[192:195]\n"
      "ds_read_b64_tr_b16 a[220:221], v25 offset:4096\n"
      "v_cvt_pk_bf16_f32 v166, v212, v213\n"
      "v_cvt_pk_bf16_f32 v167, v214, v215\n"
      "buffer_load_dwordx4 v12, s[24:27], s88 offen offset:2048 lds\n"
      "s_waitcnt lgkmcnt(7)\n"
      "v_mfma_f32_16x16x32_bf16 v[192:195], a[204:207], a[12:15], v[192:195]\n"
      "ds_read_b64_tr_b16 a[222:223], v25 offset:6144\n"
      "v_mfma_f32_16x16x32_bf16 v[196:199], a[204:207], a[16:19], 0\n"
      "buffer_load_dwordx4 v13, s[24:27], s88 offen offset:3072 lds\n"
      "v_mfma_f32_16x16x32_bf16 v[196:199], a[200:203], a[4:7], v[196:199]\n"
      "v_mfma_f32_16x16x32_bf16 v[196:199], a[196:199], v[88:91], v[196:199]\n"
      "buffer_store_dwordx4 v[156:159], v32, s[36:39], s90 offen sc1\n"
      "v_mfma_f32_16x16x32_bf16 v[196:199], a[192:195], v[64:67], v[196:199]\n"
      "v_mfma_f32_16x16x32_bf16 v[200:203], a[192:195], v[68:71], 0\n"
      "buffer_store_dwordx4 v[160:163], v32, s[36:39], s90 offen offset:1024 sc1\n"
      "v_mfma_f32_16x16x32_bf16 v[200:203], a[196:199], v[92:95], v[200:203]\n"
      "v_mfma_f32_16x16x32_bf16 v[200:203], a[200:203], a[8:11], v[200:203]\n"
      "buffer_store_dwordx4 v[164:167], v32, s[36:39], s90 offen offset:2048 sc1\n"
      "v_mfma_f32_16x16x32_bf16 v[200:203], a[204:207], a[20:23], v[200:203]\n"
      "s_waitcnt vmcnt(12) lgkmcnt(0)\n"
      "s_barrier\n"
      "v_mfma_f32_16x16x32_bf16 v[204:207], a[208:211], v[60:63], 0\n"
      "ds_read_b64_tr_b16 a[192:193], v18 offset:16384\n"
      "v_mfma_f32_16x16x32_bf16 v[204:207], a[212:215], v[84:87], v[204:207]\n"
      "ds_read_b64_tr_b16 a[194:195], v18 offset:18432\n"
      "v_mfma_f32_16x16x32_bf16 v[204:207], a[216:219], a[0:3], v[204:207]\n"
      "ds_read_b64_tr_b16 a[196:197], v18 offset:20480\n"
      "v_mfma_f32_16x16x32_bf16 v[204:207], a[220:223], a[12:15], v[204:207]\n"
      "ds_read_b64_tr_b16 a[198:199], v18 offset:22528\n"
      "v_mfma_f32_16x16x32_bf16 v[208:211], a[220:223], a[16:19], 0\n"
      "ds_read_b64_tr_b16 a[200:201], v24 offset:16384\n"
      "v_mfma_f32_16x16x32_bf16 v[208:211], a[216:219], a[4:7], v[208:211]\n"
      "ds_read_b64_tr_b16 a[202:203], v24 offset:18432\n"
      "v_mfma_f32_16x16x32_bf16 v[208:211], a[212:215], v[88:91], v[208:211]\n"
      "ds_read_b64_tr_b16 a[204:205], v24 offset:20480\n"
      "v_mfma_f32_16x16x32_bf16 v[208:211], a[208:211], v[64:67], v[208:211]\n"
      "ds_read_b64_tr_b16 a[206:207], v24 offset:22528\n"
      "v_mfma_f32_16x16x32_bf16 v[212:215], a[208:211], v[68:71], 0\n"
      "ds_read_b64_tr_b16 a[208:209], v19 offset:16384\n"
      "v_mfma_f32_16x16x32_bf16 v[212:215], a[212:215], v[92:95], v[212:215]\n"
      "ds_read_b64_tr_b16 a[210:211], v19 offset:18432\n"
      "v_mfma_f32_16x16x32_bf16 v[212:215], a[216:219], a[8:11], v[212:215]\n"
      "ds_read_b64_tr_b16 a[212:213], v19 offset:20480\n"
      "v_mfma_f32_16x16x32_bf16 v[212:215], a[220:223], a[20:23], v[212:215]\n"
      "ds_read_b64_tr_b16 a[214:215], v19 offset:22528\n"
      "s_waitcnt lgkmcnt(10)\n"
      "v_mfma_f32_16x16x32_bf16 v[192:195], a[192:195], v[108:111], v[192:195]\n"
      "ds_read_b64_tr_b16 a[216:217], v25 offset:16384\n"
      "s_waitcnt lgkmcnt(9)\n"
      "v_mfma_f32_16x16x32_bf16 v[192:195], a[196:199], v[168:171], v[192:195]\n"
      "ds_read_b64_tr_b16 a[218:219], v25 offset:18432\n"
      "s_waitcnt lgkmcnt(8)\n"
      "v_mfma_f32_16x16x32_bf16 v[192:195], a[200:203], a[24:27], v[192:195]\n"
      "ds_read_b64_tr_b16 a[220:221], v25 offset:20480\n"
      "s_waitcnt lgkmcnt(7)\n"
      "v_mfma_f32_16x16x32_bf16 v[192:195], a[204:207], a[36:39], v[192:195]\n"
      "ds_read_b64_tr_b16 a[222:223], v25 offset:22528\n"
      "s_add_u32 s88, s77, s82\n"
      "s_add_u32 m0, s85, 0x24000\n"
      "v_mfma_f32_16x16x32_bf16 v[196:199], a[204:207], a[40:43], v[196:199]\n"
      "buffer_load_dwordx4 v10, s[24:27], s88 offen lds\n"
      "v_mfma_f32_16x16x32_bf16 v[196:199], a[200:203], a[28:31], v[196:199]\n"
      "v_mfma_f32_16x16x32_bf16 v[196:199], a[196:199], v[172:175], v[196:199]\n"
      "buffer_load_dwordx4 v11, s[24:27], s88 offen offset:1024 lds\n"
      "v_mfma_f32_16x16x32_bf16 v[196:199], a[192:195], v[112:115], v[196:199]\n"
      "v_mfma_f32_16x16x32_bf16 v[200:203], a[192:195], v[116:119], v[200:203]\n"
      "buffer_load_dwordx4 v12, s[24:27], s88 offen offset:2048 lds\n"
      "v_mfma_f32_16x16x32_bf16 v[200:203], a[196:199], v[176:179], v[200:203]\n"
      "v_mfma_f32_16x16x32_bf16 v[200:203], a[200:203], a[32:35], v[200:203]\n"
      "buffer_load_dwordx4 v13, s[24:27], s88 offen offset:3072 lds\n"
      "v_mfma_f32_16x16x32_bf16 v[200:203], a[204:207], a[44:47], v[200:203]\n"
      "s_waitcnt lgkmcnt(6)\n"
      "v_mfma_f32_16x16x32_bf16 v[204:207], a[208:211], v[108:111], v[204:207]\n"
      "ds_read_b64_tr_b16 a[192:193], v14 offset:49152\n"
      "s_waitcnt lgkmcnt(5)\n"
      "v_mfma_f32_16x16x32_bf16 v[204:207], a[212:215], v[168:171], v[204:207]\n"
      "ds_read_b64_tr_b16 a[194:195], v14 offset:51200\n"
      "s_waitcnt lgkmcnt(4)\n"
      "v_mfma_f32_16x16x32_bf16 v[204:207], a[216:219], a[24:27], v[204:207]\n"
      "ds_read_b64_tr_b16 a[196:197], v14 offset:53248\n"
      "s_waitcnt lgkmcnt(3)\n"
      "v_mfma_f32_16x16x32_bf16 v[204:207], a[220:223], a[36:39], v[204:207]\n"
      "ds_read_b64_tr_b16 a[198:199], v14 offset:55296\n"
      "v_mfma_f32_16x16x32_bf16 v[208:211], a[220:223], a[40:43], v[208:211]\n"
      "ds_read_b64_tr_b16 a[200:201], v26 offset:49152\n"
      "v_mfma_f32_16x16x32_bf16 v[208:211], a[216:219], a[28:31], v[208:211]\n"
      "ds_read_b64_tr_b16 a[202:203], v26 offset:51200\n"
      "v_mfma_f32_16x16x32_bf16 v[208:211], a[212:215], v[172:175], v[208:211]\n"
      "ds_read_b64_tr_b16 a[204:205], v26 offset:53248\n"
      "v_mfma_f32_16x16x32_bf16 v[208:211], a[208:211], v[112:115], v[208:211]\n"
      "ds_read_b64_tr_b16 a[206:207], v26 offset:55296\n"
      "v_mfma_f32_16x16x32_bf16 v[212:215], a[208:211], v[116:119], v[212:215]\n"
      "ds_read_b64_tr_b16 a[208:209], v15 offset:49152\n"
      "v_mfma_f32_16x16x32_bf16 v[212:215], a[212:215], v[176:179], v[212:215]\n"
      "ds_read_b64_tr_b16 a[210:211], v15 offset:51200\n"
      "v_mfma_f32_16x16x32_bf16 v[212:215], a[216:219], a[32:35], v[212:215]\n"
      "ds_read_b64_tr_b16 a[212:213], v15 offset:53248\n"
      "v_mfma_f32_16x16x32_bf16 v[212:215], a[220:223], a[44:47], v[212:215]\n"
      "ds_read_b64_tr_b16 a[214:215], v15 offset:55296\n"
      "s_waitcnt lgkmcnt(10)\n"
      "v_mfma_f32_16x16x32_bf16 v[192:195], a[192:195], v[180:183], v[192:195]\n"
      "ds_read_b64_tr_b16 a[216:217], v27 offset:49152\n"
      "s_waitcnt lgkmcnt(9)\n"
      "v_mfma_f32_16x16x32_bf16 v[192:195], a[196:199], v[240:243], v[192:195]\n"
      "ds_read_b64_tr_b16 a[218:219], v27 offset:51200\n"
      "s_waitcnt lgkmcnt(8)\n"
      "v_mfma_f32_16x16x32_bf16 v[192:195], a[200:203], a[48:51], v[192:195]\n"
      "ds_read_b64_tr_b16 a[220:221], v27 offset:53248\n"
      "s_waitcnt lgkmcnt(7)\n"
      "v_mfma_f32_16x16x32_bf16 v[192:195], a[204:207], a[60:63], v[192:195]\n"
      "ds_read_b64_tr_b16 a[222:223], v27 offset:55296\n"
      "s_add_u32 s82, s82, 0x80\n"
      "s_cmp_eq_u32 s82, 0x380\n"
      "s_cselect_b32 s82, 0, s82\n"
      "s_add_u32 s88, s74, s82\n"
      "s_add_u32 m0, s85, 0x18000\n"
      "v_mfma_f32_16x16x32_bf16 v[196:199], a[204:207], a[64:67], v[196:199]\n"
      "buffer_load_dwordx4 v10, s[24:27], s88 offen lds\n"
      "v_mfma_f32_16x16x32_bf16 v[196:199], a[200:203], a[52:55], v[196:199]\n"
      "v_mfma_f32_16x16x32_bf16 v[196:199], a[196:199], v[244:247], v[196:199]\n"
      "buffer_load_dwordx4 v11, s[24:27], s88 offen offset:1024 lds\n"
      "v_mfma_f32_16x16x32_bf16 v[196:199], a[192:195], v[184:187], v[196:199]\n"
      "v_mfma_f32_16x16x32_bf16 v[200:203], a[192:195], v[188:191], v[200:203]\n"
      "buffer_load_dwordx4 v12, s[24:27], s88 offen offset:2048 lds\n"
      "v_mfma_f32_16x16x32_bf16 v[200:203], a[196:199], v[248:251], v[200:203]\n"
      "v_mfma_f32_16x16x32_bf16 v[200:203], a[200:203], a[56:59], v[200:203]\n"
      "buffer_load_dwordx4 v13, s[24:27], s88 offen offset:3072 lds\n"
      "v_mfma_f32_16x16x32_bf16 v[200:203], a[204:207], a[68:71], v[200:203]\n"
      "s_waitcnt lgkmcnt(6)\n"
      "v_mfma_f32_16x16x32_bf16 v[204:207], a[208:211], v[180:183], v[204:207]\n"
      "ds_read_b64_tr_b16 a[192:193], v14\n"
      "s_waitcnt lgkmcnt(5)\n"
      "v_mfma_f32_16x16x32_bf16 v[204:207], a[212:215], v[240:243], v[204:207]\n"
      "ds_read_b64_tr_b16 a[194:195], v14 offset:2048\n"
      "s_waitcnt lgkmcnt(4)\n"
      "v_mfma_f32_16x16x32_bf16 v[204:207], a[216:219], a[48:51], v[204:207]\n"
      "ds_read_b64_tr_b16 a[196:197], v14 offset:4096\n"
      "s_waitcnt lgkmcnt(3)\n"
      "v_mfma_f32_16x16x32_bf16 v[204:207], a[220:223], a[60:63], v[204:207]\n"
      "ds_read_b64_tr_b16 a[198:199], v14 offset:6144\n"
      "v_mfma_f32_16x16x32_bf16 v[208:211], a[220:223], a[64:67], v[208:211]\n"
      "ds_read_b64_tr_b16 a[200:201], v26\n"
      "v_mfma_f32_16x16x32_bf16 v[208:211], a[216:219], a[52:55], v[208:211]\n"
      "ds_read_b64_tr_b16 a[202:203], v26 offset:2048\n"
      "v_mfma_f32_16x16x32_bf16 v[208:211], a[212:215], v[244:247], v[208:211]\n"
      "ds_read_b64_tr_b16 a[204:205], v26 offset:4096\n"
      "v_mfma_f32_16x16x32_bf16 v[208:211], a[208:211], v[184:187], v[208:211]\n"
      "ds_read_b64_tr_b16 a[206:207], v26 offset:6144\n"
      "v_mfma_f32_16x16x32_bf16 v[212:215], a[208:211], v[188:191], v[212:215]\n"
      "ds_read_b64_tr_b16 a[208:209], v15\n"
      "v_mfma_f32_16x16x32_bf16 v[212:215], a[212:215], v[248:251], v[212:215]\n"
      "ds_read_b64_tr_b16 a[210:211], v15 offset:2048\n"
      "v_mfma_f32_16x16x32_bf16 v[212:215], a[216:219], a[56:59], v[212:215]\n"
      "ds_read_b64_tr_b16 a[212:213], v15 offset:4096\n"
      "v_mfma_f32_16x16x32_bf16 v[212:215], a[220:223], a[68:71], v[212:215]\n"
      "ds_read_b64_tr_b16 a[214:215], v15 offset:6144\n"
      "s_waitcnt lgkmcnt(10)\n"
      "v_mfma_f32_16x16x32_bf16 v[192:195], a[192:195], v[252:255], v[192:195]\n"
      "ds_read_b64_tr_b16 a[216:217], v27\n"
      "s_waitcnt lgkmcnt(9)\n"
      "v_mfma_f32_16x16x32_bf16 v[192:195], a[196:199], v[224:227], v[192:195]\n"
      "ds_read_b64_tr_b16 a[218:219], v27 offset:2048\n"
      "s_add_u32 s88, s75, s82\n"
      "s_add_u32 m0, s85, 0x8000\n"
      "s_waitcnt lgkmcnt(8)\n"
      "v_mfma_f32_16x16x32_bf16 v[192:195], a[200:203], a[72:75], v[192:195]\n"
      "ds_read_b64_tr_b16 a[220:221], v27 offset:4096\n"
      "buffer_load_dwordx4 v10, s[24:27], s88 offen lds\n"
      "s_waitcnt lgkmcnt(7)\n"
      "v_mfma_f32_16x16x32_bf16 v[192:195], a[204:207], a[84:87], v[192:195]\n"
      "ds_read_b64_tr_b16 a[222:223], v27 offset:6144\n"
      "v_mfma_f32_16x16x32_bf16 v[196:199], a[204:207], a[88:91], v[196:199]\n"
      "buffer_load_dwordx4 v11, s[24:27], s88 offen offset:1024 lds\n"
      "v_mfma_f32_16x16x32_bf16 v[196:199], a[200:203], a[76:79], v[196:199]\n"
      "v_mfma_f32_16x16x32_bf16 v[196:199], a[196:199], v[228:231], v[196:199]\n"
      "buffer_load_dwordx4 v12, s[24:27], s88 offen offset:2048 lds\n"
      "v_mfma_f32_16x16x32_bf16 v[196:199], a[192:195], v[216:219], v[196:199]\n"
      "v_mfma_f32_16x16x32_bf16 v[200:203], a[192:195], v[220:223], v[200:203]\n"
      "buffer_load_dwordx4 v13, s[24:27], s88 offen offset:3072 lds\n"
      "v_mfma_f32_16x16x32_bf16 v[200:203], a[196:199], v[232:235], v[200:203]\n"
      "v_mfma_f32_16x16x32_bf16 v[200:203], a[200:203], a[80:83], v[200:203]\n"
      "s_waitcnt vmcnt(12)\n"
      "buffer_store_dwordx2 v[34:35], v36, s[36:39], s48 offen sc1\n"
      "v_mfma_f32_16x16x32_bf16 v[200:203], a[204:207], a[92:95], v[200:203]\n"
      "s_add_u32 s48, s48, 0x80\n"
      "s_cmp_eq_u32 s48, 0x380\n"
      "s_cselect_b32 s48, 0, s48\n"
      "s_waitcnt lgkmcnt(6)\n"
      "v_mfma_f32_16x16x32_bf16 v[204:207], a[208:211], v[252:255], v[204:207]\n"
      "ds_read_b64_tr_b16 a[192:193], v16\n"
      "s_waitcnt lgkmcnt(5)\n"
      "v_mfma_f32_16x16x32_bf16 v[204:207], a[212:215], v[224:227], v[204:207]\n"
      "ds_read_b64_tr_b16 a[194:195], v16 offset:2048\n"
      "s_waitcnt lgkmcnt(4)\n"
      "v_mfma_f32_16x16x32_bf16 v[204:207], a[216:219], a[72:75], v[204:207]\n"
      "ds_read_b64_tr_b16 a[196:197], v16 offset:4096\n"
      "v_cvt_pk_bf16_f32 v144, v192, v193\n"
      "s_mul_i32 s90, s83, 0x300\n"
      "s_add_u32 s83, s83, 0x80\n"
      "s_cmp_eq_u32 s83, 0x380\n"
      "s_cselect_b32 s83, 0, s83\n"
      "s_waitcnt lgkmcnt(3)\n"
      "v_mfma_f32_16x16x32_bf16 v[204:207], a[220:223], a[84:87], v[204:207]\n"
      "ds_read_b64_tr_b16 a[198:199], v16 offset:6144\n"
      "v_cvt_pk_bf16_f32 v145, v194, v195\n"
      "v_mfma_f32_16x16x32_bf16 v[208:211], a[220:223], a[88:91], v[208:211]\n"
      "ds_read_b64_tr_b16 a[200:201], v28\n"
      "v_cvt_pk_bf16_f32 v148, v196, v197\n"
      "v_mfma_f32_16x16x32_bf16 v[208:211], a[216:219], a[76:79], v[208:211]\n"
      "ds_read_b64_tr_b16 a[202:203], v28 offset:2048\n"
      "v_mfma_f32_16x16x32_bf16 v[208:211], a[212:215], v[228:231], v[208:211]\n"
      "ds_read_b64_tr_b16 a[204:205], v28 offset:4096\n"
      "v_cvt_pk_bf16_f32 v149, v198, v199\n"
      "v_mfma_f32_16x16x32_bf16 v[208:211], a[208:211], v[216:219], v[208:211]\n"
      "ds_read_b64_tr_b16 a[206:207], v28 offset:6144\n"
      "v_cvt_pk_bf16_f32 v152, v200, v201\n"
      "v_mfma_f32_16x16x32_bf16 v[212:215], a[208:211], v[220:223], v[212:215]\n"
      "ds_read_b64_tr_b16 a[208:209], v17\n"
      "v_cvt_pk_bf16_f32 v153, v202, v203\n"
      "v_mfma_f32_16x16x32_bf16 v[212:215], a[212:215], v[232:235], v[212:215]\n"
      "ds_read_b64_tr_b16 a[210:211], v17 offset:2048\n"
      "s_add_u32 s88, s76, s82\n"
      "s_add_u32 m0, s85, 0\n"
      "v_mfma_f32_16x16x32_bf16 v[212:215], a[216:219], a[80:83], v[212:215]\n"
      "ds_read_b64_tr_b16 a[212:213], v17 offset:4096\n"
      "v_cvt_pk_bf16_f32 v146, v204, v205\n"
      "v_cvt_pk_bf16_f32 v147, v206, v207\n"
      "buffer_load_dwordx4 v10, s[24:27], s88 offen lds\n"
      "v_mfma_f32_16x16x32_bf16 v[212:215], a[220:223], a[92:95], v[212:215]\n"
      "ds_read_b64_tr_b16 a[214:215], v17 offset:6144\n"
      "s_waitcnt lgkmcnt(10)\n"
      "v_mfma_f32_16x16x32_bf16 v[192:195], a[192:195], v[60:63], 0\n"
      "ds_read_b64_tr_b16 a[216:217], v29\n"
      "v_cvt_pk_bf16_f32 v150, v208, v209\n"
      "v_cvt_pk_bf16_f32 v151, v210, v211\n"
      "buffer_load_dwordx4 v11, s[24:27], s88 offen offset:1024 lds\n"
      "s_waitcnt lgkmcnt(9)\n"
      "v_mfma_f32_16x16x32_bf16 v[192:195], a[196:199], v[84:87], v[192:195]\n"
      "ds_read_b64_tr_b16 a[218:219], v29 offset:2048\n"
      "s_waitcnt lgkmcnt(8)\n"
      "v_mfma_f32_16x16x32_bf16 v[192:195], a[200:203], a[0:3], v[192:195]\n"
      "ds_read_b64_tr_b16 a[220:221], v29 offset:4096\n"
      "v_cvt_pk_bf16_f32 v154, v212, v213\n"
      "v_cvt_pk_bf16_f32 v155, v214, v215\n"
      "buffer_load_dwordx4 v12, s[24:27], s88 offen offset:2048 lds\n"
      "s_waitcnt lgkmcnt(7)\n"
      "v_mfma_f32_16x16x32_bf16 v[192:195], a[204:207], a[12:15], v[192:195]\n"
      "ds_read_b64_tr_b16 a[222:223], v29 offset:6144\n"
      "v_mfma_f32_16x16x32_bf16 v[196:199], a[204:207], a[16:19], 0\n"
      "buffer_load_dwordx4 v13, s[24:27], s88 offen offset:3072 lds\n"
      "v_mfma_f32_16x16x32_bf16 v[196:199], a[200:203], a[4:7], v[196:199]\n"
      "v_mfma_f32_16x16x32_bf16 v[196:199], a[196:199], v[88:91], v[196:199]\n"
      "buffer_store_dwordx4 v[144:147], v32, s[36:39], s90 offen sc1\n"
      "v_mfma_f32_16x16x32_bf16 v[196:199], a[192:195], v[64:67], v[196:199]\n"
      "v_mfma_f32_16x16x32_bf16 v[200:203], a[192:195], v[68:71], 0\n"
      "buffer_store_dwordx4 v[148:151], v32, s[36:39], s90 offen offset:1024 sc1\n"
      "v_mfma_f32_16x16x32_bf16 v[200:203], a[196:199], v[92:95], v[200:203]\n"
      "v_mfma_f32_16x16x32_bf16 v[200:203], a[200:203], a[8:11], v[200:203]\n"
      "buffer_store_dwordx4 v[152:155], v32, s[36:39], s90 offen offset:2048 sc1\n"
      "v_mfma_f32_16x16x32_bf16 v[200:203], a[204:207], a[20:23], v[200:203]\n"
      "s_waitcnt vmcnt(12) lgkmcnt(0)\n"
      "s_barrier\n"
      "v_mfma_f32_16x16x32_bf16 v[204:207], a[208:211], v[60:63], 0\n"
      "ds_read_b64_tr_b16 a[192:193], v14 offset:16384\n"
      "v_mfma_f32_16x16x32_bf16 v[204:207], a[212:215], v[84:87], v[204:207]\n"
      "ds_read_b64_tr_b16 a[194:195], v14 offset:18432\n"
      "v_mfma_f32_16x16x32_bf16 v[204:207], a[216:219], a[0:3], v[204:207]\n"
      "ds_read_b64_tr_b16 a[196:197], v14 offset:20480\n"
      "v_mfma_f32_16x16x32_bf16 v[204:207], a[220:223], a[12:15], v[204:207]\n"
      "ds_read_b64_tr_b16 a[198:199], v14 offset:22528\n"
      "v_mfma_f32_16x16x32_bf16 v[208:211], a[220:223], a[16:19], 0\n"
      "ds_read_b64_tr_b16 a[200:201], v26 offset:16384\n"
      "v_mfma_f32_16x16x32_bf16 v[208:211], a[216:219], a[4:7], v[208:211]\n"
      "ds_read_b64_tr_b16 a[202:203], v26 offset:18432\n"
      "v_mfma_f32_16x16x32_bf16 v[208:211], a[212:215], v[88:91], v[208:211]\n"
      "ds_read_b64_tr_b16 a[204:205], v26 offset:20480\n"
      "v_mfma_f32_16x16x32_bf16 v[208:211], a[208:211], v[64:67], v[208:211]\n"
      "ds_read_b64_tr_b16 a[206:207], v26 offset:22528\n"
      "v_mfma_f32_16x16x32_bf16 v[212:215], a[208:211], v[68:71], 0\n"
      "ds_read_b64_tr_b16 a[208:209], v15 offset:16384\n"
      "v_mfma_f32_16x16x32_bf16 v[212:215], a[212:215], v[92:95], v[212:215]\n"
      "ds_read_b64_tr_b16 a[210:211], v15 offset:18432\n"
      "v_mfma_f32_16x16x32_bf16 v[212:215], a[216:219], a[8:11], v[212:215]\n"
      "ds_read_b64_tr_b16 a[212:213], v15 offset:20480\n"
      "v_mfma_f32_16x16x32_bf16 v[212:215], a[220:223], a[20:23], v[212:215]\n"
      "ds_read_b64_tr_b16 a[214:215], v15 offset:22528\n"
      "s_waitcnt lgkmcnt(10)\n"
      "v_mfma_f32_16x16x32_bf16 v[192:195], a[192:195], v[108:111], v[192:195]\n"
      "ds_read_b64_tr_b16 a[216:217], v27 offset:16384\n"
      "s_waitcnt lgkmcnt(9)\n"
      "v_mfma_f32_16x16x32_bf16 v[192:195], a[196:199], v[168:171], v[192:195]\n"
      "ds_read_b64_tr_b16 a[218:219], v27 offset:18432\n"
      "s_waitcnt lgkmcnt(8)\n"
      "v_mfma_f32_16x16x32_bf16 v[192:195], a[200:203], a[24:27], v[192:195]\n"
      "ds_read_b64_tr_b16 a[220:221], v27 offset:20480\n"
      "s_waitcnt lgkmcnt(7)\n"
      "v_mfma_f32_16x16x32_bf16 v[192:195], a[204:207], a[36:39], v[192:195]\n"
      "ds_read_b64_tr_b16 a[222:223], v27 offset:22528\n"
      "s_add_u32 s88, s77, s82\n"
      "s_add_u32 m0, s85, 0x4000\n"
      "v_mfma_f32_16x16x32_bf16 v[196:199], a[204:207], a[40:43], v[196:199]\n"
      "buffer_load_dwordx4 v10, s[24:27], s88 offen lds\n"
      "v_mfma_f32_16x16x32_bf16 v[196:199], a[200:203], a[28:31], v[196:199]\n"
      "v_mfma_f32_16x16x32_bf16 v[196:199], a[196:199], v[172:175], v[196:199]\n"
      "buffer_load_dwordx4 v11, s[24:27], s88 offen offset:1024 lds\n"
      "v_mfma_f32_16x16x32_bf16 v[196:199], a[192:195], v[112:115], v[196:199]\n"
      "v_mfma_f32_16x16x32_bf16 v[200:203], a[192:195], v[116:119], v[200:203]\n"
      "buffer_load_dwordx4 v12, s[24:27], s88 offen offset:2048 lds\n"
      "v_mfma_f32_16x16x32_bf16 v[200:203], a[196:199], v[176:179], v[200:203]\n"
      "v_mfma_f32_16x16x32_bf16 v[200:203], a[200:203], a[32:35], v[200:203]\n"
      "buffer_load_dwordx4 v13, s[24:27], s88 offen offset:3072 lds\n"
      "v_mfma_f32_16x16x32_bf16 v[200:203], a[204:207], a[44:47], v[200:203]\n"
      "s_waitcnt lgkmcnt(6)\n"
      "v_mfma_f32_16x16x32_bf16 v[204:207], a[208:211], v[108:111], v[204:207]\n"
      "ds_read_b64_tr_b16 a[192:193], v18 offset:49152\n"
      "s_waitcnt lgkmcnt(5)\n"
      "v_mfma_f32_16x16x32_bf16 v[204:207], a[212:215], v[168:171], v[204:207]\n"
      "ds_read_b64_tr_b16 a[194:195], v18 offset:51200\n"
      "s_waitcnt lgkmcnt(4)\n"
      "v_mfma_f32_16x16x32_bf16 v[204:207], a[216:219], a[24:27], v[204:207]\n"
      "ds_read_b64_tr_b16 a[196:197], v18 offset:53248\n"
      "s_waitcnt lgkmcnt(3)\n"
      "v_mfma_f32_16x16x32_bf16 v[204:207], a[220:223], a[36:39], v[204:207]\n"
      "ds_read_b64_tr_b16 a[198:199], v18 offset:55296\n"
      "v_mfma_f32_16x16x32_bf16 v[208:211], a[220:223], a[40:43], v[208:211]\n"
      "ds_read_b64_tr_b16 a[200:201], v24 offset:49152\n"
      "v_mfma_f32_16x16x32_bf16 v[208:211], a[216:219], a[28:31], v[208:211]\n"
      "ds_read_b64_tr_b16 a[202:203], v24 offset:51200\n"
      "v_mfma_f32_16x16x32_bf16 v[208:211], a[212:215], v[172:175], v[208:211]\n"
      "ds_read_b64_tr_b16 a[204:205], v24 offset:53248\n"
      "v_mfma_f32_16x16x32_bf16 v[208:211], a[208:211], v[112:115], v[208:211]\n"
      "ds_read_b64_tr_b16 a[206:207], v24 offset:55296\n"
      "v_mfma_f32_16x16x32_bf16 v[212:215], a[208:211], v[116:119], v[212:215]\n"
      "ds_read_b64_tr_b16 a[208:209], v19 offset:49152\n"
      "v_mfma_f32_16x16x32_bf16 v[212:215], a[212:215], v[176:179], v[212:215]\n"
      "ds_read_b64_tr_b16 a[210:211], v19 offset:51200\n"
      "v_mfma_f32_16x16x32_bf16 v[212:215], a[216:219], a[32:35], v[212:215]\n"
      "ds_read_b64_tr_b16 a[212:213], v19 offset:53248\n"
      "v_mfma_f32_16x16x32_bf16 v[212:215], a[220:223], a[44:47], v[212:215]\n"
      "ds_read_b64_tr_b16 a[214:215], v19 offset:55296\n"
      "s_waitcnt lgkmcnt(10)\n"
      "v_mfma_f32_16x16x32_bf16 v[192:195], a[192:195], v[180:183], v[192:195]\n"
      "ds_read_b64_tr_b16 a[216:217], v25 offset:49152\n"
      "s_waitcnt lgkmcnt(9)\n"
      "v_mfma_f32_16x16x32_bf16 v[192:195], a[196:199], v[240:243], v[192:195]\n"
      "ds_read_b64_tr_b16 a[218:219], v25 offset:51200\n"
      "s_waitcnt lgkmcnt(8)\n"
      "v_mfma_f32_16x16x32_bf16 v[192:195], a[200:203], a[48:51], v[192:195]\n"
      "ds_read_b64_tr_b16 a[220:221], v25 offset:53248\n"
      "s_waitcnt lgkmcnt(7)\n"
      "v_mfma_f32_16x16x32_bf16 v[192:195], a[204:207], a[60:63], v[192:195]\n"
      "ds_read_b64_tr_b16 a[222:223], v25 offset:55296\n"
      "s_add_u32 s82, s82, 0x80\n"
      "s_cmp_eq_u32 s82, 0x380\n"
      "s_cselect_b32 s82, 0, s82\n"
      "s_add_u32 s88, s74, s82\n"
      "s_add_u32 m0, s85, 0x1c000\n"
      "v_mfma_f32_16x16x32_bf16 v[196:199], a[204:207], a[64:67], v[196:199]\n"
      "buffer_load_dwordx4 v10, s[24:27], s88 offen lds\n"
      "v_mfma_f32_16x16x32_bf16 v[196:199], a[200:203], a[52:55], v[196:199]\n"
      "v_mfma_f32_16x16x32_bf16 v[196:199], a[196:199], v[244:247], v[196:199]\n"
      "buffer_load_dwordx4 v11, s[24:27], s88 offen offset:1024 lds\n"
      "v_mfma_f32_16x16x32_bf16 v[196:199], a[192:195], v[184:187], v[196:199]\n"
      "v_mfma_f32_16x16x32_bf16 v[200:203], a[192:195], v[188:191], v[200:203]\n"
      "buffer_load_dwordx4 v12, s[24:27], s88 offen offset:2048 lds\n"
      "v_mfma_f32_16x16x32_bf16 v[200:203], a[196:199], v[248:251], v[200:203]\n"
      "v_mfma_f32_16x16x32_bf16 v[200:203], a[200:203], a[56:59], v[200:203]\n"
      "buffer_load_dwordx4 v13, s[24:27], s88 offen offset:3072 lds\n"
      "v_mfma_f32_16x16x32_bf16 v[200:203], a[204:207], a[68:71], v[200:203]\n"
      "s_waitcnt lgkmcnt(6)\n"
      "v_mfma_f32_16x16x32_bf16 v[204:207], a[208:211], v[180:183], v[204:207]\n"
      "ds_read_b64_tr_b16 a[192:193], v16 offset:16384\n"
      "s_waitcnt lgkmcnt(5)\n"
      "v_mfma_f32_16x16x32_bf16 v[204:207], a[212:215], v[240:243], v[204:207]\n"
      "ds_read_b64_tr_b16 a[194:195], v16 offset:18432\n"
      "s_waitcnt lgkmcnt(4)\n"
      "v_mfma_f32_16x16x32_bf16 v[204:207], a[216:219], a[48:51], v[204:207]\n"
      "ds_read_b64_tr_b16 a[196:197], v16 offset:20480\n"
      "s_waitcnt lgkmcnt(3)\n"
      "v_mfma_f32_16x16x32_bf16 v[204:207], a[220:223], a[60:63], v[204:207]\n"
      "ds_read_b64_tr_b16 a[198:199], v16 offset:22528\n"
      "v_mfma_f32_16x16x32_bf16 v[208:211], a[220:223], a[64:67], v[208:211]\n"
      "ds_read_b64_tr_b16 a[200:201], v28 offset:16384\n"
      "v_mfma_f32_16x16x32_bf16 v[208:211], a[216:219], a[52:55], v[208:211]\n"
      "ds_read_b64_tr_b16 a[202:203], v28 offset:18432\n"
      "v_mfma_f32_16x16x32_bf16 v[208:211], a[212:215], v[244:247], v[208:211]\n"
      "ds_read_b64_tr_b16 a[204:205], v28 offset:20480\n"
      "v_mfma_f32_16x16x32_bf16 v[208:211], a[208:211], v[184:187], v[208:211]\n"
      "ds_read_b64_tr_b16 a[206:207], v28 offset:22528\n"
      "v_mfma_f32_16x16x32_bf16 v[212:215], a[208:211], v[188:191], v[212:215]\n"
      "ds_read_b64_tr_b16 a[208:209], v17 offset:16384\n"
      "v_mfma_f32_16x16x32_bf16 v[212:215], a[212:215], v[248:251], v[212:215]\n"
      "ds_read_b64_tr_b16 a[210:211], v17 offset:18432\n"
      "v_mfma_f32_16x16x32_bf16 v[212:215], a[216:219], a[56:59], v[212:215]\n"
      "ds_read_b64_tr_b16 a[212:213], v17 offset:20480\n"
      "v_mfma_f32_16x16x32_bf16 v[212:215], a[220:223], a[68:71], v[212:215]\n"
      "ds_read_b64_tr_b16 a[214:215], v17 offset:22528\n"
      "s_waitcnt lgkmcnt(10)\n"
      "v_mfma_f32_16x16x32_bf16 v[192:195], a[192:195], v[252:255], v[192:195]\n"
      "ds_read_b64_tr_b16 a[216:217], v29 offset:16384\n"
      "s_waitcnt lgkmcnt(9)\n"
      "v_mfma_f32_16x16x32_bf16 v[192:195], a[196:199], v[224:227], v[192:195]\n"
      "ds_read_b64_tr_b16 a[218:219], v29 offset:18432\n"
      "s_add_u32 s88, s75, s82\n"
      "s_add_u32 m0, s85, 0x10000\n"
      "s_waitcnt lgkmcnt(8)\n"
      "v_mfma_f32_16x16x32_bf16 v[192:195], a[200:203], a[72:75], v[192:195]\n"
      "ds_read_b64_tr_b16 a[220:221], v29 offset:20480\n"
      "buffer_load_dwordx4 v10, s[24:27], s88 offen lds\n"
      "s_waitcnt lgkmcnt(7)\n"
      "v_mfma_f32_16x16x32_bf16 v[192:195], a[204:207], a[84:87], v[192:195]\n"
      "ds_read_b64_tr_b16 a[222:223], v29 offset:22528\n"
      "v_mfma_f32_16x16x32_bf16 v[196:199], a[204:207], a[88:91], v[196:199]\n"
      "buffer_load_dwordx4 v11, s[24:27], s88 offen offset:1024 lds\n"
      "v_mfma_f32_16x16x32_bf16 v[196:199], a[200:203], a[76:79], v[196:199]\n"
      "v_mfma_f32_16x16x32_bf16 v[196:199], a[196:199], v[228:231], v[196:199]\n"
      "buffer_load_dwordx4 v12, s[24:27], s88 offen offset:2048 lds\n"
      "v_mfma_f32_16x16x32_bf16 v[196:199], a[192:195], v[216:219], v[196:199]\n"
      "v_mfma_f32_16x16x32_bf16 v[200:203], a[192:195], v[220:223], v[200:203]\n"
      "buffer_load_dwordx4 v13, s[24:27], s88 offen offset:3072 lds\n"
      "v_mfma_f32_16x16x32_bf16 v[200:203], a[196:199], v[232:235], v[200:203]\n"
      "v_mfma_f32_16x16x32_bf16 v[200:203], a[200:203], a[80:83], v[200:203]\n"
      "s_waitcnt vmcnt(12)\n"
      "buffer_store_dwordx2 v[34:35], v36, s[36:39], s48 offen sc1\n"
      "v_mfma_f32_16x16x32_bf16 v[200:203], a[204:207], a[92:95], v[200:203]\n"
      "s_add_u32 s48, s48, 0x80\n"
      "s_cmp_eq_u32 s48, 0x380\n"
      "s_cselect_b32 s48, 0, s48\n"
      "s_waitcnt lgkmcnt(6)\n"
      "v_mfma_f32_16x16x32_bf16 v[204:207], a[208:211], v[252:255], v[204:207]\n"
      "ds_read_b64_tr_b16 a[192:193], v14 offset:32768\n"
      "s_waitcnt lgkmcnt(5)\n"
      "v_mfma_f32_16x16x32_bf16 v[204:207], a[212:215], v[224:227], v[204:207]\n"
      "ds_read_b64_tr_b16 a[194:195], v14 offset:34816\n"
      "s_waitcnt lgkmcnt(4)\n"
      "v_mfma_f32_16x16x32_bf16 v[204:207], a[216:219], a[72:75], v[204:207]\n"
      "ds_read_b64_tr_b16 a[196:197], v14 offset:36864\n"
      "v_cvt_pk_bf16_f32 v156, v192, v193\n"
      "s_mul_i32 s90, s83, 0x300\n"
      "s_add_u32 s83, s83, 0x80\n"
      "s_cmp_eq_u32 s83, 0x380\n"
      "s_cselect_b32 s83, 0, s83\n"
      "s_waitcnt lgkmcnt(3)\n"
      "v_mfma_f32_16x16x32_bf16 v[204:207], a[220:223], a[84:87], v[204:207]\n"
      "ds_read_b64_tr_b16 a[198:199], v14 offset:38912\n"
      "v_cvt_pk_bf16_f32 v157, v194, v195\n"
      "v_mfma_f32_16x16x32_bf16 v[208:211], a[220:223], a[88:91], v[208:211]\n"
      "ds_read_b64_tr_b16 a[200:201], v26 offset:32768\n"
      "v_cvt_pk_bf16_f32 v160, v196, v197\n"
      "v_mfma_f32_16x16x32_bf16 v[208:211], a[216:219], a[76:79], v[208:211]\n"
      "ds_read_b64_tr_b16 a[202:203], v26 offset:34816\n"
      "v_mfma_f32_16x16x32_bf16 v[208:211], a[212:215], v[228:231], v[208:211]\n"
      "ds_read_b64_tr_b16 a[204:205], v26 offset:36864\n"
      "v_cvt_pk_bf16_f32 v161, v198, v199\n"
      "v_mfma_f32_16x16x32_bf16 v[208:211], a[208:211], v[216:219], v[208:211]\n"
      "ds_read_b64_tr_b16 a[206:207], v26 offset:38912\n"
      "v_cvt_pk_bf16_f32 v164, v200, v201\n"
      "v_mfma_f32_16x16x32_bf16 v[212:215], a[208:211], v[220:223], v[212:215]\n"
      "ds_read_b64_tr_b16 a[208:209], v15 offset:32768\n"
      "v_cvt_pk_bf16_f32 v165, v202, v203\n"
      "v_mfma_f32_16x16x32_bf16 v[212:215], a[212:215], v[232:235], v[212:215]\n"
      "ds_read_b64_tr_b16 a[210:211], v15 offset:34816\n"
      "s_add_u32 s88, s76, s82\n"
      "s_add_u32 m0, s85, 0x20000\n"
      "v_mfma_f32_16x16x32_bf16 v[212:215], a[216:219], a[80:83], v[212:215]\n"
      "ds_read_b64_tr_b16 a[212:213], v15 offset:36864\n"
      "v_cvt_pk_bf16_f32 v158, v204, v205\n"
      "v_cvt_pk_bf16_f32 v159, v206, v207\n"
      "buffer_load_dwordx4 v10, s[24:27], s88 offen lds\n"
      "v_mfma_f32_16x16x32_bf16 v[212:215], a[220:223], a[92:95], v[212:215]\n"
      "ds_read_b64_tr_b16 a[214:215], v15 offset:38912\n"
      "s_waitcnt lgkmcnt(10)\n"
      "v_mfma_f32_16x16x32_bf16 v[192:195], a[192:195], v[60:63], 0\n"
      "ds_read_b64_tr_b16 a[216:217], v27 offset:32768\n"
      "v_cvt_pk_bf16_f32 v162, v208, v209\n"
      "v_cvt_pk_bf16_f32 v163, v210, v211\n"
      "buffer_load_dwordx4 v11, s[24:27], s88 offen offset:1024 lds\n"
      "s_waitcnt lgkmcnt(9)\n"
      "v_mfma_f32_16x16x32_bf16 v[192:195], a[196:199], v[84:87], v[192:195]\n"
      "ds_read_b64_tr_b16 a[218:219], v27 offset:34816\n"
      "s_waitcnt lgkmcnt(8)\n"
      "v_mfma_f32_16x16x32_bf16 v[192:195], a[200:203], a[0:3], v[192:195]\n"
      "ds_read_b64_tr_b16 a[220:221], v27 offset:36864\n"
      "v_cvt_pk_bf16_f32 v166, v212, v213\n"
      "v_cvt_pk_bf16_f32 v167, v214, v215\n"
      "buffer_load_dwordx4 v12, s[24:27], s88 offen offset:2048 lds\n"
      "s_waitcnt lgkmcnt(7)\n"
      "v_mfma_f32_16x16x32_bf16 v[192:195], a[204:207], a[12:15], v[192:195]\n"
      "ds_read_b64_tr_b16 a[222:223], v27 offset:38912\n"
      "v_mfma_f32_16x16x32_bf16 v[196:199], a[204:207], a[16:19], 0\n"
      "buffer_load_dwordx4 v13, s[24:27], s88 offen offset:3072 lds\n"
      "v_mfma_f32_16x16x32_bf16 v[196:199], a[200:203], a[4:7], v[196:199]\n"
      "v_mfma_f32_16x16x32_bf16 v[196:199], a[196:199], v[88:91], v[196:199]\n"
      "buffer_store_dwordx4 v[156:159], v32, s[36:39], s90 offen sc1\n"
      "v_mfma_f32_16x16x32_bf16 v[196:199], a[192:195], v[64:67], v[196:199]\n"
      "v_mfma_f32_16x16x32_bf16 v[200:203], a[192:195], v[68:71], 0\n"
      "buffer_store_dwordx4 v[160:163], v32, s[36:39], s90 offen offset:1024 sc1\n"
      "v_mfma_f32_16x16x32_bf16 v[200:203], a[196:199], v[92:95], v[200:203]\n"
      "v_mfma_f32_16x16x32_bf16 v[200:203], a[200:203], a[8:11], v[200:203]\n"
      "buffer_store_dwordx4 v[164:167], v32, s[36:39], s90 offen offset:2048 sc1\n"
      "v_mfma_f32_16x16x32_bf16 v[200:203], a[204:207], a[20:23], v[200:203]\n"
      "s_waitcnt vmcnt(12) lgkmcnt(0)\n"
      "s_barrier\n"
      "v_mfma_f32_16x16x32_bf16 v[204:207], a[208:211], v[60:63], 0\n"
      "ds_read_b64_tr_b16 a[192:193], v18 offset:32768\n"
      "v_mfma_f32_16x16x32_bf16 v[204:207], a[212:215], v[84:87], v[204:207]\n"
      "ds_read_b64_tr_b16 a[194:195], v18 offset:34816\n"
      "v_mfma_f32_16x16x32_bf16 v[204:207], a[216:219], a[0:3], v[204:207]\n"
      "ds_read_b64_tr_b16 a[196:197], v18 offset:36864\n"
      "v_mfma_f32_16x16x32_bf16 v[204:207], a[220:223], a[12:15], v[204:207]\n"
      "ds_read_b64_tr_b16 a[198:199], v18 offset:38912\n"
      "v_mfma_f32_16x16x32_bf16 v[208:211], a[220:223], a[16:19], 0\n"
      "ds_read_b64_tr_b16 a[200:201], v24 offset:32768\n"
      "v_mfma_f32_16x16x32_bf16 v[208:211], a[216:219], a[4:7], v[208:211]\n"
      "ds_read_b64_tr_b16 a[202:203], v24 offset:34816\n"
      "v_mfma_f32_16x16x32_bf16 v[208:211], a[212:215], v[88:91], v[208:211]\n"
      "ds_read_b64_tr_b16 a[204:205], v24 offset:36864\n"
      "v_mfma_f32_16x16x32_bf16 v[208:211], a[208:211], v[64:67], v[208:211]\n"
      "ds_read_b64_tr_b16 a[206:207], v24 offset:38912\n"
      "v_mfma_f32_16x16x32_bf16 v[212:215], a[208:211], v[68:71], 0\n"
      "ds_read_b64_tr_b16 a[208:209], v19 offset:32768\n"
      "v_mfma_f32_16x16x32_bf16 v[212:215], a[212:215], v[92:95], v[212:215]\n"
      "ds_read_b64_tr_b16 a[210:211], v19 offset:34816\n"
      "v_mfma_f32_16x16x32_bf16 v[212:215], a[216:219], a[8:11], v[212:215]\n"
      "ds_read_b64_tr_b16 a[212:213], v19 offset:36864\n"
      "v_mfma_f32_16x16x32_bf16 v[212:215], a[220:223], a[20:23], v[212:215]\n"
      "ds_read_b64_tr_b16 a[214:215], v19 offset:38912\n"
      "s_waitcnt lgkmcnt(10)\n"
      "v_mfma_f32_16x16x32_bf16 v[192:195], a[192:195], v[108:111], v[192:195]\n"
      "ds_read_b64_tr_b16 a[216:217], v25 offset:32768\n"
      "s_waitcnt lgkmcnt(9)\n"
      "v_mfma_f32_16x16x32_bf16 v[192:195], a[196:199], v[168:171], v[192:195]\n"
      "ds_read_b64_tr_b16 a[218:219], v25 offset:34816\n"
      "s_waitcnt lgkmcnt(8)\n"
      "v_mfma_f32_16x16x32_bf16 v[192:195], a[200:203], a[24:27], v[192:195]\n"
      "ds_read_b64_tr_b16 a[220:221], v25 offset:36864\n"
      "s_waitcnt lgkmcnt(7)\n"
      "v_mfma_f32_16x16x32_bf16 v[192:195], a[204:207], a[36:39], v[192:195]\n"
      "ds_read_b64_tr_b16 a[222:223], v25 offset:38912\n"
      "s_add_u32 s88, s77, s82\n"
      "s_add_u32 m0, s85, 0x14000\n"
      "v_mfma_f32_16x16x32_bf16 v[196:199], a[204:207], a[40:43], v[196:199]\n"
      "buffer_load_dwordx4 v10, s[24:27], s88 offen lds\n"
      "v_mfma_f32_16x16x32_bf16 v[196:199], a[200:203], a[28:31], v[196:199]\n"
      "v_mfma_f32_16x16x32_bf16 v[196:199], a[196:199], v[172:175], v[196:199]\n"
      "buffer_load_dwordx4 v11, s[24:27], s88 offen offset:1024 lds\n"
      "v_mfma_f32_16x16x32_bf16 v[196:199], a[192:195], v[112:115], v[196:199]\n"
      "v_mfma_f32_16x16x32_bf16 v[200:203], a[192:195], v[116:119], v[200:203]\n"
      "buffer_load_dwordx4 v12, s[24:27], s88 offen offset:2048 lds\n"
      "v_mfma_f32_16x16x32_bf16 v[200:203], a[196:199], v[176:179], v[200:203]\n"
      "v_mfma_f32_16x16x32_bf16 v[200:203], a[200:203], a[32:35], v[200:203]\n"
      "buffer_load_dwordx4 v13, s[24:27], s88 offen offset:3072 lds\n"
      "v_mfma_f32_16x16x32_bf16 v[200:203], a[204:207], a[44:47], v[200:203]\n"
      "s_waitcnt lgkmcnt(6)\n"
      "v_mfma_f32_16x16x32_bf16 v[204:207], a[208:211], v[108:111], v[204:207]\n"
      "ds_read_b64_tr_b16 a[192:193], v18\n"
      "s_waitcnt lgkmcnt(5)\n"
      "v_mfma_f32_16x16x32_bf16 v[204:207], a[212:215], v[168:171], v[204:207]\n"
      "ds_read_b64_tr_b16 a[194:195], v18 offset:2048\n"
      "s_waitcnt lgkmcnt(4)\n"
      "v_mfma_f32_16x16x32_bf16 v[204:207], a[216:219], a[24:27], v[204:207]\n"
      "ds_read_b64_tr_b16 a[196:197], v18 offset:4096\n"
      "s_waitcnt lgkmcnt(3)\n"
      "v_mfma_f32_16x16x32_bf16 v[204:207], a[220:223], a[36:39], v[204:207]\n"
      "ds_read_b64_tr_b16 a[198:199], v18 offset:6144\n"
      "v_mfma_f32_16x16x32_bf16 v[208:211], a[220:223], a[40:43], v[208:211]\n"
      "ds_read_b64_tr_b16 a[200:201], v24\n"
      "v_mfma_f32_16x16x32_bf16 v[208:211], a[216:219], a[28:31], v[208:211]\n"
      "ds_read_b64_tr_b16 a[202:203], v24 offset:2048\n"
      "v_mfma_f32_16x16x32_bf16 v[208:211], a[212:215], v[172:175], v[208:211]\n"
      "ds_read_b64_tr_b16 a[204:205], v24 offset:4096\n"
      "v_mfma_f32_16x16x32_bf16 v[208:211], a[208:211], v[112:115], v[208:211]\n"
      "ds_read_b64_tr_b16 a[206:207], v24 offset:6144\n"
      "v_mfma_f32_16x16x32_bf16 v[212:215], a[208:211], v[116:119], v[212:215]\n"
      "ds_read_b64_tr_b16 a[208:209], v19\n"
      "v_mfma_f32_16x16x32_bf16 v[212:215], a[212:215], v[176:179], v[212:215]\n"
      "ds_read_b64_tr_b16 a[210:211], v19 offset:2048\n"
      "v_mfma_f32_16x16x32_bf16 v[212:215], a[216:219], a[32:35], v[212:215]\n"
      "ds_read_b64_tr_b16 a[212:213], v19 offset:4096\n"
      "v_mfma_f32_16x16x32_bf16 v[212:215], a[220:223], a[44:47], v[212:215]\n"
      "ds_read_b64_tr_b16 a[214:215], v19 offset:6144\n"
      "s_waitcnt lgkmcnt(10)\n"
      "v_mfma_f32_16x16x32_bf16 v[192:195], a[192:195], v[180:183], v[192:195]\n"
      "ds_read_b64_tr_b16 a[216:217], v25\n"
      "s_waitcnt lgkmcnt(9)\n"
      "v_mfma_f32_16x16x32_bf16 v[192:195], a[196:199], v[240:243], v[192:195]\n"
      "ds_read_b64_tr_b16 a[218:219], v25 offset:2048\n"
      "s_waitcnt lgkmcnt(8)\n"
      "v_mfma_f32_16x16x32_bf16 v[192:195], a[200:203], a[48:51], v[192:195]\n"
      "ds_read_b64_tr_b16 a[220:221], v25 offset:4096\n"
      "s_waitcnt lgkmcnt(7)\n"
      "v_mfma_f32_16x16x32_bf16 v[192:195], a[204:207], a[60:63], v[192:195]\n"
      "ds_read_b64_tr_b16 a[222:223], v25 offset:6144\n"
      "s_add_u32 s82, s82, 0x80\n"
      "s_cmp_eq_u32 s82, 0x380\n"
      "s_cselect_b32 s82, 0, s82\n"
      "s_add_u32 s88, s74, s82\n"
      "s_add_u32 m0, s85, 0xc000\n"
      "v_mfma_f32_16x16x32_bf16 v[196:199], a[204:207], a[64:67], v[196:199]\n"
      "buffer_load_dwordx4 v10, s[24:27], s88 offen lds\n"
      "v_mfma_f32_16x16x32_bf16 v[196:199], a[200:203], a[52:55], v[196:199]\n"
      "v_mfma_f32_16x16x32_bf16 v[196:199], a[196:199], v[244:247], v[196:199]\n"
      "buffer_load_dwordx4 v11, s[24:27], s88 offen offset:1024 lds\n"
      "v_mfma_f32_16x16x32_bf16 v[196:199], a[192:195], v[184:187], v[196:199]\n"
      "v_mfma_f32_16x16x32_bf16 v[200:203], a[192:195], v[188:191], v[200:203]\n"
      "buffer_load_dwordx4 v12, s[24:27], s88 offen offset:2048 lds\n"
      "v_mfma_f32_16x16x32_bf16 v[200:203], a[196:199], v[248:251], v[200:203]\n"
      "v_mfma_f32_16x16x32_bf16 v[200:203], a[200:203], a[56:59], v[200:203]\n"
      "buffer_load_dwordx4 v13, s[24:27], s88 offen offset:3072 lds\n"
      "v_mfma_f32_16x16x32_bf16 v[200:203], a[204:207], a[68:71], v[200:203]\n"
      "s_waitcnt lgkmcnt(6)\n"
      "v_mfma_f32_16x16x32_bf16 v[204:207], a[208:211], v[180:183], v[204:207]\n"
      "ds_read_b64_tr_b16 a[192:193], v18 offset:16384\n"
      "s_waitcnt lgkmcnt(5)\n"
      "v_mfma_f32_16x16x32_bf16 v[204:207], a[212:215], v[240:243], v[204:207]\n"
      "ds_read_b64_tr_b16 a[194:195], v18 offset:18432\n"
      "s_waitcnt lgkmcnt(4)\n"
      "v_mfma_f32_16x16x32_bf16 v[204:207], a[216:219], a[48:51], v[204:207]\n"
      "ds_read_b64_tr_b16 a[196:197], v18 offset:20480\n"
      "s_waitcnt lgkmcnt(3)\n"
      "v_mfma_f32_16x16x32_bf16 v[204:207], a[220:223], a[60:63], v[204:207]\n"
      "ds_read_b64_tr_b16 a[198:199], v18 offset:22528\n"
      "v_mfma_f32_16x16x32_bf16 v[208:211], a[220:223], a[64:67], v[208:211]\n"
      "ds_read_b64_tr_b16 a[200:201], v24 offset:16384\n"
      "v_mfma_f32_16x16x32_bf16 v[208:211], a[216:219], a[52:55], v[208:211]\n"
      "ds_read_b64_tr_b16 a[202:203], v24 offset:18432\n"
      "v_mfma_f32_16x16x32_bf16 v[208:211], a[212:215], v[244:247], v[208:211]\n"
      "ds_read_b64_tr_b16 a[204:205], v24 offset:20480\n"
      "v_mfma_f32_16x16x32_bf16 v[208:211], a[208:211], v[184:187], v[208:211]\n"
      "ds_read_b64_tr_b16 a[206:207], v24 offset:22528\n"
      "v_mfma_f32_16x16x32_bf16 v[212:215], a[208:211], v[188:191], v[212:215]\n"
      "ds_read_b64_tr_b16 a[208:209], v19 offset:16384\n"
      "v_mfma_f32_16x16x32_bf16 v[212:215], a[212:215], v[248:251], v[212:215]\n"
      "ds_read_b64_tr_b16 a[210:211], v19 offset:18432\n"
      "v_mfma_f32_16x16x32_bf16 v[212:215], a[216:219], a[56:59], v[212:215]\n"
      "ds_read_b64_tr_b16 a[212:213], v19 offset:20480\n"
      "v_mfma_f32_16x16x32_bf16 v[212:215], a[220:223], a[68:71], v[212:215]\n"
      "ds_read_b64_tr_b16 a[214:215], v19 offset:22528\n"
      "s_waitcnt lgkmcnt(10)\n"
      "v_mfma_f32_16x16x32_bf16 v[192:195], a[192:195], v[252:255], v[192:195]\n"
      "ds_read_b64_tr_b16 a[216:217], v25 offset:16384\n"
      "s_waitcnt lgkmcnt(9)\n"
      "v_mfma_f32_16x16x32_bf16 v[192:195], a[196:199], v[224:227], v[192:195]\n"
      "ds_read_b64_tr_b16 a[218:219], v25 offset:18432\n"
      "s_add_u32 s88, s75, s82\n"
      "s_add_u32 m0, s85, 0x24000\n"
      "s_waitcnt lgkmcnt(8)\n"
      "v_mfma_f32_16x16x32_bf16 v[192:195], a[200:203], a[72:75], v[192:195]\n"
      "ds_read_b64_tr_b16 a[220:221], v25 offset:20480\n"
      "buffer_load_dwordx4 v10, s[24:27], s88 offen lds\n"
      "s_waitcnt lgkmcnt(7)\n"
      "v_mfma_f32_16x16x32_bf16 v[192:195], a[204:207], a[84:87], v[192:195]\n"
      "ds_read_b64_tr_b16 a[222:223], v25 offset:22528\n"
      "v_mfma_f32_16x16x32_bf16 v[196:199], a[204:207], a[88:91], v[196:199]\n"
      "buffer_load_dwordx4 v11, s[24:27], s88 offen offset:1024 lds\n"
      "v_mfma_f32_16x16x32_bf16 v[196:199], a[200:203], a[76:79], v[196:199]\n"
      "v_mfma_f32_16x16x32_bf16 v[196:199], a[196:199], v[228:231], v[196:199]\n"
      "buffer_load_dwordx4 v12, s[24:27], s88 offen offset:2048 lds\n"
      "v_mfma_f32_16x16x32_bf16 v[196:199], a[192:195], v[216:219], v[196:199]\n"
      "v_mfma_f32_16x16x32_bf16 v[200:203], a[192:195], v[220:223], v[200:203]\n"
      "buffer_load_dwordx4 v13, s[24:27], s88 offen offset:3072 lds\n"
      "v_mfma_f32_16x16x32_bf16 v[200:203], a[196:199], v[232:235], v[200:203]\n"
      "v_mfma_f32_16x16x32_bf16 v[200:203], a[200:203], a[80:83], v[200:203]\n"
      "s_waitcnt vmcnt(12)\n"
      "buffer_store_dwordx2 v[34:35], v36, s[36:39], s48 offen sc1\n"
      "v_mfma_f32_16x16x32_bf16 v[200:203], a[204:207], a[92:95], v[200:203]\n"
      "s_add_u32 s48, s48, 0x80\n"
      "s_cmp_eq_u32 s48, 0x380\n"
      "s_cselect_b32 s48, 0, s48\n"
      "s_waitcnt lgkmcnt(6)\n"
      "v_mfma_f32_16x16x32_bf16 v[204:207], a[208:211], v[252:255], v[204:207]\n"
      "ds_read_b64_tr_b16 a[192:193], v14 offset:49152\n"
      "s_waitcnt lgkmcnt(5)\n"
      "v_mfma_f32_16x16x32_bf16 v[204:207], a[212:215], v[224:227], v[204:207]\n"
      "ds_read_b64_tr_b16 a[194:195], v14 offset:51200\n"
      "s_waitcnt lgkmcnt(4)\n"
      "v_mfma_f32_16x16x32_bf16 v[204:207], a[216:219], a[72:75], v[204:207]\n"
      "ds_read_b64_tr_b16 a[196:197], v14 offset:53248\n"
      "v_cvt_pk_bf16_f32 v144, v192, v193\n"
      "s_mul_i32 s90, s83, 0x300\n"
      "s_add_u32 s83, s83, 0x80\n"
      "s_cmp_eq_u32 s83, 0x380\n"
      "s_cselect_b32 s83, 0, s83\n"
      "s_waitcnt lgkmcnt(3)\n"
      "v_mfma_f32_16x16x32_bf16 v[204:207], a[220:223], a[84:87], v[204:207]\n"
      "ds_read_b64_tr_b16 a[198:199], v14 offset:55296\n"
      "v_cvt_pk_bf16_f32 v145, v194, v195\n"
      "v_mfma_f32_16x16x32_bf16 v[208:211], a[220:223], a[88:91], v[208:211]\n"
      "ds_read_b64_tr_b16 a[200:201], v26 offset:49152\n"
      "v_cvt_pk_bf16_f32 v148, v196, v197\n"
      "v_mfma_f32_16x16x32_bf16 v[208:211], a[216:219], a[76:79], v[208:211]\n"
      "ds_read_b64_tr_b16 a[202:203], v26 offset:51200\n"
      "v_mfma_f32_16x16x32_bf16 v[208:211], a[212:215], v[228:231], v[208:211]\n"
      "ds_read_b64_tr_b16 a[204:205], v26 offset:53248\n"
      "v_cvt_pk_bf16_f32 v149, v198, v199\n"
      "v_mfma_f32_16x16x32_bf16 v[208:211], a[208:211], v[216:219], v[208:211]\n"
      "ds_read_b64_tr_b16 a[206:207], v26 offset:55296\n"
      "v_cvt_pk_bf16_f32 v152, v200, v201\n"
      "v_mfma_f32_16x16x32_bf16 v[212:215], a[208:211], v[220:223], v[212:215]\n"
      "ds_read_b64_tr_b16 a[208:209], v15 offset:49152\n"
      "v_cvt_pk_bf16_f32 v153, v202, v203\n"
      "v_mfma_f32_16x16x32_bf16 v[212:215], a[212:215], v[232:235], v[212:215]\n"
      "ds_read_b64_tr_b16 a[210:211], v15 offset:51200\n"
      "s_add_u32 s88, s76, s82\n"
      "s_add_u32 m0, s85, 0x18000\n"
      "v_mfma_f32_16x16x32_bf16 v[212:215], a[216:219], a[80:83], v[212:215]\n"
      "ds_read_b64_tr_b16 a[212:213], v15 offset:53248\n"
      "v_cvt_pk_bf16_f32 v146, v204, v205\n"
      "v_cvt_pk_bf16_f32 v147, v206, v207\n"
      "buffer_load_dwordx4 v10, s[24:27], s88 offen lds\n"
      "v_mfma_f32_16x16x32_bf16 v[212:215], a[220:223], a[92:95], v[212:215]\n"
      "ds_read_b64_tr_b16 a[214:215], v15 offset:55296\n"
      "s_waitcnt lgkmcnt(10)\n"
      "v_mfma_f32_16x16x32_bf16 v[192:195], a[192:195], v[60:63], 0\n"
      "ds_read_b64_tr_b16 a[216:217], v27 offset:49152\n"
      "v_cvt_pk_bf16_f32 v150, v208, v209\n"
      "v_cvt_pk_bf16_f32 v151, v210, v211\n"
      "buffer_load_dwordx4 v11, s[24:27], s88 offen offset:1024 lds\n"
      "s_waitcnt lgkmcnt(9)\n"
      "v_mfma_f32_16x16x32_bf16 v[192:195], a[196:199], v[84:87], v[192:195]\n"
      "ds_read_b64_tr_b16 a[218:219], v27 offset:51200\n"
      "s_waitcnt lgkmcnt(8)\n"
      "v_mfma_f32_16x16x32_bf16 v[192:195], a[200:203], a[0:3], v[192:195]\n"
      "ds_read_b64_tr_b16 a[220:221], v27 offset:53248\n"
      "v_cvt_pk_bf16_f32 v154, v212, v213\n"
      "v_cvt_pk_bf16_f32 v155, v214, v215\n"
      "buffer_load_dwordx4 v12, s[24:27], s88 offen offset:2048 lds\n"
      "s_waitcnt lgkmcnt(7)\n"
      "v_mfma_f32_16x16x32_bf16 v[192:195], a[204:207], a[12:15], v[192:195]\n"
      "ds_read_b64_tr_b16 a[222:223], v27 offset:55296\n"
      "v_mfma_f32_16x16x32_bf16 v[196:199], a[204:207], a[16:19], 0\n"
      "buffer_load_dwordx4 v13, s[24:27], s88 offen offset:3072 lds\n"
      "v_mfma_f32_16x16x32_bf16 v[196:199], a[200:203], a[4:7], v[196:199]\n"
      "v_mfma_f32_16x16x32_bf16 v[196:199], a[196:199], v[88:91], v[196:199]\n"
      "buffer_store_dwordx4 v[144:147], v32, s[36:39], s90 offen sc1\n"
      "v_mfma_f32_16x16x32_bf16 v[196:199], a[192:195], v[64:67], v[196:199]\n"
      "v_mfma_f32_16x16x32_bf16 v[200:203], a[192:195], v[68:71], 0\n"
      "buffer_store_dwordx4 v[148:151], v32, s[36:39], s90 offen offset:1024 sc1\n"
      "v_mfma_f32_16x16x32_bf16 v[200:203], a[196:199], v[92:95], v[200:203]\n"
      "v_mfma_f32_16x16x32_bf16 v[200:203], a[200:203], a[8:11], v[200:203]\n"
      "buffer_store_dwordx4 v[152:155], v32, s[36:39], s90 offen offset:2048 sc1\n"
      "v_mfma_f32_16x16x32_bf16 v[200:203], a[204:207], a[20:23], v[200:203]\n"
      "s_waitcnt vmcnt(12)\n"
      "s_barrier\n"
      "s_waitcnt lgkmcnt(6)\n"
      "v_mfma_f32_16x16x32_bf16 v[204:207], a[208:211], v[60:63], 0\n"
      "ds_read_b64_tr_b16 a[192:193], v14\n"
      "s_waitcnt lgkmcnt(5)\n"
      "v_mfma_f32_16x16x32_bf16 v[204:207], a[212:215], v[84:87], v[204:207]\n"
      "ds_read_b64_tr_b16 a[194:195], v14 offset:2048\n"
      "s_waitcnt lgkmcnt(4)\n"
      "v_mfma_f32_16x16x32_bf16 v[204:207], a[216:219], a[0:3], v[204:207]\n"
      "ds_read_b64_tr_b16 a[196:197], v14 offset:4096\n"
      "s_waitcnt lgkmcnt(3)\n"
      "v_mfma_f32_16x16x32_bf16 v[204:207], a[220:223], a[12:15], v[204:207]\n"
      "ds_read_b64_tr_b16 a[198:199], v14 offset:6144\n"
      "v_mfma_f32_16x16x32_bf16 v[208:211], a[220:223], a[16:19], 0\n"
      "ds_read_b64_tr_b16 a[200:201], v26\n"
      "v_mfma_f32_16x16x32_bf16 v[208:211], a[216:219], a[4:7], v[208:211]\n"
      "ds_read_b64_tr_b16 a[202:203], v26 offset:2048\n"
      "v_mfma_f32_16x16x32_bf16 v[208:211], a[212:215], v[88:91], v[208:211]\n"
      "ds_read_b64_tr_b16 a[204:205], v26 offset:4096\n"
      "v_mfma_f32_16x16x32_bf16 v[208:211], a[208:211], v[64:67], v[208:211]\n"
      "ds_read_b64_tr_b16 a[206:207], v26 offset:6144\n"
      "v_mfma_f32_16x16x32_bf16 v[212:215], a[208:211], v[68:71], 0\n"
      "ds_read_b64_tr_b16 a[208:209], v15\n"
      "v_mfma_f32_16x16x32_bf16 v[212:215], a[212:215], v[92:95], v[212:215]\n"
      "ds_read_b64_tr_b16 a[210:211], v15 offset:2048\n"
      "v_mfma_f32_16x16x32_bf16 v[212:215], a[216:219], a[8:11], v[212:215]\n"
      "ds_read_b64_tr_b16 a[212:213], v15 offset:4096\n"
      "v_mfma_f32_16x16x32_bf16 v[212:215], a[220:223], a[20:23], v[212:215]\n"
      "ds_read_b64_tr_b16 a[214:215], v15 offset:6144\n"
      "s_waitcnt lgkmcnt(10)\n"
      "v_mfma_f32_16x16x32_bf16 v[192:195], a[192:195], v[108:111], v[192:195]\n"
      "ds_read_b64_tr_b16 a[216:217], v27\n"
      "s_waitcnt lgkmcnt(9)\n"
      "v_mfma_f32_16x16x32_bf16 v[192:195], a[196:199], v[168:171], v[192:195]\n"
      "ds_read_b64_tr_b16 a[218:219], v27 offset:2048\n"
      "s_waitcnt lgkmcnt(8)\n"
      "v_mfma_f32_16x16x32_bf16 v[192:195], a[200:203], a[24:27], v[192:195]\n"
      "ds_read_b64_tr_b16 a[220:221], v27 offset:4096\n"
      "s_waitcnt lgkmcnt(7)\n"
      "v_mfma_f32_16x16x32_bf16 v[192:195], a[204:207], a[36:39], v[192:195]\n"
      "ds_read_b64_tr_b16 a[222:223], v27 offset:6144\n"
      "s_add_u32 s88, s77, s82\n"
      "s_add_u32 m0, s85, 0x8000\n"
      "v_mfma_f32_16x16x32_bf16 v[196:199], a[204:207], a[40:43], v[196:199]\n"
      "buffer_load_dwordx4 v10, s[24:27], s88 offen lds\n"
      "v_mfma_f32_16x16x32_bf16 v[196:199], a[200:203], a[28:31], v[196:199]\n"
      "v_mfma_f32_16x16x32_bf16 v[196:199], a[196:199], v[172:175], v[196:199]\n"
      "buffer_load_dwordx4 v11, s[24:27], s88 offen offset:1024 lds\n"
      "v_mfma_f32_16x16x32_bf16 v[196:199], a[192:195], v[112:115], v[196:199]\n"
      "v_mfma_f32_16x16x32_bf16 v[200:203], a[192:195], v[116:119], v[200:203]\n"
      "buffer_load_dwordx4 v12, s[24:27], s88 offen offset:2048 lds\n"
      "v_mfma_f32_16x16x32_bf16 v[200:203], a[196:199], v[176:179], v[200:203]\n"
      "v_mfma_f32_16x16x32_bf16 v[200:203], a[200:203], a[32:35], v[200:203]\n"
      "buffer_load_dwordx4 v13, s[24:27], s88 offen offset:3072 lds\n"
      "v_mfma_f32_16x16x32_bf16 v[200:203], a[204:207], a[44:47], v[200:203]\n"
      "s_waitcnt lgkmcnt(6)\n"
      "v_mfma_f32_16x16x32_bf16 v[204:207], a[208:211], v[108:111], v[204:207]\n"
      "ds_read_b64_tr_b16 a[192:193], v16\n"
      "s_waitcnt lgkmcnt(5)\n"
      "v_mfma_f32_16x16x32_bf16 v[204:207], a[212:215], v[168:171], v[204:207]\n"
      "ds_read_b64_tr_b16 a[194:195], v16 offset:2048\n"
      "s_waitcnt lgkmcnt(4)\n"
      "v_mfma_f32_16x16x32_bf16 v[204:207], a[216:219], a[24:27], v[204:207]\n"
      "ds_read_b64_tr_b16 a[196:197], v16 offset:4096\n"
      "s_waitcnt lgkmcnt(3)\n"
      "v_mfma_f32_16x16x32_bf16 v[204:207], a[220:223], a[36:39], v[204:207]\n"
      "ds_read_b64_tr_b16 a[198:199], v16 offset:6144\n"
      "v_mfma_f32_16x16x32_bf16 v[208:211], a[220:223], a[40:43], v[208:211]\n"
      "ds_read_b64_tr_b16 a[200:201], v28\n"
      "v_mfma_f32_16x16x32_bf16 v[208:211], a[216:219], a[28:31], v[208:211]\n"
      "ds_read_b64_tr_b16 a[202:203], v28 offset:2048\n"
      "v_mfma_f32_16x16x32_bf16 v[208:211], a[212:215], v[172:175], v[208:211]\n"
      "ds_read_b64_tr_b16 a[204:205], v28 offset:4096\n"
      "v_mfma_f32_16x16x32_bf16 v[208:211], a[208:211], v[112:115], v[208:211]\n"
      "ds_read_b64_tr_b16 a[206:207], v28 offset:6144\n"
      "v_mfma_f32_16x16x32_bf16 v[212:215], a[208:211], v[116:119], v[212:215]\n"
      "ds_read_b64_tr_b16 a[208:209], v17\n"
      "v_mfma_f32_16x16x32_bf16 v[212:215], a[212:215], v[176:179], v[212:215]\n"
      "ds_read_b64_tr_b16 a[210:211], v17 offset:2048\n"
      "v_mfma_f32_16x16x32_bf16 v[212:215], a[216:219], a[32:35], v[212:215]\n"
      "ds_read_b64_tr_b16 a[212:213], v17 offset:4096\n"
      "v_mfma_f32_16x16x32_bf16 v[212:215], a[220:223], a[44:47], v[212:215]\n"
      "ds_read_b64_tr_b16 a[214:215], v17 offset:6144\n"
      "s_waitcnt lgkmcnt(10)\n"
      "v_mfma_f32_16x16x32_bf16 v[192:195], a[192:195], v[180:183], v[192:195]\n"
      "ds_read_b64_tr_b16 a[216:217], v29\n"
      "s_waitcnt lgkmcnt(9)\n"
      "v_mfma_f32_16x16x32_bf16 v[192:195], a[196:199], v[240:243], v[192:195]\n"
      "ds_read_b64_tr_b16 a[218:219], v29 offset:2048\n"
      "s_waitcnt lgkmcnt(8)\n"
      "v_mfma_f32_16x16x32_bf16 v[192:195], a[200:203], a[48:51], v[192:195]\n"
      "ds_read_b64_tr_b16 a[220:221], v29 offset:4096\n"
      "s_waitcnt lgkmcnt(7)\n"
      "v_mfma_f32_16x16x32_bf16 v[192:195], a[204:207], a[60:63], v[192:195]\n"
      "ds_read_b64_tr_b16 a[222:223], v29 offset:6144\n"
      "v_mfma_f32_16x16x32_bf16 v[196:199], a[204:207], a[64:67], v[196:199]\n"
      "v_mfma_f32_16x16x32_bf16 v[196:199], a[200:203], a[52:55], v[196:199]\n"
      "v_mfma_f32_16x16x32_bf16 v[196:199], a[196:199], v[244:247], v[196:199]\n"
      "v_mfma_f32_16x16x32_bf16 v[196:199], a[192:195], v[184:187], v[196:199]\n"
      "v_mfma_f32_16x16x32_bf16 v[200:203], a[192:195], v[188:191], v[200:203]\n"
      "v_mfma_f32_16x16x32_bf16 v[200:203], a[196:199], v[248:251], v[200:203]\n"
      "v_mfma_f32_16x16x32_bf16 v[200:203], a[200:203], a[56:59], v[200:203]\n"
      "v_mfma_f32_16x16x32_bf16 v[200:203], a[204:207], a[68:71], v[200:203]\n"
      "s_waitcnt lgkmcnt(6)\n"
      "v_mfma_f32_16x16x32_bf16 v[204:207], a[208:211], v[180:183], v[204:207]\n"
      "ds_read_b64_tr_b16 a[192:193], v14 offset:16384\n"
      "s_waitcnt lgkmcnt(5)\n"
      "v_mfma_f32_16x16x32_bf16 v[204:207], a[212:215], v[240:243], v[204:207]\n"
      "ds_read_b64_tr_b16 a[194:195], v14 offset:18432\n"
      "s_waitcnt lgkmcnt(4)\n"
      "v_mfma_f32_16x16x32_bf16 v[204:207], a[216:219], a[48:51], v[204:207]\n"
      "ds_read_b64_tr_b16 a[196:197], v14 offset:20480\n"
      "s_waitcnt lgkmcnt(3)\n"
      "v_mfma_f32_16x16x32_bf16 v[204:207], a[220:223], a[60:63], v[204:207]\n"
      "ds_read_b64_tr_b16 a[198:199], v14 offset:22528\n"
      "v_mfma_f32_16x16x32_bf16 v[208:211], a[220:223], a[64:67], v[208:211]\n"
      "ds_read_b64_tr_b16 a[200:201], v26 offset:16384\n"
      "v_mfma_f32_16x16x32_bf16 v[208:211], a[216:219], a[52:55], v[208:211]\n"
      "ds_read_b64_tr_b16 a[202:203], v26 offset:18432\n"
      "v_mfma_f32_16x16x32_bf16 v[208:211], a[212:215], v[244:247], v[208:211]\n"
      "ds_read_b64_tr_b16 a[204:205], v26 offset:20480\n"
      "v_mfma_f32_16x16x32_bf16 v[208:211], a[208:211], v[184:187], v[208:211]\n"
      "ds_read_b64_tr_b16 a[206:207], v26 offset:22528\n"
      "v_mfma_f32_16x16x32_bf16 v[212:215], a[208:211], v[188:191], v[212:215]\n"
      "ds_read_b64_tr_b16 a[208:209], v15 offset:16384\n"
      "v_mfma_f32_16x16x32_bf16 v[212:215], a[212:215], v[248:251], v[212:215]\n"
      "ds_read_b64_tr_b16 a[210:211], v15 offset:18432\n"
      "v_mfma_f32_16x16x32_bf16 v[212:215], a[216:219], a[56:59], v[212:215]\n"
      "ds_read_b64_tr_b16 a[212:213], v15 offset:20480\n"
      "v_mfma_f32_16x16x32_bf16 v[212:215], a[220:223], a[68:71], v[212:215]\n"
      "ds_read_b64_tr_b16 a[214:215], v15 offset:22528\n"
      "s_waitcnt lgkmcnt(10)\n"
      "v_mfma_f32_16x16x32_bf16 v[192:195], a[192:195], v[252:255], v[192:195]\n"
      "ds_read_b64_tr_b16 a[216:217], v27 offset:16384\n"
      "s_waitcnt lgkmcnt(9)\n"
      "v_mfma_f32_16x16x32_bf16 v[192:195], a[196:199], v[224:227], v[192:195]\n"
      "ds_read_b64_tr_b16 a[218:219], v27 offset:18432\n"
      "s_waitcnt lgkmcnt(8)\n"
      "v_mfma_f32_16x16x32_bf16 v[192:195], a[200:203], a[72:75], v[192:195]\n"
      "ds_read_b64_tr_b16 a[220:221], v27 offset:20480\n"
      "s_waitcnt lgkmcnt(7)\n"
      "v_mfma_f32_16x16x32_bf16 v[192:195], a[204:207], a[84:87], v[192:195]\n"
      "ds_read_b64_tr_b16 a[222:223], v27 offset:22528\n"
      "v_mfma_f32_16x16x32_bf16 v[196:199], a[204:207], a[88:91], v[196:199]\n"
      "v_mfma_f32_16x16x32_bf16 v[196:199], a[200:203], a[76:79], v[196:199]\n"
      "v_mfma_f32_16x16x32_bf16 v[196:199], a[196:199], v[228:231], v[196:199]\n"
      "v_mfma_f32_16x16x32_bf16 v[196:199], a[192:195], v[216:219], v[196:199]\n"
      "v_mfma_f32_16x16x32_bf16 v[200:203], a[192:195], v[220:223], v[200:203]\n"
      "v_mfma_f32_16x16x32_bf16 v[200:203], a[196:199], v[232:235], v[200:203]\n"
      "v_mfma_f32_16x16x32_bf16 v[200:203], a[200:203], a[80:83], v[200:203]\n"
      "s_waitcnt vmcnt(4)\n"
      "buffer_store_dwordx2 v[34:35], v36, s[36:39], s48 offen sc1\n"
      "v_mfma_f32_16x16x32_bf16 v[200:203], a[204:207], a[92:95], v[200:203]\n"
      "s_add_u32 s48, s48, 0x80\n"
      "s_cmp_eq_u32 s48, 0x380\n"
      "s_cselect_b32 s48, 0, s48\n"
      "s_waitcnt lgkmcnt(6)\n"
      "v_mfma_f32_16x16x32_bf16 v[204:207], a[208:211], v[252:255], v[204:207]\n"
      "ds_read_b64_tr_b16 a[192:193], v18 offset:49152\n"
      "s_waitcnt lgkmcnt(5)\n"
      "v_mfma_f32_16x16x32_bf16 v[204:207], a[212:215], v[224:227], v[204:207]\n"
      "ds_read_b64_tr_b16 a[194:195], v18 offset:51200\n"
      "s_waitcnt lgkmcnt(4)\n"
      "v_mfma_f32_16x16x32_bf16 v[204:207], a[216:219], a[72:75], v[204:207]\n"
      "ds_read_b64_tr_b16 a[196:197], v18 offset:53248\n"
      "v_cvt_pk_bf16_f32 v156, v192, v193\n"
      "s_mul_i32 s90, s83, 0x300\n"
      "s_add_u32 s83, s83, 0x80\n"
      "s_cmp_eq_u32 s83, 0x380\n"
      "s_cselect_b32 s83, 0, s83\n"
      "s_waitcnt lgkmcnt(3)\n"
      "v_mfma_f32_16x16x32_bf16 v[204:207], a[220:223], a[84:87], v[204:207]\n"
      "ds_read_b64_tr_b16 a[198:199], v18 offset:55296\n"
      "v_cvt_pk_bf16_f32 v157, v194, v195\n"
      "v_mfma_f32_16x16x32_bf16 v[208:211], a[220:223], a[88:91], v[208:211]\n"
      "ds_read_b64_tr_b16 a[200:201], v24 offset:49152\n"
      "v_cvt_pk_bf16_f32 v160, v196, v197\n"
      "v_mfma_f32_16x16x32_bf16 v[208:211], a[216:219], a[76:79], v[208:211]\n"
      "ds_read_b64_tr_b16 a[202:203], v24 offset:51200\n"
      "v_mfma_f32_16x16x32_bf16 v[208:211], a[212:215], v[228:231], v[208:211]\n"
      "ds_read_b64_tr_b16 a[204:205], v24 offset:53248\n"
      "v_cvt_pk_bf16_f32 v161, v198, v199\n"
      "v_mfma_f32_16x16x32_bf16 v[208:211], a[208:211], v[216:219], v[208:211]\n"
      "ds_read_b64_tr_b16 a[206:207], v24 offset:55296\n"
      "v_cvt_pk_bf16_f32 v164, v200, v201\n"
      "v_mfma_f32_16x16x32_bf16 v[212:215], a[208:211], v[220:223], v[212:215]\n"
      "ds_read_b64_tr_b16 a[208:209], v19 offset:49152\n"
      "v_cvt_pk_bf16_f32 v165, v202, v203\n"
      "v_mfma_f32_16x16x32_bf16 v[212:215], a[212:215], v[232:235], v[212:215]\n"
      "ds_read_b64_tr_b16 a[210:211], v19 offset:51200\n"
      "v_mfma_f32_16x16x32_bf16 v[212:215], a[216:219], a[80:83], v[212:215]\n"
      "ds_read_b64_tr_b16 a[212:213], v19 offset:53248\n"
      "v_cvt_pk_bf16_f32 v158, v204, v205\n"
      "v_cvt_pk_bf16_f32 v159, v206, v207\n"
      "v_mfma_f32_16x16x32_bf16 v[212:215], a[220:223], a[92:95], v[212:215]\n"
      "ds_read_b64_tr_b16 a[214:215], v19 offset:55296\n"
      "s_waitcnt lgkmcnt(10)\n"
      "v_mfma_f32_16x16x32_bf16 v[192:195], a[192:195], v[60:63], 0\n"
      "ds_read_b64_tr_b16 a[216:217], v25 offset:49152\n"
      "v_cvt_pk_bf16_f32 v162, v208, v209\n"
      "v_cvt_pk_bf16_f32 v163, v210, v211\n"
      "s_waitcnt lgkmcnt(9)\n"
      "v_mfma_f32_16x16x32_bf16 v[192:195], a[196:199], v[84:87], v[192:195]\n"
      "ds_read_b64_tr_b16 a[218:219], v25 offset:51200\n"
      "s_waitcnt lgkmcnt(8)\n"
      "v_mfma_f32_16x16x32_bf16 v[192:195], a[200:203], a[0:3], v[192:195]\n"
      "ds_read_b64_tr_b16 a[220:221], v25 offset:53248\n"
      "v_cvt_pk_bf16_f32 v166, v212, v213\n"
      "v_cvt_pk_bf16_f32 v167, v214, v215\n"
      "s_waitcnt lgkmcnt(7)\n"
      "v_mfma_f32_16x16x32_bf16 v[192:195], a[204:207], a[12:15], v[192:195]\n"
      "ds_read_b64_tr_b16 a[222:223], v25 offset:55296\n"
      "v_mfma_f32_16x16x32_bf16 v[196:199], a[204:207], a[16:19], 0\n"
      "v_mfma_f32_16x16x32_bf16 v[196:199], a[200:203], a[4:7], v[196:199]\n"
      "v_mfma_f32_16x16x32_bf16 v[196:199], a[196:199], v[88:91], v[196:199]\n"
      "buffer_store_dwordx4 v[156:159], v32, s[36:39], s90 offen sc1\n"
      "v_mfma_f32_16x16x32_bf16 v[196:199], a[192:195], v[64:67], v[196:199]\n"
      "v_mfma_f32_16x16x32_bf16 v[200:203], a[192:195], v[68:71], 0\n"
      "buffer_store_dwordx4 v[160:163], v32, s[36:39], s90 offen offset:1024 sc1\n"
      "v_mfma_f32_16x16x32_bf16 v[200:203], a[196:199], v[92:95], v[200:203]\n"
      "v_mfma_f32_16x16x32_bf16 v[200:203], a[200:203], a[8:11], v[200:203]\n"
      "buffer_store_dwordx4 v[164:167], v32, s[36:39], s90 offen offset:2048 sc1\n"
      "v_mfma_f32_16x16x32_bf16 v[200:203], a[204:207], a[20:23], v[200:203]\n"
      "s_waitcnt vmcnt(4)\n"
      "s_barrier\n"
      "s_waitcnt lgkmcnt(6)\n"
      "v_mfma_f32_16x16x32_bf16 v[204:207], a[208:211], v[60:63], 0\n"
      "ds_read_b64_tr_b16 a[192:193], v16 offset:16384\n"
      "s_waitcnt lgkmcnt(5)\n"
      "v_mfma_f32_16x16x32_bf16 v[204:207], a[212:215], v[84:87], v[204:207]\n"
      "ds_read_b64_tr_b16 a[194:195], v16 offset:18432\n"
      "s_waitcnt lgkmcnt(4)\n"
      "v_mfma_f32_16x16x32_bf16 v[204:207], a[216:219], a[0:3], v[204:207]\n"
      "ds_read_b64_tr_b16 a[196:197], v16 offset:20480\n"
      "s_waitcnt lgkmcnt(3)\n"
      "v_mfma_f32_16x16x32_bf16 v[204:207], a[220:223], a[12:15], v[204:207]\n"
      "ds_read_b64_tr_b16 a[198:199], v16 offset:22528\n"
      "v_mfma_f32_16x16x32_bf16 v[208:211], a[220:223], a[16:19], 0\n"
      "ds_read_b64_tr_b16 a[200:201], v28 offset:16384\n"
      "v_mfma_f32_16x16x32_bf16 v[208:211], a[216:219], a[4:7], v[208:211]\n"
      "ds_read_b64_tr_b16 a[202:203], v28 offset:18432\n"
      "v_mfma_f32_16x16x32_bf16 v[208:211], a[212:215], v[88:91], v[208:211]\n"
      "ds_read_b64_tr_b16 a[204:205], v28 offset:20480\n"
      "v_mfma_f32_16x16x32_bf16 v[208:211], a[208:211], v[64:67], v[208:211]\n"
      "ds_read_b64_tr_b16 a[206:207], v28 offset:22528\n"
      "v_mfma_f32_16x16x32_bf16 v[212:215], a[208:211], v[68:71], 0\n"
      "ds_read_b64_tr_b16 a[208:209], v17 offset:16384\n"
      "v_mfma_f32_16x16x32_bf16 v[212:215], a[212:215], v[92:95], v[212:215]\n"
      "ds_read_b64_tr_b16 a[210:211], v17 offset:18432\n"
      "v_mfma_f32_16x16x32_bf16 v[212:215], a[216:219], a[8:11], v[212:215]\n"
      "ds_read_b64_tr_b16 a[212:213], v17 offset:20480\n"
      "v_mfma_f32_16x16x32_bf16 v[212:215], a[220:223], a[20:23], v[212:215]\n"
      "ds_read_b64_tr_b16 a[214:215], v17 offset:22528\n"
      "s_waitcnt lgkmcnt(10)\n"
      "v_mfma_f32_16x16x32_bf16 v[192:195], a[192:195], v[108:111], v[192:195]\n"
      "ds_read_b64_tr_b16 a[216:217], v29 offset:16384\n"
      "s_waitcnt lgkmcnt(9)\n"
      "v_mfma_f32_16x16x32_bf16 v[192:195], a[196:199], v[168:171], v[192:195]\n"
      "ds_read_b64_tr_b16 a[218:219], v29 offset:18432\n"
      "s_waitcnt lgkmcnt(8)\n"
      "v_mfma_f32_16x16x32_bf16 v[192:195], a[200:203], a[24:27], v[192:195]\n"
      "ds_read_b64_tr_b16 a[220:221], v29 offset:20480\n"
      "s_waitcnt lgkmcnt(7)\n"
      "v_mfma_f32_16x16x32_bf16 v[192:195], a[204:207], a[36:39], v[192:195]\n"
      "ds_read_b64_tr_b16 a[222:223], v29 offset:22528\n"
      "v_mfma_f32_16x16x32_bf16 v[196:199], a[204:207], a[40:43], v[196:199]\n"
      "v_mfma_f32_16x16x32_bf16 v[196:199], a[200:203], a[28:31], v[196:199]\n"
      "v_mfma_f32_16x16x32_bf16 v[196:199], a[196:199], v[172:175], v[196:199]\n"
      "v_mfma_f32_16x16x32_bf16 v[196:199], a[192:195], v[112:115], v[196:199]\n"
      "v_mfma_f32_16x16x32_bf16 v[200:203], a[192:195], v[116:119], v[200:203]\n"
      "v_mfma_f32_16x16x32_bf16 v[200:203], a[196:199], v[176:179], v[200:203]\n"
      "v_mfma_f32_16x16x32_bf16 v[200:203], a[200:203], a[32:35], v[200:203]\n"
      "v_mfma_f32_16x16x32_bf16 v[200:203], a[204:207], a[44:47], v[200:203]\n"
      "s_waitcnt lgkmcnt(6)\n"
      "v_mfma_f32_16x16x32_bf16 v[204:207], a[208:211], v[108:111], v[204:207]\n"
      "ds_read_b64_tr_b16 a[192:193], v14 offset:32768\n"
      "s_waitcnt lgkmcnt(5)\n"
      "v_mfma_f32_16x16x32_bf16 v[204:207], a[212:215], v[168:171], v[204:207]\n"
      "ds_read_b64_tr_b16 a[194:195], v14 offset:34816\n"
      "s_waitcnt lgkmcnt(4)\n"
      "v_mfma_f32_16x16x32_bf16 v[204:207], a[216:219], a[24:27], v[204:207]\n"
      "ds_read_b64_tr_b16 a[196:197], v14 offset:36864\n"
      "s_waitcnt lgkmcnt(3)\n"
      "v_mfma_f32_16x16x32_bf16 v[204:207], a[220:223], a[36:39], v[204:207]\n"
      "ds_read_b64_tr_b16 a[198:199], v14 offset:38912\n"
      "v_mfma_f32_16x16x32_bf16 v[208:211], a[220:223], a[40:43], v[208:211]\n"
      "ds_read_b64_tr_b16 a[200:201], v26 offset:32768\n"
      "v_mfma_f32_16x16x32_bf16 v[208:211], a[216:219], a[28:31], v[208:211]\n"
      "ds_read_b64_tr_b16 a[202:203], v26 offset:34816\n"
      "v_mfma_f32_16x16x32_bf16 v[208:211], a[212:215], v[172:175], v[208:211]\n"
      "ds_read_b64_tr_b16 a[204:205], v26 offset:36864\n"
      "v_mfma_f32_16x16x32_bf16 v[208:211], a[208:211], v[112:115], v[208:211]\n"
      "ds_read_b64_tr_b16 a[206:207], v26 offset:38912\n"
      "v_mfma_f32_16x16x32_bf16 v[212:215], a[208:211], v[116:119], v[212:215]\n"
      "ds_read_b64_tr_b16 a[208:209], v15 offset:32768\n"
      "v_mfma_f32_16x16x32_bf16 v[212:215], a[212:215], v[176:179], v[212:215]\n"
      "ds_read_b64_tr_b16 a[210:211], v15 offset:34816\n"
      "v_mfma_f32_16x16x32_bf16 v[212:215], a[216:219], a[32:35], v[212:215]\n"
      "ds_read_b64_tr_b16 a[212:213], v15 offset:36864\n"
      "v_mfma_f32_16x16x32_bf16 v[212:215], a[220:223], a[44:47], v[212:215]\n"
      "ds_read_b64_tr_b16 a[214:215], v15 offset:38912\n"
      "s_waitcnt lgkmcnt(10)\n"
      "v_mfma_f32_16x16x32_bf16 v[192:195], a[192:195], v[180:183], v[192:195]\n"
      "ds_read_b64_tr_b16 a[216:217], v27 offset:32768\n"
      "s_waitcnt lgkmcnt(9)\n"
      "v_mfma_f32_16x16x32_bf16 v[192:195], a[196:199], v[240:243], v[192:195]\n"
      "ds_read_b64_tr_b16 a[218:219], v27 offset:34816\n"
      "s_waitcnt lgkmcnt(8)\n"
      "v_mfma_f32_16x16x32_bf16 v[192:195], a[200:203], a[48:51], v[192:195]\n"
      "ds_read_b64_tr_b16 a[220:221], v27 offset:36864\n"
      "s_waitcnt lgkmcnt(7)\n"
      "v_mfma_f32_16x16x32_bf16 v[192:195], a[204:207], a[60:63], v[192:195]\n"
      "ds_read_b64_tr_b16 a[222:223], v27 offset:38912\n"
      "v_mfma_f32_16x16x32_bf16 v[196:199], a[204:207], a[64:67], v[196:199]\n"
      "v_mfma_f32_16x16x32_bf16 v[196:199], a[200:203], a[52:55], v[196:199]\n"
      "v_mfma_f32_16x16x32_bf16 v[196:199], a[196:199], v[244:247], v[196:199]\n"
      "v_mfma_f32_16x16x32_bf16 v[196:199], a[192:195], v[184:187], v[196:199]\n"
      "v_mfma_f32_16x16x32_bf16 v[200:203], a[192:195], v[188:191], v[200:203]\n"
      "v_mfma_f32_16x16x32_bf16 v[200:203], a[196:199], v[248:251], v[200:203]\n"
      "v_mfma_f32_16x16x32_bf16 v[200:203], a[200:203], a[56:59], v[200:203]\n"
      "s_waitcnt vmcnt(0)\n"
      "buffer_store_dwordx2 v[34:35], v36, s[36:39], s48 offen sc1\n"
      "v_mfma_f32_16x16x32_bf16 v[200:203], a[204:207], a[68:71], v[200:203]\n"
      "s_add_u32 s48, s48, 0x80\n"
      "s_cmp_eq_u32 s48, 0x380\n"
      "s_cselect_b32 s48, 0, s48\n"
      "s_waitcnt lgkmcnt(6)\n"
      "v_mfma_f32_16x16x32_bf16 v[204:207], a[208:211], v[180:183], v[204:207]\n"
      "ds_read_b64_tr_b16 a[192:193], v18 offset:32768\n"
      "s_waitcnt lgkmcnt(5)\n"
      "v_mfma_f32_16x16x32_bf16 v[204:207], a[212:215], v[240:243], v[204:207]\n"
      "ds_read_b64_tr_b16 a[194:195], v18 offset:34816\n"
      "s_waitcnt lgkmcnt(4)\n"
      "v_mfma_f32_16x16x32_bf16 v[204:207], a[216:219], a[48:51], v[204:207]\n"
      "ds_read_b64_tr_b16 a[196:197], v18 offset:36864\n"
      "s_waitcnt lgkmcnt(3)\n"
      "v_mfma_f32_16x16x32_bf16 v[204:207], a[220:223], a[60:63], v[204:207]\n"
      "ds_read_b64_tr_b16 a[198:199], v18 offset:38912\n"
      "v_mfma_f32_16x16x32_bf16 v[208:211], a[220:223], a[64:67], v[208:211]\n"
      "ds_read_b64_tr_b16 a[200:201], v24 offset:32768\n"
      "v_mfma_f32_16x16x32_bf16 v[208:211], a[216:219], a[52:55], v[208:211]\n"
      "ds_read_b64_tr_b16 a[202:203], v24 offset:34816\n"
      "v_mfma_f32_16x16x32_bf16 v[208:211], a[212:215], v[244:247], v[208:211]\n"
      "ds_read_b64_tr_b16 a[204:205], v24 offset:36864\n"
      "v_mfma_f32_16x16x32_bf16 v[208:211], a[208:211], v[184:187], v[208:211]\n"
      "ds_read_b64_tr_b16 a[206:207], v24 offset:38912\n"
      "v_mfma_f32_16x16x32_bf16 v[212:215], a[208:211], v[188:191], v[212:215]\n"
      "ds_read_b64_tr_b16 a[208:209], v19 offset:32768\n"
      "v_mfma_f32_16x16x32_bf16 v[212:215], a[212:215], v[248:251], v[212:215]\n"
      "ds_read_b64_tr_b16 a[210:211], v19 offset:34816\n"
      "v_mfma_f32_16x16x32_bf16 v[212:215], a[216:219], a[56:59], v[212:215]\n"
      "ds_read_b64_tr_b16 a[212:213], v19 offset:36864\n"
      "v_mfma_f32_16x16x32_bf16 v[212:215], a[220:223], a[68:71], v[212:215]\n"
      "ds_read_b64_tr_b16 a[214:215], v19 offset:38912\n"
      "s_waitcnt lgkmcnt(10)\n"
      "v_mfma_f32_16x16x32_bf16 v[192:195], a[192:195], v[252:255], v[192:195]\n"
      "ds_read_b64_tr_b16 a[216:217], v25 offset:32768\n"
      "s_waitcnt lgkmcnt(9)\n"
      "v_mfma_f32_16x16x32_bf16 v[192:195], a[196:199], v[224:227], v[192:195]\n"
      "ds_read_b64_tr_b16 a[218:219], v25 offset:34816\n"
      "s_waitcnt lgkmcnt(8)\n"
      "v_mfma_f32_16x16x32_bf16 v[192:195], a[200:203], a[72:75], v[192:195]\n"
      "ds_read_b64_tr_b16 a[220:221], v25 offset:36864\n"
      "s_waitcnt lgkmcnt(7)\n"
      "v_mfma_f32_16x16x32_bf16 v[192:195], a[204:207], a[84:87], v[192:195]\n"
      "ds_read_b64_tr_b16 a[222:223], v25 offset:38912\n"
      "v_mfma_f32_16x16x32_bf16 v[196:199], a[204:207], a[88:91], v[196:199]\n"
      "v_mfma_f32_16x16x32_bf16 v[196:199], a[200:203], a[76:79], v[196:199]\n"
      "v_mfma_f32_16x16x32_bf16 v[196:199], a[196:199], v[228:231], v[196:199]\n"
      "v_mfma_f32_16x16x32_bf16 v[196:199], a[192:195], v[216:219], v[196:199]\n"
      "v_mfma_f32_16x16x32_bf16 v[200:203], a[192:195], v[220:223], v[200:203]\n"
      "v_mfma_f32_16x16x32_bf16 v[200:203], a[196:199], v[232:235], v[200:203]\n"
      "v_mfma_f32_16x16x32_bf16 v[200:203], a[200:203], a[80:83], v[200:203]\n"
      "v_mfma_f32_16x16x32_bf16 v[200:203], a[204:207], a[92:95], v[200:203]\n"
      "s_waitcnt lgkmcnt(6)\n"
      "v_mfma_f32_16x16x32_bf16 v[204:207], a[208:211], v[252:255], v[204:207]\n"
      "v_cvt_pk_bf16_f32 v144, v192, v193\n"
      "s_mul_i32 s90, s83, 0x300\n"
      "s_waitcnt lgkmcnt(4)\n"
      "v_mfma_f32_16x16x32_bf16 v[204:207], a[212:215], v[224:227], v[204:207]\n"
      "v_cvt_pk_bf16_f32 v145, v194, v195\n"
      "s_waitcnt lgkmcnt(2)\n"
      "v_mfma_f32_16x16x32_bf16 v[204:207], a[216:219], a[72:75], v[204:207]\n"
      "v_cvt_pk_bf16_f32 v148, v196, v197\n"
      "s_waitcnt lgkmcnt(0)\n"
      "v_mfma_f32_16x16x32_bf16 v[204:207], a[220:223], a[84:87], v[204:207]\n"
      "v_cvt_pk_bf16_f32 v149, v198, v199\n"
      "v_mfma_f32_16x16x32_bf16 v[208:211], a[220:223], a[88:91], v[208:211]\n"
      "v_cvt_pk_bf16_f32 v152, v200, v201\n"
      "v_mfma_f32_16x16x32_bf16 v[208:211], a[216:219], a[76:79], v[208:211]\n"
      "v_cvt_pk_bf16_f32 v153, v202, v203\n"
      "s_nop 2\n"
      "v_cvt_pk_bf16_f32 v146, v204, v205\n"
      "v_cvt_pk_bf16_f32 v147, v206, v207\n"
      "v_mfma_f32_16x16x32_bf16 v[208:211], a[212:215], v[228:231], v[208:211]\n"
      "v_mfma_f32_16x16x32_bf16 v[208:211], a[208:211], v[216:219], v[208:211]\n"
      "buffer_store_dwordx4 v[144:147], v32, s[36:39], s90 offen sc1\n"
      "v_mfma_f32_16x16x32_bf16 v[212:215], a[208:211], v[220:223], v[212:215]\n"
      "v_mfma_f32_16x16x32_bf16 v[212:215], a[212:215], v[232:235], v[212:215]\n"
      "s_nop 4\n"
      "v_cvt_pk_bf16_f32 v150, v208, v209\n"
      "v_cvt_pk_bf16_f32 v151, v210, v211\n"
      "v_mfma_f32_16x16x32_bf16 v[212:215], a[216:219], a[80:83], v[212:215]\n"
      "v_mfma_f32_16x16x32_bf16 v[212:215], a[220:223], a[92:95], v[212:215]\n"
      "buffer_store_dwordx4 v[148:151], v32, s[36:39], s90 offen offset:1024 sc1\n"
      "s_nop 6\n"
      "v_cvt_pk_bf16_f32 v154, v212, v213\n"
      "v_cvt_pk_bf16_f32 v155, v214, v215\n"
      "buffer_store_dwordx4 v[152:155], v32, s[36:39], s90 offen offset:2048 sc1\n"
      "s_waitcnt vmcnt(0)\n"
      "buffer_store_dwordx2 v[34:35], v36, s[36:39], s48 offen sc1\n"
      "8:\n"
      "s_endpgm\n"
      "20:\n"
      "s_mov_b32 s49, 0x100000\n"
      "120:\n"
      "s_sleep 1\n"
      "buffer_load_dwordx2 v[54:55], v37, s[36:39], 0 offen sc1\n"
      "s_waitcnt vmcnt(0)\n"
      "v_cmp_eq_u64 vcc, v[54:55], v[34:35]\n"
      "s_nop 4\n"
      "s_and_b32 s92, vcc_lo, s98\n"
      "s_cmp_eq_u32 s92, s98\n"
      "s_cbranch_scc1 21b\n"
      "s_sub_u32 s49, s49, 1\n"
      "s_cmp_lg_u32 s49, 0\n"
      "s_cbranch_scc1 120b\n"
      "s_endpgm\n"
      "22:\n"
      "s_mov_b32 s49, 0x100000\n"
      "122:\n"
      "s_sleep 1\n"
      "buffer_load_dwordx2 v[54:55], v37, s[36:39], 0 offen sc1\n"
      "s_waitcnt vmcnt(0)\n"
      "v_cmp_eq_u64 vcc, v[54:55], v[34:35]\n"
      "s_nop 4\n"
      "s_and_b32 s92, vcc_lo, s98\n"
      "s_cmp_eq_u32 s92, s98\n"
      "s_cbranch_scc1 23b\n"
      "s_sub_u32 s49, s49, 1\n"
      "s_cmp_lg_u32 s49, 0\n"
      "s_cbranch_scc1 122b\n"
      "s_endpgm\n"
      "24:\n"
      "s_mov_b32 s49, 0x100000\n"
      "124:\n"
      "s_sleep 1\n"
      "buffer_load_dwordx2 v[54:55], v37, s[36:39], 0 offen sc1\n"
      "s_waitcnt vmcnt(0)\n"
      "v_cmp_eq_u64 vcc, v[54:55], v[34:35]\n"
      "s_nop 4\n"
      "s_and_b32 s92, vcc_lo, s98\n"
      "s_cmp_eq_u32 s92, s98\n"
      "s_cbranch_scc1 25b\n"
      "s_sub_u32 s49, s49, 1\n"
      "s_cmp_lg_u32 s49, 0\n"
      "s_cbranch_scc1 124b\n"
      "s_endpgm\n"
      "26:\n"
      "s_mov_b32 s49, 0x100000\n"
      "126:\n"
      "s_sleep 1\n"
      "buffer_load_dwordx2 v[54:55], v37, s[36:39], 0 offen sc1\n"
      "s_waitcnt vmcnt(0)\n"
      "v_cmp_eq_u64 vcc, v[54:55], v[34:35]\n"
      "s_nop 4\n"
      "s_and_b32 s92, vcc_lo, s98\n"
      "s_cmp_eq_u32 s92, s98\n"
      "s_cbranch_scc1 27b\n"
      "s_sub_u32 s49, s49, 1\n"
      "s_cmp_lg_u32 s49, 0\n"
      "s_cbranch_scc1 126b\n"
      "s_endpgm\n"
      "28:\n"
      "s_mov_b32 s49, 0x100000\n"
      "128:\n"
      "s_sleep 1\n"
      "buffer_load_dwordx2 v[54:55], v37, s[36:39], 0 offen sc1\n"
      "s_waitcnt vmcnt(0)\n"
      "v_cmp_eq_u64 vcc, v[54:55], v[34:35]\n"
      "s_nop 4\n"
      "s_and_b32 s92, vcc_lo, s98\n"
      "s_cmp_eq_u32 s92, s98\n"
      "s_cbranch_scc1 29b\n"
      "s_sub_u32 s49, s49, 1\n"
      "s_cmp_lg_u32 s49, 0\n"
      "s_cbranch_scc1 128b\n"
      "s_endpgm\n"
      "30:\n"
      "s_mov_b32 s49, 0x100000\n"
      "130:\n"
      "s_sleep 1\n"
      "buffer_load_dwordx2 v[54:55], v37, s[36:39], 0 offen sc1\n"
      "s_waitcnt vmcnt(0)\n"
      "v_cmp_eq_u64 vcc, v[54:55], v[34:35]\n"
      "s_nop 4\n"
      "s_and_b32 s92, vcc_lo, s93\n"
      "s_cmp_eq_u32 s92, s93\n"
      "s_cbranch_scc1 31b\n"
      "s_sub_u32 s49, s49, 1\n"
      "s_cmp_lg_u32 s49, 0\n"
      "s_cbranch_scc1 130b\n"
      "s_endpgm\n"
      "32:\n"
      "s_mov_b32 s49, 0x100000\n"
      "132:\n"
      "s_sleep 1\n"
      "buffer_load_dwordx2 v[54:55], v37, s[36:39], 0 offen sc1\n"
      "s_waitcnt vmcnt(0)\n"
      "v_cmp_eq_u64 vcc, v[54:55], v[34:35]\n"
      "s_nop 4\n"
      "s_and_b32 s92, vcc_lo, s98\n"
      "s_cmp_eq_u32 s92, s98\n"
      "s_cbranch_scc1 33b\n"
      "s_sub_u32 s49, s49, 1\n"
      "s_cmp_lg_u32 s49, 0\n"
      "s_cbranch_scc1 132b\n"
      "s_endpgm\n"
      "9:\n"
      :
      : "s"(__builtin_amdgcn_kernarg_segment_ptr()), "s"(blockIdx.x), "v"(threadIdx.x), "s"((unsigned)(size_t)smem)
      : "v1", "v2", "v3", "v4", "v5", "v6", "v7", "v8", "v9", "v10", "v11", "v12", "v13", "v14", "v15", "v16",
        "v17", "v18", "v19", "v20", "v21", "v22", "v23", "v24", "v25", "v26", "v27", "v28", "v29", "v30", "v31", "v32",
        "v33", "v34", "v35", "v36", "v37", "v38", "v39", "v40", "v41", "v42", "v43", "v44", "v45", "v46", "v47", "v48",
        "v49", "v50", "v51", "v52", "v53", "v54", "v55", "v56", "v57", "v58", "v59", "v60", "v61", "v62", "v63", "v64",
        "v65", "v66", "v67", "v68", "v69", "v70", "v71", "v72", "v73", "v74", "v75", "v76", "v77", "v78", "v79", "v80",
        "v81", "v82", "v83", "v84", "v85", "v86", "v87", "v88", "v89", "v90", "v91", "v92", "v93", "v94", "v95", "v96",
        "v97", "v98", "v99", "v100", "v101", "v102", "v103", "v104", "v105", "v106", "v107", "v108", "v109", "v110", "v111", "v112",
        "v113", "v114", "v115", "v116", "v117", "v118", "v119", "v120", "v121", "v122", "v123", "v124", "v125", "v126", "v127", "v128",
        "v129", "v130", "v131", "v132", "v133", "v134", "v135", "v136", "v137", "v138", "v139", "v140", "v141", "v142", "v143", "v144",
        "v145", "v146", "v147", "v148", "v149", "v150", "v151", "v152", "v153", "v154", "v155", "v156", "v157", "v158", "v159", "v160",
        "v161", "v162", "v163", "v164", "v165", "v166", "v167", "v168", "v169", "v170", "v171", "v172", "v173", "v174", "v175", "v176",
        "v177", "v178", "v179", "v180", "v181", "v182", "v183", "v184", "v185", "v186", "v187", "v188", "v189", "v190", "v191", "v192",
        "v193", "v194", "v195", "v196", "v197", "v198", "v199", "v200", "v201", "v202", "v203", "v204", "v205", "v206", "v207", "v208",
        "v209", "v210", "v211", "v212", "v213", "v214", "v215", "v216", "v217", "v218", "v219", "v220", "v221", "v222", "v223", "v224",
        "v225", "v226", "v227", "v228", "v229", "v230", "v231", "v232", "v233", "v234", "v235", "v236", "v237", "v238", "v239", "v240",
        "v241", "v242", "v243", "v244", "v245", "v246", "v247", "v248", "v249", "v250", "v251", "v252", "v253", "v254", "v255", "a0",
        "a1", "a2", "a3", "a4", "a5", "a6", "a7", "a8", "a9", "a10", "a11", "a12", "a13", "a14", "a15", "a16",
        "a17", "a18", "a19", "a20", "a21", "a22", "a23", "a24", "a25", "a26", "a27", "a28", "a29", "a30", "a31", "a32",
        "a33", "a34", "a35", "a36", "a37", "a38", "a39", "a40", "a41", "a42", "a43", "a44", "a45", "a46", "a47", "a48",
        "a49", "a50", "a51", "a52", "a53", "a54", "a55", "a56", "a57", "a58", "a59", "a60", "a61", "a62", "a63", "a64",
        "a65", "a66", "a67", "a68", "a69", "a70", "a71", "a72", "a73", "a74", "a75", "a76", "a77", "a78", "a79", "a80",
        "a81", "a82", "a83", "a84", "a85", "a86", "a87", "a88", "a89", "a90", "a91", "a92", "a93", "a94", "a95", "a96",
        "a97", "a98", "a99", "a100", "a101", "a102", "a103", "a104", "a105", "a106", "a107", "a108", "a109", "a110", "a111", "a112",
        "a113", "a114", "a115", "a116", "a117", "a118", "a119", "a120", "a121", "a122", "a123", "a124", "a125", "a126", "a127", "a128",
        "a129", "a130", "a131", "a132", "a133", "a134", "a135", "a136", "a137", "a138", "a139", "a140", "a141", "a142", "a143", "a144",
        "a145", "a146", "a147", "a148", "a149", "a150", "a151", "a152", "a153", "a154", "a155", "a156", "a157", "a158", "a159", "a160",
        "a161", "a162", "a163", "a164", "a165", "a166", "a167", "a168", "a169", "a170", "a171", "a172", "a173", "a174", "a175", "a176",
        "a177", "a178", "a179", "a180", "a181", "a182", "a183", "a184", "a185", "a186", "a187", "a188", "a189", "a190", "a191", "a192",
        "a193", "a194", "a195", "a196", "a197", "a198", "a199", "a200", "a201", "a202", "a203", "a204", "a205", "a206", "a207", "a208",
        "a209", "a210", "a211", "a212", "a213", "a214", "a215", "a216", "a217", "a218", "a219", "a220", "a221", "a222", "a223", "a224",
        "a225", "a226", "a227", "a228", "a229", "a230", "a231", "a232", "a233", "a234", "a235", "a236", "a237", "a238", "a239", "a240",
        "a241", "a242", "a243", "a244", "a245", "a246", "a247", "a248", "a249", "a250", "a251", "a252", "a253", "a254", "a255", "s8",
        "s9", "s10", "s11", "s12", "s13", "s14", "s15", "s16", "s17", "s18", "s19", "s20", "s21", "s22", "s23", "s24",
        "s25", "s26", "s27", "s28", "s29", "s30", "s31", "s33", "s34", "s35", "s36", "s37", "s38", "s39", "s40", "s41",
        "s42", "s43", "s44", "s45", "s46", "s47", "s48", "s49", "s50", "s51", "s52", "s53", "s54", "s55", "s56", "s57",
        "s58", "s59", "s60", "s61", "s62", "s63", "s64", "s65", "s66", "s67", "s68", "s69", "s70", "s71", "s72", "s73",
        "s74", "s75", "s76", "s77", "s78", "s79", "s80", "s81", "s82", "s83", "s84", "s85", "s86", "s87", "s88", "s89",
        "s90", "s91", "s92", "s93", "s94", "s95", "s96", "s97", "s98", "s99", "vcc", "scc", "memory");
}

extern "C" void kernel_launch(void* const* d_in, const int* in_sizes, int n_in, void* d_out, int out_size, void* d_ws, size_t ws_size,
                              hipStream_t stream) {
  fused_kernel<<<256, 256, 0, stream>>>(d_in[0], d_in[1], d_in[2], d_in[3], d_in[4], d_in[5], d_in[6], d_in[7], d_in[8], d_in[9], d_out, d_ws);
}
```
